```python
import torch

def _softmax(x, axis=-1):
    return torch.softmax(x, dim=axis)
def _gelu(x):
    return torch.nn.functional.gelu(x, approximate="tanh")
def _silu(x):
    return torch.nn.functional.silu(x)
def _sigmoid(x):
    return torch.sigmoid(x)
def _layernorm(x, axis=-1, eps=1e-5):
    m = x.mean(dim=axis, keepdim=True)
    v = x.var(dim=axis, keepdim=True, unbiased=False)
    return (x - m) / torch.sqrt(v + eps)
def _scatter_add(tgt, idx, src, axis=0):
    return tgt.clone().index_add_(axis, idx, src)
def _segment_sum(src, seg, n):
    out = torch.zeros((n,) + src.shape[1:], dtype=src.dtype)
    return out.index_add_(0, seg, src)
def _pad(x, pw):
    flat = tuple(v for pair in reversed(pw) for v in pair)
    return torch.nn.functional.pad(x, flat)

kernel_name = 'standalone__seq2seq_rnn_t_joint__speech'

def setup_inputs():

    _w = (lambda a: (lambda t: t.to(torch.bfloat16) if t.is_floating_point() else t)(torch.as_tensor(a)))
    B, T, D, K = 2, 16000, 256, 3
    U = 8
    sc = (1.0 / (K * D)) ** 0.5
    sd = (1.0 / D) ** 0.5
    return {
        "x": _w((lambda *s: torch.randn(*s, dtype=torch.bfloat16) * 0.2)(B, T, D) * 0.5),
        "w1": _w((lambda *s: torch.randn(*s, dtype=torch.bfloat16) * 3.2)(K, D, D) * sc),
        "w2": _w((lambda *s: torch.randn(*s, dtype=torch.bfloat16) * 3.2)(K, D, D) * sc),
        "g": _w((lambda *s: torch.randn(*s, dtype=torch.bfloat16) * 3.2)(B, U, D) * 0.5),
        "w_f": _w((lambda *s: torch.randn(*s, dtype=torch.bfloat16) * 3.2)(D, D) * sd),
        "w_g": _w((lambda *s: torch.randn(*s, dtype=torch.bfloat16) * 3.2)(D, D) * sd),
        "w_o": _w((lambda *s: torch.randn(*s, dtype=torch.bfloat16) * 3.2)(D, D) * sd),
    }

def reference(x, w1, w2, g, w_f, w_g, w_o):
    B, T, D = x.shape
    K = w1.shape[0]
    pad = K // 2
    xp = _pad(x, ((0,0),(pad,pad),(0,0)))
    h = torch.zeros_like(x)
    for i in range(K):
        h = h + torch.einsum("btc,cd->btd", xp[:, i:i+T, :], w1[i])
    h = _gelu(h)[:, ::2, :]
    T1 = h.shape[1]
    hp = _pad(h, ((0,0),(pad,pad),(0,0)))
    f = torch.zeros_like(h)
    for i in range(K):
        f = f + torch.einsum("btc,cd->btd", hp[:, i:i+T1, :], w2[i])
    f = _gelu(f)[:, ::2, :]
    fe = torch.einsum("btd,de->bte", f, w_f)
    ge = torch.einsum("bud,de->bue", g, w_g)
    j = torch.tanh(fe[:, :, None, :] + ge[:, None, :, :])
    return torch.einsum("btud,dv->btuv", j, w_o)
```

```cpp
#include <hip/hip_runtime.h>
#include <hip/hip_bf16.h>
#include <stdint.h>

typedef unsigned short u16;
__device__ __forceinline__ float bf2f(u16 v) { return __uint_as_float(((unsigned)v) << 16); }
__device__ __forceinline__ u16 f2bf(float f) { __hip_bfloat16 b = __float2bfloat16(f); return *(u16*)&b; }
__device__ __forceinline__ float gelu_t(float x) {
    float u = 0.7978845608028654f * (x + 0.044715f * x * x * x);
    return 0.5f * x * (1.f + tanhf(u));
}

__global__ void conv_k(const u16* in, const u16* w, u16* out, int Tin, int Tout) {
    int row = blockIdx.x;
    int b = row / Tout, t = row % Tout;
    int d = threadIdx.x;
    __shared__ float xs[768];
    for (int i = 0; i < 3; i++) {
        int ti = 2 * t + i - 1;
        xs[i * 256 + d] = (ti >= 0 && ti < Tin) ? bf2f(in[((size_t)b * Tin + ti) * 256 + d]) : 0.f;
    }
    __syncthreads();
    float acc = 0.f;
    for (int k = 0; k < 768; k++) acc += xs[k] * bf2f(w[(size_t)k * 256 + d]);
    out[(size_t)row * 256 + d] = f2bf(gelu_t(acc));
}
__global__ void lin_k(const u16* in, const u16* w, float* out) {
    int row = blockIdx.x; int d = threadIdx.x;
    __shared__ float xs[256];
    xs[d] = bf2f(in[(size_t)row * 256 + d]);
    __syncthreads();
    float acc = 0.f;
    for (int k = 0; k < 256; k++) acc += xs[k] * bf2f(w[(size_t)k * 256 + d]);
    out[(size_t)row * 256 + d] = acc;
}
__global__ void joint_k(const float* fe, const float* ge, const u16* wo, u16* out) {
    int row = blockIdx.x;
    int u = row & 7, bt = row >> 3, b = bt / 4000;
    int d = threadIdx.x;
    __shared__ float xs[256];
    xs[d] = bf2f(f2bf(tanhf(fe[(size_t)bt * 256 + d] + ge[(b * 8 + u) * 256 + d])));
    __syncthreads();
    float acc = 0.f;
    for (int k = 0; k < 256; k++) acc += xs[k] * bf2f(wo[(size_t)k * 256 + d]);
    out[(size_t)row * 256 + d] = f2bf(acc);
}

extern "C" void kernel_launch(void* const* d_in, const int* in_sizes, int n_in,
                              void* d_out, int out_size, void* d_ws, size_t ws_size,
                              hipStream_t stream) {
    const u16* x = (const u16*)d_in[0];
    const u16* w1 = (const u16*)d_in[1];
    const u16* w2 = (const u16*)d_in[2];
    const u16* g = (const u16*)d_in[3];
    const u16* wf = (const u16*)d_in[4];
    const u16* wg = (const u16*)d_in[5];
    const u16* wo = (const u16*)d_in[6];
    char* ws = (char*)d_ws;
    u16* h = (u16*)ws; ws += (size_t)16000 * 256 * 2;
    u16* f = (u16*)ws; ws += (size_t)8000 * 256 * 2;
    float* fe = (float*)ws; ws += (size_t)8000 * 256 * 4;
    float* ge = (float*)ws;
    conv_k<<<16000, 256, 0, stream>>>(x, w1, h, 16000, 8000);
    conv_k<<<8000, 256, 0, stream>>>(h, w2, f, 8000, 4000);
    lin_k<<<8000, 256, 0, stream>>>(f, wf, fe);
    lin_k<<<16, 256, 0, stream>>>(g, wg, ge);
    joint_k<<<64000, 256, 0, stream>>>(fe, ge, wo, (u16*)d_out);
}
```

```cpp
#include <hip/hip_runtime.h>
#include <hip/hip_bf16.h>
#include <stdint.h>
#include <type_traits>

typedef unsigned int u32;
typedef u32 u32x4 __attribute__((ext_vector_type(4)));
typedef float f32x4 __attribute__((ext_vector_type(4)));
typedef short bf16x8 __attribute__((ext_vector_type(8)));
typedef __bf16 bf16x2_t __attribute__((ext_vector_type(2)));
typedef float f32x2 __attribute__((ext_vector_type(2)));
#define LAS __attribute__((address_space(3)))

#define NRING 5
#define ST_AUX 16
#define X_AUX 2
#define SBMASK 0

constexpr int PITCH = 528;
constexpr int XE_OFF = 0;
constexpr int XO_OFF = 68 * PITCH;
constexpr int EX_OFF = 72192;
constexpr int EX_WAVE = 17408;
constexpr int HE_OFF = 0;
constexpr int HO_OFF = 33 * PITCH;
constexpr int F_OFF = 34816;
constexpr int GP_OFF = 147456;
constexpr int GF_OFF = 141824;
constexpr int SH_OFF = 0;
constexpr int J0_OFF = 131072;
constexpr int J1_OFF = 0;
constexpr int EB_OFF = 147456;
constexpr int LDS_BYTES = 163840;

template <int I> using IC = std::integral_constant<int, I>;
template <int B, int E, class F> __device__ __forceinline__ void sfor(F&& f) {
    if constexpr (B < E) { f(IC<B>{}); sfor<B + 1, E>(f); }
}

__device__ __forceinline__ f32x4 mfma16(u32x4 a, u32x4 b, f32x4 c) {
    return __builtin_amdgcn_mfma_f32_16x16x32_bf16(__builtin_bit_cast(bf16x8, a), __builtin_bit_cast(bf16x8, b), c, 0, 0, 0);
}
__device__ __forceinline__ u32 pk2(float lo, float hi) {
    f32x2 v = {lo, hi};
    bf16x2_t b = __builtin_convertvector(v, bf16x2_t);
    return __builtin_bit_cast(u32, b);
}
__device__ __forceinline__ float gelu_f(float x) {
    const float c0 = -2.0f * 0.7978845608028654f * 1.4426950408889634f;
    const float c1 = c0 * 0.044715f;
    float t = __builtin_fmaf(x * x, c1, c0);
    float e = __builtin_amdgcn_exp2f(x * t);
    return x * __builtin_amdgcn_rcpf(1.0f + e);
}
__device__ __forceinline__ u32x4 lds_rd(LAS char* L, int off) { return *(LAS u32x4*)(L + off); }
__device__ __forceinline__ f32x4 lds_rdf(LAS char* L, int off) { return *(LAS f32x4*)(L + off); }
__device__ __forceinline__ void lds_wr(LAS char* L, int off, u32x4 v) { *(LAS u32x4*)(L + off) = v; }
__device__ __forceinline__ void lds_wrf(LAS char* L, int off, f32x4 v) { *(LAS f32x4*)(L + off) = v; }
__device__ __forceinline__ void wg_barrier() {
    asm volatile("s_waitcnt lgkmcnt(0)" ::: "memory");
    __builtin_amdgcn_s_barrier();
    asm volatile("" ::: "memory");
}
#define SB() __builtin_amdgcn_sched_barrier(SBMASK)

constexpr int NSTEP = 36;
constexpr int step_mat(int t) { return t < 12 ? 0 : t < 16 ? 1 : t < 28 ? 2 : t < 32 ? 3 : 4; }
constexpr int step_loc(int t) { return t < 12 ? t : t < 16 ? t - 12 : t < 28 ? t - 16 : t < 32 ? t - 28 : t - 32; }
constexpr int cv_tap(int l) { return (l % 6) / 2; }
constexpr int cv_i(int l) { return 2 * (l / 6) + (l & 1); }
constexpr int step_soff(int t) {
    int m = step_mat(t), l = step_loc(t);
    return (m == 0 || m == 2) ? (cv_tap(l) * 256 + 32 * cv_i(l)) * 512 : (32 * l) * 512;
}

#define STAMP(i)

__global__ void __launch_bounds__(256, 1)
rnnt_fused(const void* __restrict__ px, const void* __restrict__ pw1, const void* __restrict__ pw2, const void* __restrict__ pg,
           const void* __restrict__ pwf, const void* __restrict__ pwg, const void* __restrict__ pwo, void* __restrict__ pout
           ) {
    extern __shared__ __attribute__((aligned(16))) char smem[];
    LAS char* L = (LAS char*)smem;
    const int tid = threadIdx.x, lane = tid & 63;
    const int wv = __builtin_amdgcn_readfirstlane(tid >> 6);
    const int nh = wv & 1, kh = wv >> 1;
    const int c = lane & 15, q = lane >> 4;
    const int bid = blockIdx.x;
    const int b = bid / 125, tile = bid - b * 125, t0 = tile * 32;
    STAMP(0);

    const __amdgpu_buffer_rsrc_t rx = __builtin_amdgcn_make_buffer_rsrc((char*)px + (size_t)b * 16000 * 512, 0, 16000 * 512, 0x00020000);
    const __amdgpu_buffer_rsrc_t rw1 = __builtin_amdgcn_make_buffer_rsrc((void*)pw1, 0, 768 * 512, 0x00020000);
    const __amdgpu_buffer_rsrc_t rw2 = __builtin_amdgcn_make_buffer_rsrc((void*)pw2, 0, 768 * 512, 0x00020000);
    const __amdgpu_buffer_rsrc_t rwf = __builtin_amdgcn_make_buffer_rsrc((void*)pwf, 0, 256 * 512, 0x00020000);
    const __amdgpu_buffer_rsrc_t rwg = __builtin_amdgcn_make_buffer_rsrc((void*)pwg, 0, 256 * 512, 0x00020000);
    const __amdgpu_buffer_rsrc_t rwo = __builtin_amdgcn_make_buffer_rsrc((void*)pwo, 0, 256 * 512, 0x00020000);
    const __amdgpu_buffer_rsrc_t rg = __builtin_amdgcn_make_buffer_rsrc((char*)pg + (size_t)b * 8 * 512, 0, 8 * 512, 0x00020000);
    const __amdgpu_buffer_rsrc_t ro = __builtin_amdgcn_make_buffer_rsrc((char*)pout + ((size_t)b * 4000 + t0) * 4096, 0, 32 * 4096, 0x00020000);

    u32x4 xr[17];
    {
        const int p0 = tid >> 5, ch = tid & 31;
        sfor<0, 17>([&](auto I) __attribute__((always_inline)) {
            constexpr int it = I;
            int p = p0 + 8 * it;
            int t = 4 * t0 - 3 + p;
            int off = t * 512 + ch * 16;
            if (it == 0) off = (t < 0) ? 0x7ffffff0 : off;
            if (it == 16) off = (p > 130) ? 0x7ffffff0 : off;
            xr[it] = __builtin_amdgcn_raw_buffer_load_b128(rx, off, 0, X_AUX);
        });
    }
    __builtin_amdgcn_sched_barrier(0);
    const int wvoff = (8 * (q >> 1) + 128 * (q & 1) + 16 * kh) * 512 + 256 * nh + 16 * c;
    u32x4 raw[NRING][8];
    auto issue = [&](auto T) __attribute__((always_inline)) {
        constexpr int t = T;
        if constexpr (t < NSTEP) {
            constexpr int m = step_mat(t);
            const __amdgpu_buffer_rsrc_t r = m == 0 ? rw1 : m == 1 ? rwg : m == 2 ? rw2 : m == 3 ? rwf : rwo;
            sfor<0, 8>([&](auto I) __attribute__((always_inline)) {
                constexpr int i = I;
                raw[t % NRING][i] = __builtin_amdgcn_raw_buffer_load_b128(r, wvoff + i * 512, step_soff(t), 0);
            });
        }
    };
    auto xpose = [&](auto T, u32x4(&bf)[8]) __attribute__((always_inline)) {
        constexpr int t = T;
        sfor<0, 4>([&](auto D) __attribute__((always_inline)) {
            constexpr int d = D;
            sfor<0, 4>([&](auto P) __attribute__((always_inline)) {
                constexpr int p = P;
                u32 lo = raw[t % NRING][2 * p][d], hi = raw[t % NRING][2 * p + 1][d];
                bf[2 * d][p] = __builtin_amdgcn_perm(hi, lo, 0x05040100u);
                bf[2 * d + 1][p] = __builtin_amdgcn_perm(hi, lo, 0x07060302u);
            });
        });
    };
    sfor<0, NRING>([&](auto T) __attribute__((always_inline)) { issue(T); });
    __builtin_amdgcn_sched_barrier(0);
    {
        const int p0 = tid >> 5, ch = tid & 31;
        const int xw = ((p0 & 1) ? XO_OFF : XE_OFF) + (p0 >> 1) * PITCH + ch * 16;
        sfor<0, 17>([&](auto I) __attribute__((always_inline)) { constexpr int it = I; lds_wr(L, xw + it * 4 * PITCH, xr[it]); });
    }
    u32x4 ga[4];
    {
        const int goff = (c & 7) * 512 + 16 * (q >> 1) + 256 * (q & 1) + 32 * kh;
        sfor<0, 4>([&](auto I) __attribute__((always_inline)) { constexpr int i = I; ga[i] = __builtin_amdgcn_raw_buffer_load_b128(rg, goff + 64 * i, 0, 0); });
    }
    wg_barrier();
    STAMP(1);

    const int abase0 = 16 * (q >> 1) + 256 * (q & 1) + 32 * kh;
    const int abase = abase0 + c * PITCH;
    const int exw = EX_OFF + wv * EX_WAVE + lane * 16;
    const int exr = EX_OFF + (wv ^ 2) * EX_WAVE + lane * 16;

    {
        const int akeep = abase + kh * 32 * PITCH, asend = abase + (1 - kh) * 32 * PITCH;
        f32x4 acc[5][8];
        sfor<0, 5>([&](auto M) __attribute__((always_inline)) { sfor<0, 8>([&](auto J) __attribute__((always_inline)) { acc[M][J] = f32x4{0.f, 0.f, 0.f, 0.f}; }); });
        u32x4 a[2][5];
        auto lda = [&](auto T) __attribute__((always_inline)) {
            constexpr int t = T;
            constexpr int tap = cv_tap(t), i = cv_i(t);
            constexpr int arr = (tap == 1) ? XO_OFF : XE_OFF;
            constexpr int sh = (tap == 2) ? 1 : 0;
            sfor<0, 2>([&](auto M) __attribute__((always_inline)) {
                constexpr int m = M;
                a[t & 1][2 + m] = lds_rd(L, asend + arr + (16 * m + 1 + sh) * PITCH + 64 * i);
                a[t & 1][m] = lds_rd(L, akeep + arr + (16 * m + 1 + sh) * PITCH + 64 * i);
            });
            a[t & 1][4] = lds_rd(L, abase0 + arr + sh * PITCH + 64 * i);
        };
        lda(IC<0>{});
        sfor<0, 12>([&](auto T) __attribute__((always_inline)) {
            constexpr int t = T;
            if constexpr (t < 11) lda(IC<t + 1>{});
            u32x4 bf[8];
            xpose(T, bf);
            issue(IC<t + NRING>{});
            sfor<0, 8>([&](auto J) __attribute__((always_inline)) { sfor<0, 5>([&](auto M) __attribute__((always_inline)) { acc[M][J] = mfma16(a[t & 1][M], bf[J], acc[M][J]); }); });
            SB();
        });
        STAMP(2);
        sfor<0, 2>([&](auto M) __attribute__((always_inline)) { sfor<0, 8>([&](auto J) __attribute__((always_inline)) { lds_wrf(L, exw + (M * 8 + J) * 1024, acc[2 + M][J]); }); });
        lds_wrf(L, EX_OFF + wv * EX_WAVE + 16384 + c * 32, f32x4{acc[4][0][0], acc[4][1][0], acc[4][2][0], acc[4][3][0]});
        lds_wrf(L, EX_OFF + wv * EX_WAVE + 16384 + c * 32 + 16, f32x4{acc[4][4][0], acc[4][5][0], acc[4][6][0], acc[4][7][0]});
        {
            f32x4 gacc[8];
            sfor<0, 8>([&](auto J) __attribute__((always_inline)) { gacc[J] = f32x4{0.f, 0.f, 0.f, 0.f}; });
            sfor<12, 16>([&](auto T) __attribute__((always_inline)) {
                constexpr int t = T;
                u32x4 bf[8];
                xpose(T, bf);
                issue(IC<t + NRING>{});
                sfor<0, 8>([&](auto J) __attribute__((always_inline)) { gacc[J] = mfma16(ga[t - 12], bf[J], gacc[J]); });
                SB();
            });
            const int o = GP_OFF + kh * 8192 + (4 * (q & 1)) * 1024 + nh * 512 + c * 32;
            sfor<0, 4>([&](auto R) __attribute__((always_inline)) {
                constexpr int r = R;
                lds_wrf(L, o + r * 1024, f32x4{gacc[0][r], gacc[1][r], gacc[2][r], gacc[3][r]});
                lds_wrf(L, o + r * 1024 + 16, f32x4{gacc[4][r], gacc[5][r], gacc[6][r], gacc[7][r]});
            });
        }
        wg_barrier();
        STAMP(3);
        sfor<0, 2>([&](auto M) __attribute__((always_inline)) { sfor<0, 8>([&](auto J) __attribute__((always_inline)) { acc[M][J] += lds_rdf(L, exr + (M * 8 + J) * 1024); }); });
        const int hw = (2 * q + 16 * kh) * PITCH + 256 * nh + 16 * c;
        sfor<0, 2>([&](auto M) __attribute__((always_inline)) {
            constexpr int m = M;
            sfor<0, 4>([&](auto R) __attribute__((always_inline)) {
                constexpr int r = R;
                u32x4 v;
                sfor<0, 4>([&](auto D) __attribute__((always_inline)) { v[(int)D] = pk2(gelu_f(acc[m][2 * D][r]), gelu_f(acc[m][2 * D + 1][r])); });
                constexpr int o = ((r & 1) ? HE_OFF + ((r + 1) / 2) * PITCH : HO_OFF + (r / 2) * PITCH) + 8 * m * PITCH;
                lds_wr(L, hw + o, v);
            });
        });
        {
            f32x2 p0 = *(LAS f32x2*)(L + EX_OFF + wv * EX_WAVE + 16384 + c * 32 + q * 8);
            f32x2 p1 = *(LAS f32x2*)(L + EX_OFF + (wv ^ 2) * EX_WAVE + 16384 + c * 32 + q * 8);
            u32 v = pk2(gelu_f(p0[0] + p1[0]), gelu_f(p0[1] + p1[1]));
            v = (tile == 0) ? 0u : v;
            *(LAS u32*)(L + HE_OFF + 256 * nh + 16 * c + 4 * q) = v;
        }
        {
            const float C2 = 2.0f * 1.4426950408889634f;
            const int o = GP_OFF + tid * 32;
            f32x4 s0 = (lds_rdf(L, o) + lds_rdf(L, o + 8192)) * C2;
            f32x4 s1 = (lds_rdf(L, o + 16) + lds_rdf(L, o + 8192 + 16)) * C2;
            f32x4 e0, e1;
            bool ok = true;
            sfor<0, 4>([&](auto D) __attribute__((always_inline)) {
                constexpr int d = D;
                e0[d] = __builtin_amdgcn_exp2f(s0[d]); e1[d] = __builtin_amdgcn_exp2f(s1[d]);
                ok = ok & (__builtin_fabsf(s0[d]) <= 120.f) & (__builtin_fabsf(s1[d]) <= 120.f);
            });
            lds_wrf(L, o, s0); lds_wrf(L, o + 16, s1);
            lds_wrf(L, o + 8192, e0); lds_wrf(L, o + 8192 + 16, e1);
            const bool allok = __builtin_amdgcn_ballot_w64(!ok) == 0ull;
            *(LAS u32*)(L + GF_OFF + wv * 4) = allok ? 1u : 0u;
        }
        wg_barrier();
        STAMP(4);
    }
    bool gfast;
    {
        u32x4 gf = lds_rd(L, GF_OFF);
        gfast = __builtin_amdgcn_readfirstlane(gf[0] & gf[1] & gf[2] & gf[3]) != 0u;
    }
    const int akeep = abase + kh * 16 * PITCH, asend = abase + (1 - kh) * 16 * PITCH;
    {
        f32x4 acc[2][8];
        sfor<0, 2>([&](auto M) __attribute__((always_inline)) { sfor<0, 8>([&](auto J) __attribute__((always_inline)) { acc[M][J] = f32x4{0.f, 0.f, 0.f, 0.f}; }); });
        u32x4 a[2][2];
        auto lda = [&](auto T) __attribute__((always_inline)) {
            constexpr int l = T;
            constexpr int tap = cv_tap(l), i = cv_i(l);
            constexpr int arr = (tap == 1) ? HO_OFF : HE_OFF;
            constexpr int sh = (tap == 2) ? 1 : 0;
            a[l & 1][1] = lds_rd(L, asend + arr + sh * PITCH + 64 * i);
            a[l & 1][0] = lds_rd(L, akeep + arr + sh * PITCH + 64 * i);
        };
        lda(IC<0>{});
        sfor<16, 28>([&](auto T) __attribute__((always_inline)) {
            constexpr int t = T, l = t - 16;
            if constexpr (l < 11) lda(IC<l + 1>{});
            u32x4 bf[8];
            xpose(T, bf);
            issue(IC<t + NRING>{});
            sfor<0, 8>([&](auto J) __attribute__((always_inline)) { sfor<0, 2>([&](auto M) __attribute__((always_inline)) { acc[M][J] = mfma16(a[l & 1][M], bf[J], acc[M][J]); }); });
            SB();
        });
        STAMP(5);
        sfor<0, 8>([&](auto J) __attribute__((always_inline)) { lds_wrf(L, exw + J * 1024, acc[1][J]); });
        wg_barrier();
        sfor<0, 8>([&](auto J) __attribute__((always_inline)) { acc[0][J] += lds_rdf(L, exr + J * 1024); });
        const int fw = F_OFF + (4 * q + 16 * kh) * PITCH + 256 * nh + 16 * c;
        sfor<0, 4>([&](auto R) __attribute__((always_inline)) {
            constexpr int r = R;
            u32x4 v;
            sfor<0, 4>([&](auto D) __attribute__((always_inline)) { v[(int)D] = pk2(gelu_f(acc[0][2 * D][r]), gelu_f(acc[0][2 * D + 1][r])); });
            lds_wr(L, fw + r * PITCH, v);
        });
        wg_barrier();
        STAMP(6);
    }
    float ea[4][8];
    bool fast;
    {
        f32x4 acc[2][8];
        sfor<0, 2>([&](auto M) __attribute__((always_inline)) { sfor<0, 8>([&](auto J) __attribute__((always_inline)) { acc[M][J] = f32x4{0.f, 0.f, 0.f, 0.f}; }); });
        u32x4 a[4][2];
        sfor<0, 4>([&](auto I) __attribute__((always_inline)) {
            a[I][1] = lds_rd(L, asend + F_OFF + 64 * I);
            a[I][0] = lds_rd(L, akeep + F_OFF + 64 * I);
        });
        sfor<28, 32>([&](auto T) __attribute__((always_inline)) {
            constexpr int t = T, i = t - 28;
            u32x4 bf[8];
            xpose(T, bf);
            issue(IC<t + NRING>{});
            sfor<0, 8>([&](auto J) __attribute__((always_inline)) { sfor<0, 2>([&](auto M) __attribute__((always_inline)) { acc[M][J] = mfma16(a[i][M], bf[J], acc[M][J]); }); });
            SB();
        });
        sfor<0, 8>([&](auto J) __attribute__((always_inline)) { lds_wrf(L, exw + J * 1024, acc[1][J]); });
        wg_barrier();
        STAMP(7);
        const float C2 = 2.0f * 1.4426950408889634f;
        bool ok = true;
        sfor<0, 8>([&](auto J) __attribute__((always_inline)) {
            f32x4 s = (acc[0][J] + lds_rdf(L, exr + J * 1024)) * C2;
            sfor<0, 4>([&](auto R) __attribute__((always_inline)) { ea[R][J] = s[(int)R]; ok = ok & (__builtin_fabsf(s[(int)R]) <= 120.f); });
        });
        fast = (__builtin_amdgcn_ballot_w64(!ok) == 0ull) && gfast;
        wg_barrier();
        STAMP(8);
    }

    auto outphase = [&](auto FAST) __attribute__((always_inline)) {
        constexpr bool fastp = FAST;
        if constexpr (fastp) {
            sfor<0, 4>([&](auto R) __attribute__((always_inline)) { sfor<0, 8>([&](auto J) __attribute__((always_inline)) { ea[R][J] = __builtin_amdgcn_exp2f(ea[R][J]); }); });
        }
        float eb[4][8];
        const int eo = EB_OFF + (fastp ? 8192 : 0) + nh * 512 + c * 32;
        auto ldeb = [&](auto V) __attribute__((always_inline)) {
            sfor<0, 4>([&](auto U) __attribute__((always_inline)) {
                f32x4 v0 = lds_rdf(L, eo + (4 * V + U) * 1024), v1 = lds_rdf(L, eo + (4 * V + U) * 1024 + 16);
                sfor<0, 4>([&](auto D) __attribute__((always_inline)) { eb[U][D] = v0[(int)D]; eb[U][4 + D] = v1[(int)D]; });
            });
        };
        const int jw = kh * 8192 + q * 512 + 256 * nh;
        auto produce = [&](auto RHO, u32x4(&w)[4]) __attribute__((always_inline)) {
            constexpr int rho = RHO, r = rho & 3;
            if constexpr (r == 0) ldeb(IC<(rho >> 2)>{});
            sfor<0, 4>([&](auto UU) __attribute__((always_inline)) {
                constexpr int uu = UU;
                sfor<0, 4>([&](auto D) __attribute__((always_inline)) {
                    constexpr int d = D;
                    float tv[2];
                    sfor<0, 2>([&](auto H) __attribute__((always_inline)) {
                        constexpr int j = 2 * d + H;
                        float den;
                        if constexpr (fastp) den = __builtin_fmaf(ea[r][j], eb[uu][j], 1.0f);
                        else den = __builtin_amdgcn_exp2f(ea[r][j] + eb[uu][j]) + 1.0f;
                        tv[(int)H] = __builtin_fmaf(-2.0f, __builtin_amdgcn_rcpf(den), 1.0f);
                    });
                    w[uu][d] = pk2(tv[0], tv[1]);
                });
            });
        };
        auto jstore = [&](auto RHO, u32x4(&w)[4]) __attribute__((always_inline)) {
            constexpr int rho = RHO;
            sfor<0, 4>([&](auto UU) __attribute__((always_inline)) {
                constexpr int uu = UU;
                lds_wr(L, jw + ((rho & 1) ? J1_OFF : J0_OFF) + uu * 2048 + ((c ^ (4 * uu + q)) * 16), w[uu]);
            });
        };
        {
            u32x4 w[4];
            produce(IC<0>{}, w);
            jstore(IC<0>{}, w);
        }
        sfor<32, 36>([&](auto T) __attribute__((always_inline)) {
            constexpr int t = T;
            u32x4 bf[8];
            xpose(T, bf);
            sfor<0, 8>([&](auto J) __attribute__((always_inline)) { lds_wr(L, SH_OFF + nh * 65536 + kh * 8192 + ((t - 32) * 16 + J) * 1024 + lane * 16, bf[J]); });
        });
        wg_barrier();
        STAMP(9);
        u32x4 wo[8][8];
        const int jr = kh * 8192 + c * 512 + 256 * (q & 1);
        const int jx = (q >> 1) ^ c;
        const int ow = (16 * kh) * 4096 + q * 512 + 256 * nh + 16 * c;
        sfor<0, 8>([&](auto RHO) __attribute__((always_inline)) {
            constexpr int rho = RHO, r = rho & 3, v = rho >> 2;
            f32x4 acc[8];
            sfor<0, 8>([&](auto J) __attribute__((always_inline)) { acc[J] = f32x4{0.f, 0.f, 0.f, 0.f}; });
            u32x4 a[8];
            sfor<0, 8>([&](auto S) __attribute__((always_inline)) { a[S] = lds_rd(L, jr + ((rho & 1) ? J1_OFF : J0_OFF) + ((jx ^ (2 * S)) * 16)); });
            if constexpr (rho == 0) {
                sfor<0, 8>([&](auto S) __attribute__((always_inline)) { sfor<0, 8>([&](auto J) __attribute__((always_inline)) { wo[S][J] = lds_rd(L, SH_OFF + nh * 65536 + (S * 8 + J) * 1024 + lane * 16); }); });
            }
            u32x4 w[4];
            if constexpr (rho < 7) produce(IC<rho + 1>{}, w);
            if constexpr (rho > 0 && rho < 7) jstore(IC<rho + 1>{}, w);
            sfor<0, 8>([&](auto S) __attribute__((always_inline)) { sfor<0, 8>([&](auto J) __attribute__((always_inline)) { acc[J] = mfma16(a[S], wo[S][J], acc[J]); }); });
            if constexpr (rho == 0) { wg_barrier(); jstore(IC<1>{}, w); }
            sfor<0, 4>([&](auto R) __attribute__((always_inline)) {
                constexpr int rr = R;
                u32x4 o;
                sfor<0, 4>([&](auto D) __attribute__((always_inline)) { o[(int)D] = pk2(acc[2 * D][rr], acc[2 * D + 1][rr]); });
                __builtin_amdgcn_raw_buffer_store_b128(o, ro, ow + v * 2048, (4 * rr + r) * 4096, ST_AUX);
            });
            if constexpr (rho == 0) STAMP(10);
            if constexpr (rho < 7) wg_barrier();
        });
    };
    if (!fast) { outphase(std::false_type{}); __builtin_amdgcn_endpgm(); }
    outphase(std::true_type{});
    STAMP(11);
}

extern "C" void kernel_launch(void* const* d_in, const int* in_sizes, int n_in,
                              void* d_out, int out_size, void* d_ws, size_t ws_size,
                              hipStream_t stream) {
    (void)in_sizes; (void)n_in; (void)out_size; (void)d_ws; (void)ws_size;
    (void)hipFuncSetAttribute((const void*)rnnt_fused, hipFuncAttributeMaxDynamicSharedMemorySize, LDS_BYTES);
    rnnt_fused<<<250, 256, LDS_BYTES, stream>>>(d_in[0], d_in[1], d_in[2], d_in[3], d_in[4], d_in[5], d_in[6], d_out);
}
```

```cpp
#include <hip/hip_runtime.h>
#include <hip/hip_bf16.h>
#include <stdint.h>
typedef unsigned int u32;
typedef u32 u32x4 __attribute__((ext_vector_type(4)));
constexpr int LDS_BYTES = 163840;
typedef float f32x4 __attribute__((ext_vector_type(4)));
typedef short bf16x8 __attribute__((ext_vector_type(8)));
#define GAS __attribute__((address_space(1)))
#define LAS __attribute__((address_space(3)))
#define RLX_AGENT __ATOMIC_RELAXED, __HIP_MEMORY_SCOPE_AGENT
constexpr int WS_GE = 131072, WS_BYTES = 1 << 20;
constexpr int NPROD = 3, NMAIL = 252, NUNIT = 82;

__device__ __forceinline__ void producer(int p, int tid, const void* pg, const void* pwg, void* pws, unsigned long long* dbg) {
#define PST(k)
    PST(0)
    extern __shared__ __attribute__((aligned(16))) char smem_[];
    LAS char* smem = (LAS char*)smem_;
    const int lane = tid & 63, wv = __builtin_amdgcn_readfirstlane(tid >> 6);
    GAS char* box = (GAS char*)pws;
    GAS u32* pn = (GAS u32*)box;
    GAS unsigned long long* req = (GAS unsigned long long*)(box + 128);
    GAS u32* ack = (GAS u32*)(box + 8192);
    GAS unsigned long long* ud = (GAS unsigned long long*)(box + 12288);
    const int c = lane & 15, q = lane >> 4, cq = 2 * (q & 1) + (q >> 1), nh = p - 1;
    const __amdgpu_buffer_rsrc_t rws = __builtin_amdgcn_make_buffer_rsrc(pws, 0, WS_BYTES, 0x00020000);
    const __amdgpu_buffer_rsrc_t rwg = __builtin_amdgcn_make_buffer_rsrc((void*)pwg, 0, 256 * 512, 0x00020000);
    const __amdgpu_buffer_rsrc_t rg = __builtin_amdgcn_make_buffer_rsrc((void*)pg, 0, 16 * 512, 0x00020000);
    unsigned long long pc;
    asm volatile("s_getpc_b64 %0" : "=s"(pc));
    const __amdgpu_buffer_rsrc_t rc = __builtin_amdgcn_make_buffer_rsrc((void*)(pc & ~63ull), 0, 1792, 0x00020000);
    const u32 tcode = __builtin_amdgcn_raw_buffer_load_b32(rc, tid * 64, 0, 0);
    __builtin_amdgcn_sched_barrier(0);
    const u32 n0 = __hip_atomic_load(ack + 249 + p, RLX_AGENT);
    u32x4 raw[2][8], ga[2];
#pragma unroll
    for (int k = 0; k < 2; k++) {
        ga[k] = __builtin_amdgcn_raw_buffer_load_b128(rg, c * 512 + cq * 16, (2 * wv + k) * 64, 0);
#pragma unroll
        for (int i = 0; i < 8; i++) raw[k][i] = __builtin_amdgcn_raw_buffer_load_b128(rwg, (8 * cq + i) * 512 + 16 * c, (2 * wv + k) * 16384 + nh * 256, 0);
    }
    __builtin_amdgcn_sched_barrier(0);
    LAS f32x4* red = (LAS f32x4*)smem;
#pragma unroll
    for (int j = 0; j < 8; j++) {
        f32x4 acc = {0.f, 0.f, 0.f, 0.f};
#pragma unroll
        for (int k = 0; k < 2; k++) {
            u32x4 bf;
#pragma unroll
            for (int t = 0; t < 4; t++) bf[t] = __builtin_amdgcn_perm(raw[k][2 * t + 1][j >> 1], raw[k][2 * t][j >> 1], (j & 1) ? 0x07060302u : 0x05040100u);
            acc = __builtin_amdgcn_mfma_f32_16x16x32_bf16(__builtin_bit_cast(bf16x8, ga[k]), __builtin_bit_cast(bf16x8, bf), acc, 0, 0, 0);
        }
        red[(wv * 8 + j) * 64 + lane] = acc;
    }
    __syncthreads();
    bool bad = false;
#pragma unroll
    for (int jj = 0; jj < 2; jj++) {
        const int j = 2 * wv + jj;
        const f32x4 s = ((red[j * 64 + lane] + red[(8 + j) * 64 + lane]) + (red[(16 + j) * 64 + lane] + red[(24 + j) * 64 + lane])) * (2.0f * 1.4426950408889634f);
        f32x4 ex;
#pragma unroll
        for (int r = 0; r < 4; r++) { ex[r] = __builtin_amdgcn_exp2f(s[r]); bad |= !(__builtin_fabsf(s[r]) <= 120.f); }
        const int off = (q >> 1) * 16384 + nh * 4096 + j * 512 + (q & 1) * 256 + c * 16;
        __builtin_amdgcn_raw_buffer_store_b128(__builtin_bit_cast(u32x4, s), rws, off, WS_GE, 16);
        __builtin_amdgcn_raw_buffer_store_b128(__builtin_bit_cast(u32x4, ex), rws, off + 8192, WS_GE, 16);
    }
    u32 pnv = __hip_atomic_load(pn, RLX_AGENT);
    __builtin_amdgcn_raw_buffer_store_b32(__builtin_amdgcn_ballot_w64(bad) != 0ull ? 1u : 0u, rws, 32768 + (nh * 4 + wv) * 4, WS_GE, 16);
    asm volatile("s_waitcnt vmcnt(0)" :: "v"(tcode) : "memory");
    __syncthreads();
    PST(2)
    const u32 n = n0 + 1u;
    if (tid == 0) {
        for (int spin = 0; spin < (1 << 16); spin++) {
            const unsigned long long m = ((unsigned long long)pnv << 32) | n;
            __hip_atomic_store(ud + 79 + p, m, RLX_AGENT);
            __hip_atomic_store(req + 249 + p, m, RLX_AGENT);
            __builtin_amdgcn_s_sleep(8);
            pnv = __hip_atomic_load(pn, RLX_AGENT);
            if (__hip_atomic_load(ack + 249 + p, RLX_AGENT) == n) break;
        }
    }
    PST(4)
}

__global__ void __launch_bounds__(256, 1)
rnnt_fused(const void* px, const void* pw1, const void* pw2, const void* pg, const void* pwf, const void* pwg, const void* pwo, void* pout, void* pws) {
    int bid = blockIdx.x, tid = threadIdx.x;
    typedef void* const __attribute__((address_space(4)))* kargs_t;
    kargs_t kp = (kargs_t)__builtin_amdgcn_kernarg_segment_ptr();
    asm volatile(
"s_cmp_lt_u32 %1, 3\n"
"s_cbranch_scc1 .Lhelper0\n"
"s_mov_b32 s39, 0\n"
"s_getpc_b64 s[44:45]\n"
"s_load_dwordx16 s[4:19], %0, 0x0\n"
"s_load_dwordx2 s[28:29], %0, 0x40\n"
"v_mov_b32 v0, %2\n"
"v_lshrrev_b32 v8, 6, v0\n"
"v_and_b32 v2, 15, v0\n"
"v_readfirstlane_b32 s54, v8\n"
"v_bfe_u32 v3, v0, 4, 2\n"
"s_sub_u32 s57, %1, 3\n"
"v_lshrrev_b32 v11, 1, v3\n"
"v_xor_b32 v8, v2, v11\n"
"s_lshl_b32 s59, s54, 1\n"
"s_and_b32 s55, s54, 1\n"
"s_lshr_b32 s56, s54, 1\n"
"v_xor_b32 v8, s59, v8\n"
"v_lshlrev_b32 v8, 4, v8\n"
"v_lshl_add_u32 v8, v3, 9, v8\n"
"s_lshl_b32 s59, s54, 11\n"
"v_add_u32 v8, s59, v8\n"
"v_xor_b32 v9, 0x80, v8\n"
"s_cmp_ge_u32 s57, 0x7d\n"
"s_cselect_b32 s62, 0x7d, 0\n"
"s_cselect_b32 s38, 0x4000, 0\n"
"s_sub_u32 s94, s57, s62\n"
"s_cmp_eq_u32 s94, 0\n"
"s_cselect_b32 s99, 0x600, 0\n"
"v_mov_b32 v12, 0x7ffffff0\n"
"v_cmp_gt_u32 vcc, s99, v8\n"
"s_nop 1\n"
"v_cndmask_b32 v10, v8, v12, vcc\n"
"s_lshl_b32 s91, s54, 10\n"
"s_lshl_b32 s58, s57, 16\n"
"s_nop 0\n"
"s_mov_b32 s22, 0x10600\n"
"s_mov_b32 s23, 0x20000\n"
"s_and_b32 s45, s45, 0xffff\n"
"s_and_b32 s44, s44, 0xffffff80\n"
"s_mov_b32 s46, 59996\n"
"s_mov_b32 s47, 0x20000\n"
"s_lshr_b32 s63, %1, 3\n"
"s_lshl_b32 s63, s63, 2\n"
"s_add_u32 s63, s63, s54\n"
"v_and_b32 v14, 63, v0\n"
"v_lshlrev_b32_e64 v15, 6, v14\n"
"s_mul_i32 s62, s63, 0xc00\n"
"v_add_u32 v240, s62, v15\n"
"v_cmp_lt_u32 vcc, 47, v14\n"
"s_nop 1\n"
"v_cndmask_b32 v240, v240, v12, vcc\n"
"s_lshl_b32 s62, s63, 9\n"
"v_add_u32_e64 v241, s62, v15\n"
"v_cmp_lt_u32 vcc, 7, v14\n"
"s_nop 1\n"
"v_cndmask_b32 v241, v241, v12, vcc\n"
"buffer_load_dword a250, v241, s[44:47], 0 offen\n"
"v_lshlrev_b32 v4, 14, v14\n"
"v_cmp_lt_u32 vcc, 0, v0\n"
"s_nop 1\n"
"v_cndmask_b32 v4, v4, v12, vcc\n"
"v_and_b32 v13, 1, v3\n"
"v_lshlrev_b32 v13, 1, v13\n"
"v_or_b32 v13, v13, v11\n"
"v_lshlrev_b32 v1, 12, v13\n"
"v_lshl_add_u32 v1, v2, 4, v1\n"
"s_lshl_b32 s59, s56, 14\n"
"s_lshl_b32 s99, s55, 8\n"
"s_add_u32 s59, s59, s99\n"
"v_add_u32_e64 v1, s59, v1\n"
"s_lshr_b32 s59, %1, 4\n"
"s_lshl_b32 s59, s59, 3\n"
"s_and_b32 s62, %1, 7\n"
"s_add_u32 s59, s59, s62\n"
"s_sub_u32 s59, s59, 3\n"
"s_and_b32 s62, %1, 8\n"
"s_or_b32 s62, s62, s54\n"
"s_cmp_ge_u32 s59, 0x50\n"
"s_cselect_b32 s99, 1, 0\n"
"s_or_b32 s62, s62, s99\n"
"s_lshl_b32 s93, s59, 13\n"
"s_add_u32 s93, s93, 0x40000\n"
"s_lshl_b32 s98, s59, 3\n"
"s_add_u32 s98, s98, 0x3000\n"
"s_lshr_b32 s92, s59, 1\n"
"s_and_b32 s59, s59, 1\n"
"s_lshl_b32 s59, s59, 8\n"
"s_mov_b32 s35, 0x20000\n"
"s_mov_b32 s43, 0x20000\n"
"s_waitcnt lgkmcnt(0)\n"
"s_mov_b64 s[32:33], s[8:9]\n"
"s_mov_b32 s34, 0x60000\n"
"s_mov_b32 s99, 0\n"
"s_cmp_ge_u32 s92, 24\n"
"s_cselect_b64 s[32:33], s[12:13], s[32:33]\n"
"s_cselect_b32 s34, 0x20000, s34\n"
"s_cselect_b32 s99, 24, s99\n"
"s_cmp_ge_u32 s92, 32\n"
"s_cselect_b64 s[32:33], s[16:17], s[32:33]\n"
"s_cselect_b32 s99, 32, s99\n"
"s_sub_u32 s92, s92, s99\n"
"s_lshl_b32 s92, s92, 14\n"
"s_add_u32 s92, s92, s59\n"
"s_and_b32 s33, s33, 0xffff\n"
"s_cmp_eq_u32 s62, 0\n"
"s_cselect_b32 s34, s34, 0\n"
"s_nop 0\n"
"s_cselect_b32 s42, 0x200000, 0\n"
"buffer_load_dwordx4 v[208:211], v1, s[32:35], s92 offen\n"
"buffer_load_dwordx4 v[212:215], v1, s[32:35], s92 offen offset:512\n"
"buffer_load_dwordx4 v[216:219], v1, s[32:35], s92 offen offset:1024\n"
"buffer_load_dwordx4 v[220:223], v1, s[32:35], s92 offen offset:1536\n"
"buffer_load_dwordx4 v[224:227], v1, s[32:35], s92 offen offset:2048\n"
"buffer_load_dwordx4 v[228:231], v1, s[32:35], s92 offen offset:2560\n"
"buffer_load_dwordx4 v[232:235], v1, s[32:35], s92 offen offset:3072\n"
"buffer_load_dwordx4 v[236:239], v1, s[32:35], s92 offen offset:3584\n"
"s_sub_u32 s20, s4, 0x600\n"
"s_subb_u32 s21, s5, 0\n"
"s_add_u32 s20, s20, s58\n"
"s_addc_u32 s21, s21, 0\n"
"s_and_b32 s21, s21, 0xffff\n"
"s_mov_b32 s24, s6\n"
"s_and_b32 s25, s7, 0xffff\n"
"s_mov_b32 s26, 0x60000\n"
"s_mov_b32 s27, 0x20000\n"
"buffer_load_dword a251, v240, s[24:27], 0 offen\n"
"s_bitcmp1_b32 %1, 3\n"
"s_cbranch_scc1 .LheadB\n"
"s_add_u32 m0, s91, 0\n"
"s_mov_b32 s90, 0\n"
"buffer_load_dwordx4 v10, s[20:23], s90 offen nt lds\n"
"s_add_u32 m0, s91, 0x1000\n"
"s_mov_b32 s90, 0x2000\n"
"buffer_load_dwordx4 v9, s[20:23], s90 offen nt lds\n"
"s_add_u32 m0, s91, 0x2000\n"
"s_mov_b32 s90, 0x4000\n"
"buffer_load_dwordx4 v8, s[20:23], s90 offen nt lds\n"
"s_add_u32 m0, s91, 0x3000\n"
"s_mov_b32 s90, 0x6000\n"
"buffer_load_dwordx4 v9, s[20:23], s90 offen nt lds\n"
"s_add_u32 m0, s91, 0x4000\n"
"s_mov_b32 s90, 0x8000\n"
"buffer_load_dwordx4 v8, s[20:23], s90 offen nt lds\n"
"s_add_u32 m0, s91, 0x5000\n"
"s_mov_b32 s90, 0xa000\n"
"buffer_load_dwordx4 v9, s[20:23], s90 offen nt lds\n"
"s_add_u32 m0, s91, 0x6000\n"
"s_mov_b32 s90, 0xc000\n"
"buffer_load_dwordx4 v8, s[20:23], s90 offen nt lds\n"
"s_add_u32 m0, s91, 0x7000\n"
"s_mov_b32 s90, 0xe000\n"
"buffer_load_dwordx4 v9, s[20:23], s90 offen nt lds\n"
"s_add_u32 m0, s91, 0x8000\n"
"s_mov_b32 s90, 0x10000\n"
"buffer_load_dwordx4 v8, s[20:23], s90 offen nt lds\n"
"v_lshlrev_b32_e64 v15, 7, v14\n"
"s_mul_i32 s62, s63, 0x1400\n"
"s_add_u32 s62, s62, 0x40000\n"
"v_add_u32 v241, s62, v15\n"
"v_cmp_lt_u32 vcc, 39, v14\n"
"s_nop 1\n"
"v_cndmask_b32 v241, v241, v12, vcc\n"
"v_mov_b32_e64 v12, v13\n"
"s_and_b32 s29, s29, 0xffff\n"
"s_mov_b32 s30, 0x200000\n"
"s_mov_b32 s31, 0x20000\n"
"s_mov_b32 s58, s57\n"
"s_lshl_b32 s36, s58, 3\n"
"s_add_u32 s36, s36, 0x80\n"
"s_lshl_b32 s37, s58, 2\n"
"s_add_u32 s37, s37, 0x2000\n"
"buffer_load_dword v242, v4, s[28:31], s37 offen sc1\n"
"s_lshl_b32 s58, s57, 17\n"
"s_add_u32 s48, s18, s58\n"
"s_addc_u32 s49, s19, 0\n"
"s_and_b32 s49, s49, 0xffff\n"
"s_mov_b32 s50, 0x20000\n"
"s_mov_b32 s51, 0x20000\n"
"s_add_u32 s38, s38, 0x22000\n"
"s_cmp_eq_u32 s54, 0\n"
"s_cselect_b32 s4, 1, 0\n"
"s_cselect_b32 s5, 2, 0\n"
"s_lshl_b32 s58, s55, 1\n"
"s_add_u32 s58, s58, s56\n"
"s_lshl_b32 s95, s58, 12\n"
"v_lshl_add_u32 v7, v14, 4, s95\n"
"s_lshl_b32 s58, s54, 14\n"
"s_add_u32 s99, s58, 0x12000\n"
"v_lshl_add_u32 v6, v14, 4, s99\n"
"s_lshl_b32 s58, s56, 2\n"
"v_add_u32_e64 v14, s58, v12\n"
"s_mov_b32 s52, 0x5040100\n"
"s_mov_b32 s53, 0x7060302\n"
"s_mov_b32 s60, 0xbdd2d3e8\n"
"s_mov_b32 s61, 0xc0135761\n"
"v_lshlrev_b32 v15, 1, v2\n"
"v_subrev_u32 v13, 7, v15\n"
"v_cmp_lt_u32 vcc, 3, v2\n"
"v_subrev_u32 v11, 16, v15\n"
"s_nop 0\n"
"v_cndmask_b32 v15, v15, v13, vcc\n"
"v_cmp_lt_u32 vcc, 11, v2\n"
"s_nop 1\n"
"v_cndmask_b32 v15, v15, v11, vcc\n"
"s_lshl_b32 s58, s56, 14\n"
"s_xor_b32 s99, s58, 0x4000\n"
"v_add_u32 v11, 1, v15\n"
"v_and_b32 v11, 15, v11\n"
"v_xor_b32 v11, v11, v14\n"
"v_lshlrev_b32 v11, 4, v11\n"
"v_lshl_add_u32 v11, v15, 9, v11\n"
"v_add_u32 v11, 0x200, v11\n"
"v_add_u32 v244, s58, v11\n"
"v_add_u32 v248, s99, v11\n"
"v_xor_b32 v245, 0x80, v244\n"
"v_xor_b32 v249, 0x80, v248\n"
"v_add_u32 v11, 2, v15\n"
"v_and_b32 v11, 15, v11\n"
"v_xor_b32 v11, v11, v14\n"
"v_lshlrev_b32 v11, 4, v11\n"
"v_lshl_add_u32 v11, v15, 9, v11\n"
"v_add_u32 v11, 0x200, v11\n"
"v_add_u32 v246, s58, v11\n"
"v_add_u32 v250, s99, v11\n"
"v_xor_b32 v247, 0x80, v246\n"
"v_xor_b32 v251, 0x80, v250\n"
"v_mul_u32_u24 v13, 0x210, v15\n"
"v_lshl_add_u32 v13, v12, 4, v13\n"
"v_lshrrev_b32 v14, 1, v3\n"
"v_xor_b32 v15, v3, v14\n"
"v_and_b32_e64 v15, 1, v15\n"
"v_lshl_add_u32 v15, v14, 3, v15\n"
"s_lshl_b32 s58, s55, 8\n"
"v_lshl_add_u32 v14, v2, 4, s58\n"
"s_mov_b32 s40, s28\n"
"s_mov_b32 s41, s29\n"
"s_add_u32 s92, s93, 0x1000\n"
"s_lshl_b32 s6, s57, 10\n"
"s_add_u32 s6, s6, 0x100000\n"
"s_add_u32 s7, s6, 0x400\n"
"v_and_b32 v252, 63, v0\n"
"v_lshlrev_b32 v252, 4, v252\n"
"v_mov_b32 v44, 0\n"
"v_mov_b32 v45, 0\n"
"v_mov_b32 v46, 0\n"
"v_mov_b32 v47, 0\n"
"v_lshlrev_b32_e64 v253, 4, v0\n"
"v_mov_b32 v254, 0x7ffffff0\n"
"v_cmp_lt_u32 vcc, 63, v0\n"
"s_nop 1\n"
"v_cndmask_b32 v253, v253, v254, vcc\n"
"buffer_store_dwordx4 v[44:47], v253, s[28:31], s6 offen sc1\n"
"s_waitcnt vmcnt(12)\n"
"v_perm_b32 v16, v212, v208, s52\n"
"v_perm_b32 v17, v220, v216, s52\n"
"v_perm_b32 v18, v228, v224, s52\n"
"v_perm_b32 v19, v236, v232, s52\n"
"buffer_store_dwordx4 v[16:19], v7, s[40:43], s93 offen sc1\n"
"v_perm_b32 v20, v212, v208, s53\n"
"v_perm_b32 v21, v220, v216, s53\n"
"v_perm_b32 v22, v228, v224, s53\n"
"v_perm_b32 v23, v236, v232, s53\n"
"buffer_store_dwordx4 v[20:23], v7, s[40:43], s93 offen offset:1024 sc1\n"
"v_perm_b32 v24, v213, v209, s52\n"
"v_perm_b32 v25, v221, v217, s52\n"
"v_perm_b32 v26, v229, v225, s52\n"
"v_perm_b32 v27, v237, v233, s52\n"
"buffer_store_dwordx4 v[24:27], v7, s[40:43], s93 offen offset:2048 sc1\n"
"v_perm_b32 v28, v213, v209, s53\n"
"v_perm_b32 v29, v221, v217, s53\n"
"v_perm_b32 v30, v229, v225, s53\n"
"v_perm_b32 v31, v237, v233, s53\n"
"buffer_store_dwordx4 v[28:31], v7, s[40:43], s93 offen offset:3072 sc1\n"
"v_perm_b32 v32, v214, v210, s52\n"
"v_perm_b32 v33, v222, v218, s52\n"
"v_perm_b32 v34, v230, v226, s52\n"
"v_perm_b32 v35, v238, v234, s52\n"
"buffer_store_dwordx4 v[32:35], v7, s[40:43], s92 offen sc1\n"
"v_perm_b32 v36, v214, v210, s53\n"
"v_perm_b32 v37, v222, v218, s53\n"
"v_perm_b32 v38, v230, v226, s53\n"
"v_perm_b32 v39, v238, v234, s53\n"
"buffer_store_dwordx4 v[36:39], v7, s[40:43], s92 offen offset:1024 sc1\n"
"v_perm_b32 v40, v215, v211, s52\n"
"v_perm_b32 v41, v223, v219, s52\n"
"v_perm_b32 v42, v231, v227, s52\n"
"v_perm_b32 v43, v239, v235, s52\n"
"buffer_store_dwordx4 v[40:43], v7, s[40:43], s92 offen offset:2048 sc1\n"
"v_perm_b32 v44, v215, v211, s53\n"
"v_perm_b32 v45, v223, v219, s53\n"
"v_perm_b32 v46, v231, v227, s53\n"
"v_perm_b32 v47, v239, v235, s53\n"
"buffer_store_dwordx4 v[44:47], v7, s[40:43], s92 offen offset:3072 sc1\n"
"s_add_u32 m0, s91, 0x9000\n"
"s_mov_b32 s90, 0x100\n"
"buffer_load_dwordx4 v10, s[20:23], s90 offen nt lds\n"
"s_add_u32 m0, s91, 0xa000\n"
"s_mov_b32 s90, 0x2100\n"
"buffer_load_dwordx4 v9, s[20:23], s90 offen nt lds\n"
"s_add_u32 m0, s91, 0xb000\n"
"s_mov_b32 s90, 0x4100\n"
"buffer_load_dwordx4 v8, s[20:23], s90 offen nt lds\n"
"buffer_load_dword v243, v4, s[28:31], 0 offen sc1\n"
"s_add_u32 m0, s91, 0xc000\n"
"s_mov_b32 s90, 0x6100\n"
"buffer_load_dwordx4 v9, s[20:23], s90 offen nt lds\n"
"s_add_u32 m0, s91, 0xd000\n"
"s_mov_b32 s90, 0x8100\n"
"buffer_load_dwordx4 v8, s[20:23], s90 offen nt lds\n"
"s_add_u32 m0, s91, 0xe000\n"
"s_mov_b32 s90, 0xa100\n"
"buffer_load_dwordx4 v9, s[20:23], s90 offen nt lds\n"
"s_add_u32 m0, s91, 0xf000\n"
"s_mov_b32 s90, 0xc100\n"
"buffer_load_dwordx4 v8, s[20:23], s90 offen nt lds\n"
"s_add_u32 m0, s91, 0x10000\n"
"s_mov_b32 s90, 0xe100\n"
"buffer_load_dwordx4 v9, s[20:23], s90 offen nt lds\n"
"s_add_u32 m0, s91, 0x11000\n"
"s_mov_b32 s90, 0x10100\n"
"buffer_load_dwordx4 v8, s[20:23], s90 offen nt lds\n"
"s_mov_b32 s96, 0\n"
"buffer_load_dwordx4 v[48:51], v1, s[24:27], s96 offen\n"
"s_waitcnt vmcnt(20)\n"
"v_add_u32_e64 v242, 1, v242\n"
"buffer_load_dwordx4 v[52:55], v1, s[24:27], s96 offen offset:512\n"
"buffer_load_dwordx4 v[56:59], v1, s[24:27], s96 offen offset:1024\n"
"buffer_load_dwordx4 v[60:63], v1, s[24:27], s96 offen offset:1536\n"
"buffer_load_dwordx4 v[64:67], v1, s[24:27], s96 offen offset:2048\n"
"s_waitcnt vmcnt(11)\n"
"s_cmp_lg_u32 s54, 0\n"
"s_cbranch_scc1 .Lspin_nd\n"
"s_waitcnt vmcnt(0)\n"
".Lspin_nd:\n"
"buffer_load_dwordx4 v[68:71], v1, s[24:27], s96 offen offset:2560\n"
"buffer_store_dwordx2 v[242:243], v4, s[40:43], s98 offen sc1\n"
"buffer_store_dwordx2 v[242:243], v4, s[28:31], s36 offen sc1\n"
"s_barrier\n"
"s_nop 0\n"
"ds_read_b128 a[168:171], v248\n"
"ds_read_b128 a[172:175], v248 offset:8192\n"
"ds_read_b128 a[160:163], v244\n"
"ds_read_b128 a[164:167], v244 offset:8192\n"
"buffer_load_dwordx4 v[72:75], v1, s[24:27], s96 offen offset:3072\n"
"buffer_load_dwordx4 v[76:79], v1, s[24:27], s96 offen offset:3584\n"
"buffer_load_dword v5, v4, s[28:31], 0 offen sc1\n"
"s_mov_b32 s97, 0x8000\n"
"buffer_load_dwordx4 v[80:83], v1, s[24:27], s97 offen\n"
"buffer_load_dwordx4 v[84:87], v1, s[24:27], s97 offen offset:512\n"
"buffer_load_dwordx4 v[88:91], v1, s[24:27], s97 offen offset:1024\n"
"buffer_load_dwordx4 v[92:95], v1, s[24:27], s97 offen offset:1536\n"
"s_waitcnt vmcnt(5)\n"
"s_nop 0\n"
"v_perm_b32 v16, v52, v48, s52\n"
"buffer_load_dwordx4 v[96:99], v1, s[24:27], s97 offen offset:2048\n"
"buffer_load_dwordx4 v[100:103], v1, s[24:27], s97 offen offset:2560\n"
"v_perm_b32 v17, v60, v56, s52\n"
"buffer_load_dwordx4 v[104:107], v1, s[24:27], s97 offen offset:3072\n"
"buffer_load_dwordx4 v[108:111], v1, s[24:27], s97 offen offset:3584\n"
"v_perm_b32 v18, v68, v64, s52\n"
"s_mov_b32 s96, 0x20000\n"
"buffer_load_dwordx4 v[112:115], v1, s[24:27], s96 offen\n"
"buffer_load_dwordx4 v[116:119], v1, s[24:27], s96 offen offset:512\n"
"v_perm_b32 v19, v76, v72, s52\n"
"buffer_load_dwordx4 v[120:123], v1, s[24:27], s96 offen offset:1024\n"
"v_perm_b32 v20, v52, v48, s53\n"
"v_perm_b32 v21, v60, v56, s53\n"
"v_perm_b32 v22, v68, v64, s53\n"
"v_perm_b32 v23, v76, v72, s53\n"
"v_perm_b32 v24, v53, v49, s52\n"
"v_perm_b32 v25, v61, v57, s52\n"
"v_perm_b32 v26, v69, v65, s52\n"
"v_perm_b32 v27, v77, v73, s52\n"
"v_perm_b32 v28, v53, v49, s53\n"
"v_perm_b32 v29, v61, v57, s53\n"
"v_perm_b32 v30, v69, v65, s53\n"
"v_perm_b32 v31, v77, v73, s53\n"
"v_perm_b32 v32, v54, v50, s52\n"
"v_perm_b32 v33, v62, v58, s52\n"
"v_perm_b32 v34, v70, v66, s52\n"
"v_perm_b32 v35, v78, v74, s52\n"
"v_perm_b32 v36, v54, v50, s53\n"
"v_perm_b32 v37, v62, v58, s53\n"
"v_perm_b32 v38, v70, v66, s53\n"
"v_perm_b32 v39, v78, v74, s53\n"
"v_perm_b32 v40, v55, v51, s52\n"
"v_perm_b32 v41, v63, v59, s52\n"
"v_perm_b32 v42, v71, v67, s52\n"
"v_perm_b32 v43, v79, v75, s52\n"
"s_waitcnt lgkmcnt(0)\n"
"s_nop 0\n"
"v_mfma_f32_16x16x32_bf16 a[64:67], a[168:171], v[16:19], 0\n"
"v_perm_b32 v44, v55, v51, s53\n"
"v_mfma_f32_16x16x32_bf16 a[96:99], a[172:175], v[16:19], 0\n"
"v_perm_b32 v45, v63, v59, s53\n"
"v_mfma_f32_16x16x32_bf16 a[0:3], a[160:163], v[16:19], 0\n"
"v_perm_b32 v46, v71, v67, s53\n"
"v_mfma_f32_16x16x32_bf16 a[32:35], a[164:167], v[16:19], 0\n"
"v_perm_b32 v47, v79, v75, s53\n"
"v_mfma_f32_16x16x32_bf16 a[68:71], a[168:171], v[20:23], 0\n"
"ds_read_b128 a[188:191], v249\n"
"v_mfma_f32_16x16x32_bf16 a[100:103], a[172:175], v[20:23], 0\n"
"v_mfma_f32_16x16x32_bf16 a[4:7], a[160:163], v[20:23], 0\n"
"ds_read_b128 a[192:195], v249 offset:8192\n"
"v_mfma_f32_16x16x32_bf16 a[36:39], a[164:167], v[20:23], 0\n"
"v_mfma_f32_16x16x32_bf16 a[72:75], a[168:171], v[24:27], 0\n"
"ds_read_b128 a[180:183], v245\n"
"v_mfma_f32_16x16x32_bf16 a[104:107], a[172:175], v[24:27], 0\n"
"v_mfma_f32_16x16x32_bf16 a[8:11], a[160:163], v[24:27], 0\n"
"ds_read_b128 a[184:187], v245 offset:8192\n"
"v_mfma_f32_16x16x32_bf16 a[40:43], a[164:167], v[24:27], 0\n"
"v_mfma_f32_16x16x32_bf16 a[76:79], a[168:171], v[28:31], 0\n"
"v_mfma_f32_16x16x32_bf16 a[108:111], a[172:175], v[28:31], 0\n"
"v_mfma_f32_16x16x32_bf16 a[12:15], a[160:163], v[28:31], 0\n"
"v_mfma_f32_16x16x32_bf16 a[44:47], a[164:167], v[28:31], 0\n"
"buffer_load_dwordx4 v[124:127], v1, s[24:27], s96 offen offset:1536\n"
"v_mfma_f32_16x16x32_bf16 a[80:83], a[168:171], v[32:35], 0\n"
"v_mfma_f32_16x16x32_bf16 a[112:115], a[172:175], v[32:35], 0\n"
"v_mfma_f32_16x16x32_bf16 a[16:19], a[160:163], v[32:35], 0\n"
"v_mfma_f32_16x16x32_bf16 a[48:51], a[164:167], v[32:35], 0\n"
"buffer_load_dwordx4 v[128:131], v1, s[24:27], s96 offen offset:2048\n"
"v_mfma_f32_16x16x32_bf16 a[84:87], a[168:171], v[36:39], 0\n"
"v_mfma_f32_16x16x32_bf16 a[116:119], a[172:175], v[36:39], 0\n"
"v_mfma_f32_16x16x32_bf16 a[20:23], a[160:163], v[36:39], 0\n"
"v_mfma_f32_16x16x32_bf16 a[52:55], a[164:167], v[36:39], 0\n"
"v_mfma_f32_16x16x32_bf16 a[88:91], a[168:171], v[40:43], 0\n"
"buffer_load_dwordx4 v[132:135], v1, s[24:27], s96 offen offset:2560\n"
"v_mfma_f32_16x16x32_bf16 a[120:123], a[172:175], v[40:43], 0\n"
"v_mfma_f32_16x16x32_bf16 a[24:27], a[160:163], v[40:43], 0\n"
"v_mfma_f32_16x16x32_bf16 a[56:59], a[164:167], v[40:43], 0\n"
"v_mfma_f32_16x16x32_bf16 a[92:95], a[168:171], v[44:47], 0\n"
"buffer_load_dwordx4 v[136:139], v1, s[24:27], s96 offen offset:3072\n"
"v_mfma_f32_16x16x32_bf16 a[124:127], a[172:175], v[44:47], 0\n"
"v_mfma_f32_16x16x32_bf16 a[28:31], a[160:163], v[44:47], 0\n"
"v_mfma_f32_16x16x32_bf16 a[60:63], a[164:167], v[44:47], 0\n"
"s_waitcnt vmcnt(7)\n"
"s_nop 0\n"
"v_perm_b32 v16, v84, v80, s52\n"
"v_perm_b32 v17, v92, v88, s52\n"
"buffer_load_dwordx4 v[140:143], v1, s[24:27], s96 offen offset:3584\n"
"s_mov_b32 s97, 0x28000\n"
"buffer_load_dwordx4 v[144:147], v1, s[24:27], s97 offen\n"
"v_perm_b32 v18, v100, v96, s52\n"
"buffer_load_dwordx4 v[148:151], v1, s[24:27], s97 offen offset:512\n"
"buffer_load_dwordx4 v[152:155], v1, s[24:27], s97 offen offset:1024\n"
"v_perm_b32 v19, v108, v104, s52\n"
"buffer_load_dwordx4 v[156:159], v1, s[24:27], s97 offen offset:1536\n"
"v_perm_b32 v20, v84, v80, s53\n"
"v_perm_b32 v21, v92, v88, s53\n"
"v_perm_b32 v22, v100, v96, s53\n"
"v_perm_b32 v23, v108, v104, s53\n"
"v_perm_b32 v24, v85, v81, s52\n"
"v_perm_b32 v25, v93, v89, s52\n"
"v_perm_b32 v26, v101, v97, s52\n"
"v_perm_b32 v27, v109, v105, s52\n"
"v_perm_b32 v28, v85, v81, s53\n"
"v_perm_b32 v29, v93, v89, s53\n"
"v_perm_b32 v30, v101, v97, s53\n"
"v_perm_b32 v31, v109, v105, s53\n"
"v_perm_b32 v32, v86, v82, s52\n"
"v_perm_b32 v33, v94, v90, s52\n"
"buffer_load_dwordx4 v[160:163], v1, s[24:27], s97 offen offset:2048\n"
"v_perm_b32 v34, v102, v98, s52\n"
"v_perm_b32 v35, v110, v106, s52\n"
"v_perm_b32 v36, v86, v82, s53\n"
"v_perm_b32 v37, v94, v90, s53\n"
"v_perm_b32 v38, v102, v98, s53\n"
"v_perm_b32 v39, v110, v106, s53\n"
"v_perm_b32 v40, v87, v83, s52\n"
"v_perm_b32 v41, v95, v91, s52\n"
"v_perm_b32 v42, v103, v99, s52\n"
"v_perm_b32 v43, v111, v107, s52\n"
"s_waitcnt lgkmcnt(0)\n"
"s_nop 0\n"
"v_mfma_f32_16x16x32_bf16 a[64:67], a[188:191], v[16:19], a[64:67]\n"
"v_perm_b32 v44, v87, v83, s53\n"
"v_perm_b32 v45, v95, v91, s53\n"
"v_mfma_f32_16x16x32_bf16 a[96:99], a[192:195], v[16:19], a[96:99]\n"
"buffer_load_dwordx4 v[164:167], v1, s[24:27], s97 offen offset:2560\n"
"v_perm_b32 v46, v103, v99, s53\n"
"v_mfma_f32_16x16x32_bf16 a[0:3], a[180:183], v[16:19], a[0:3]\n"
"v_perm_b32 v47, v111, v107, s53\n"
"v_mov_b32_e64 v243, v5\n"
"v_mfma_f32_16x16x32_bf16 a[32:35], a[184:187], v[16:19], a[32:35]\n"
"buffer_store_dwordx2 v[242:243], v4, s[40:43], s98 offen sc1\n"
"buffer_store_dwordx2 v[242:243], v4, s[28:31], s36 offen sc1\n"
"v_mfma_f32_16x16x32_bf16 a[68:71], a[188:191], v[20:23], a[68:71]\n"
"ds_read_b128 a[168:171], v248 offset:256\n"
"buffer_load_dwordx4 v[168:171], v1, s[24:27], s97 offen offset:3072\n"
"v_mfma_f32_16x16x32_bf16 a[100:103], a[192:195], v[20:23], a[100:103]\n"
"v_mfma_f32_16x16x32_bf16 a[4:7], a[180:183], v[20:23], a[4:7]\n"
"ds_read_b128 a[172:175], v248 offset:8448\n"
"v_mfma_f32_16x16x32_bf16 a[36:39], a[184:187], v[20:23], a[36:39]\n"
"v_mfma_f32_16x16x32_bf16 a[72:75], a[188:191], v[24:27], a[72:75]\n"
"ds_read_b128 a[160:163], v244 offset:256\n"
"v_mfma_f32_16x16x32_bf16 a[104:107], a[192:195], v[24:27], a[104:107]\n"
"buffer_load_dwordx4 v[172:175], v1, s[24:27], s97 offen offset:3584\n"
"v_mfma_f32_16x16x32_bf16 a[8:11], a[180:183], v[24:27], a[8:11]\n"
"ds_read_b128 a[164:167], v244 offset:8448\n"
"v_mfma_f32_16x16x32_bf16 a[40:43], a[184:187], v[24:27], a[40:43]\n"
"v_mfma_f32_16x16x32_bf16 a[76:79], a[188:191], v[28:31], a[76:79]\n"
"s_waitcnt vmcnt(10)\n"
"s_nop 0\n"
"v_perm_b32 v16, v116, v112, s52\n"
"v_mfma_f32_16x16x32_bf16 a[108:111], a[192:195], v[28:31], a[108:111]\n"
"v_perm_b32 v17, v124, v120, s52\n"
"s_mov_b32 s96, 0x40000\n"
"buffer_load_dwordx4 v[176:179], v1, s[24:27], s96 offen\n"
"v_mfma_f32_16x16x32_bf16 a[12:15], a[180:183], v[28:31], a[12:15]\n"
"v_perm_b32 v18, v132, v128, s52\n"
"buffer_load_dwordx4 v[180:183], v1, s[24:27], s96 offen offset:512\n"
"v_mfma_f32_16x16x32_bf16 a[44:47], a[184:187], v[28:31], a[44:47]\n"
"buffer_load_dwordx4 v[184:187], v1, s[24:27], s96 offen offset:1024\n"
"v_perm_b32 v19, v140, v136, s52\n"
"buffer_load_dwordx4 v[188:191], v1, s[24:27], s96 offen offset:1536\n"
"v_mfma_f32_16x16x32_bf16 a[80:83], a[188:191], v[32:35], a[80:83]\n"
"v_perm_b32 v20, v116, v112, s53\n"
"v_perm_b32 v21, v124, v120, s53\n"
"v_mfma_f32_16x16x32_bf16 a[112:115], a[192:195], v[32:35], a[112:115]\n"
"v_perm_b32 v22, v132, v128, s53\n"
"v_perm_b32 v23, v140, v136, s53\n"
"v_mfma_f32_16x16x32_bf16 a[16:19], a[180:183], v[32:35], a[16:19]\n"
"v_perm_b32 v24, v117, v113, s52\n"
"v_perm_b32 v25, v125, v121, s52\n"
"v_mfma_f32_16x16x32_bf16 a[48:51], a[184:187], v[32:35], a[48:51]\n"
"v_perm_b32 v26, v133, v129, s52\n"
"buffer_load_dwordx4 v[192:195], v1, s[24:27], s96 offen offset:2048\n"
"v_perm_b32 v27, v141, v137, s52\n"
"v_mfma_f32_16x16x32_bf16 a[84:87], a[188:191], v[36:39], a[84:87]\n"
"v_perm_b32 v28, v117, v113, s53\n"
"v_mfma_f32_16x16x32_bf16 a[116:119], a[192:195], v[36:39], a[116:119]\n"
"v_perm_b32 v29, v125, v121, s53\n"
"v_mfma_f32_16x16x32_bf16 a[20:23], a[180:183], v[36:39], a[20:23]\n"
"v_perm_b32 v30, v133, v129, s53\n"
"v_mfma_f32_16x16x32_bf16 a[52:55], a[184:187], v[36:39], a[52:55]\n"
"v_perm_b32 v31, v141, v137, s53\n"
"buffer_load_dwordx4 v[196:199], v1, s[24:27], s96 offen offset:2560\n"
"v_mfma_f32_16x16x32_bf16 a[88:91], a[188:191], v[40:43], a[88:91]\n"
"v_perm_b32 v32, v118, v114, s52\n"
"v_perm_b32 v33, v126, v122, s52\n"
"v_mfma_f32_16x16x32_bf16 a[120:123], a[192:195], v[40:43], a[120:123]\n"
"v_perm_b32 v34, v134, v130, s52\n"
"v_perm_b32 v35, v142, v138, s52\n"
"v_mfma_f32_16x16x32_bf16 a[24:27], a[180:183], v[40:43], a[24:27]\n"
"v_perm_b32 v36, v118, v114, s53\n"
"v_perm_b32 v37, v126, v122, s53\n"
"v_mfma_f32_16x16x32_bf16 a[56:59], a[184:187], v[40:43], a[56:59]\n"
"v_perm_b32 v38, v134, v130, s53\n"
"v_perm_b32 v39, v142, v138, s53\n"
"buffer_load_dwordx4 v[200:203], v1, s[24:27], s96 offen offset:3072\n"
"v_mfma_f32_16x16x32_bf16 a[92:95], a[188:191], v[44:47], a[92:95]\n"
"v_perm_b32 v40, v119, v115, s52\n"
"v_mfma_f32_16x16x32_bf16 a[124:127], a[192:195], v[44:47], a[124:127]\n"
"v_perm_b32 v41, v127, v123, s52\n"
"v_mfma_f32_16x16x32_bf16 a[28:31], a[180:183], v[44:47], a[28:31]\n"
"v_perm_b32 v42, v135, v131, s52\n"
"v_mfma_f32_16x16x32_bf16 a[60:63], a[184:187], v[44:47], a[60:63]\n"
"v_perm_b32 v43, v143, v139, s52\n"
"s_waitcnt lgkmcnt(0)\n"
"s_nop 0\n"
"v_mfma_f32_16x16x32_bf16 a[64:67], a[168:171], v[16:19], a[64:67]\n"
"buffer_load_dwordx4 v[204:207], v1, s[24:27], s96 offen offset:3584\n"
"v_perm_b32 v44, v119, v115, s53\n"
"v_mfma_f32_16x16x32_bf16 a[96:99], a[172:175], v[16:19], a[96:99]\n"
"v_perm_b32 v45, v127, v123, s53\n"
"v_mfma_f32_16x16x32_bf16 a[0:3], a[160:163], v[16:19], a[0:3]\n"
"v_perm_b32 v46, v135, v131, s53\n"
"v_mfma_f32_16x16x32_bf16 a[32:35], a[164:167], v[16:19], a[32:35]\n"
"v_perm_b32 v47, v143, v139, s53\n"
"v_mfma_f32_16x16x32_bf16 a[68:71], a[168:171], v[20:23], a[68:71]\n"
"s_mov_b32 s97, 0x48000\n"
"buffer_load_dwordx4 v[208:211], v1, s[24:27], s97 offen\n"
"ds_read_b128 a[188:191], v249 offset:256\n"
"v_mfma_f32_16x16x32_bf16 a[100:103], a[172:175], v[20:23], a[100:103]\n"
"v_mfma_f32_16x16x32_bf16 a[4:7], a[160:163], v[20:23], a[4:7]\n"
"ds_read_b128 a[192:195], v249 offset:8448\n"
"v_mfma_f32_16x16x32_bf16 a[36:39], a[164:167], v[20:23], a[36:39]\n"
"v_mfma_f32_16x16x32_bf16 a[72:75], a[168:171], v[24:27], a[72:75]\n"
"ds_read_b128 a[180:183], v245 offset:256\n"
"buffer_load_dwordx4 v[212:215], v1, s[24:27], s97 offen offset:512\n"
"v_mfma_f32_16x16x32_bf16 a[104:107], a[172:175], v[24:27], a[104:107]\n"
"v_mfma_f32_16x16x32_bf16 a[8:11], a[160:163], v[24:27], a[8:11]\n"
"ds_read_b128 a[184:187], v245 offset:8448\n"
"v_mfma_f32_16x16x32_bf16 a[40:43], a[164:167], v[24:27], a[40:43]\n"
"v_mfma_f32_16x16x32_bf16 a[76:79], a[168:171], v[28:31], a[76:79]\n"
"s_waitcnt vmcnt(10)\n"
"s_nop 0\n"
"v_perm_b32 v16, v148, v144, s52\n"
"v_mfma_f32_16x16x32_bf16 a[108:111], a[172:175], v[28:31], a[108:111]\n"
"buffer_load_dwordx4 v[216:219], v1, s[24:27], s97 offen offset:1024\n"
"v_perm_b32 v17, v156, v152, s52\n"
"v_mfma_f32_16x16x32_bf16 a[12:15], a[160:163], v[28:31], a[12:15]\n"
"v_perm_b32 v18, v164, v160, s52\n"
"v_mfma_f32_16x16x32_bf16 a[44:47], a[164:167], v[28:31], a[44:47]\n"
"v_perm_b32 v19, v172, v168, s52\n"
"buffer_load_dwordx4 v[220:223], v1, s[24:27], s97 offen offset:1536\n"
"v_mfma_f32_16x16x32_bf16 a[80:83], a[168:171], v[32:35], a[80:83]\n"
"v_perm_b32 v20, v148, v144, s53\n"
"v_perm_b32 v21, v156, v152, s53\n"
"v_mfma_f32_16x16x32_bf16 a[112:115], a[172:175], v[32:35], a[112:115]\n"
"v_perm_b32 v22, v164, v160, s53\n"
"v_perm_b32 v23, v172, v168, s53\n"
"v_mfma_f32_16x16x32_bf16 a[16:19], a[160:163], v[32:35], a[16:19]\n"
"v_perm_b32 v24, v149, v145, s52\n"
"v_perm_b32 v25, v157, v153, s52\n"
"v_mfma_f32_16x16x32_bf16 a[48:51], a[164:167], v[32:35], a[48:51]\n"
"v_perm_b32 v26, v165, v161, s52\n"
"buffer_load_dwordx4 v[224:227], v1, s[24:27], s97 offen offset:2048\n"
"v_perm_b32 v27, v173, v169, s52\n"
"v_mfma_f32_16x16x32_bf16 a[84:87], a[168:171], v[36:39], a[84:87]\n"
"v_perm_b32 v28, v149, v145, s53\n"
"v_mfma_f32_16x16x32_bf16 a[116:119], a[172:175], v[36:39], a[116:119]\n"
"v_perm_b32 v29, v157, v153, s53\n"
"v_mfma_f32_16x16x32_bf16 a[20:23], a[160:163], v[36:39], a[20:23]\n"
"v_perm_b32 v30, v165, v161, s53\n"
"v_mfma_f32_16x16x32_bf16 a[52:55], a[164:167], v[36:39], a[52:55]\n"
"v_perm_b32 v31, v173, v169, s53\n"
"buffer_load_dwordx4 v[228:231], v1, s[24:27], s97 offen offset:2560\n"
"v_mfma_f32_16x16x32_bf16 a[88:91], a[168:171], v[40:43], a[88:91]\n"
"v_perm_b32 v32, v150, v146, s52\n"
"v_perm_b32 v33, v158, v154, s52\n"
"v_mfma_f32_16x16x32_bf16 a[120:123], a[172:175], v[40:43], a[120:123]\n"
"v_perm_b32 v34, v166, v162, s52\n"
"v_perm_b32 v35, v174, v170, s52\n"
"v_mfma_f32_16x16x32_bf16 a[24:27], a[160:163], v[40:43], a[24:27]\n"
"v_perm_b32 v36, v150, v146, s53\n"
"v_perm_b32 v37, v158, v154, s53\n"
"v_mfma_f32_16x16x32_bf16 a[56:59], a[164:167], v[40:43], a[56:59]\n"
"v_perm_b32 v38, v166, v162, s53\n"
"v_perm_b32 v39, v174, v170, s53\n"
"buffer_load_dwordx4 v[232:235], v1, s[24:27], s97 offen offset:3072\n"
"v_mfma_f32_16x16x32_bf16 a[92:95], a[168:171], v[44:47], a[92:95]\n"
"v_perm_b32 v40, v151, v147, s52\n"
"v_mfma_f32_16x16x32_bf16 a[124:127], a[172:175], v[44:47], a[124:127]\n"
"v_perm_b32 v41, v159, v155, s52\n"
"v_mfma_f32_16x16x32_bf16 a[28:31], a[160:163], v[44:47], a[28:31]\n"
"v_perm_b32 v42, v167, v163, s52\n"
"v_mfma_f32_16x16x32_bf16 a[60:63], a[164:167], v[44:47], a[60:63]\n"
"v_perm_b32 v43, v175, v171, s52\n"
"s_waitcnt lgkmcnt(0)\n"
"s_nop 0\n"
"v_mfma_f32_16x16x32_bf16 a[64:67], a[188:191], v[16:19], a[64:67]\n"
"buffer_load_dwordx4 v[236:239], v1, s[24:27], s97 offen offset:3584\n"
"v_perm_b32 v44, v151, v147, s53\n"
"v_mfma_f32_16x16x32_bf16 a[96:99], a[192:195], v[16:19], a[96:99]\n"
"v_perm_b32 v45, v159, v155, s53\n"
"v_mfma_f32_16x16x32_bf16 a[0:3], a[180:183], v[16:19], a[0:3]\n"
"v_perm_b32 v46, v167, v163, s53\n"
"v_mfma_f32_16x16x32_bf16 a[32:35], a[184:187], v[16:19], a[32:35]\n"
"v_perm_b32 v47, v175, v171, s53\n"
"v_mfma_f32_16x16x32_bf16 a[68:71], a[188:191], v[20:23], a[68:71]\n"
"s_mov_b32 s96, 0x10000\n"
"buffer_load_dwordx4 v[48:51], v1, s[24:27], s96 offen\n"
"ds_read_b128 a[168:171], v250 offset:512\n"
"v_mfma_f32_16x16x32_bf16 a[100:103], a[192:195], v[20:23], a[100:103]\n"
"v_mfma_f32_16x16x32_bf16 a[4:7], a[180:183], v[20:23], a[4:7]\n"
"ds_read_b128 a[172:175], v250 offset:8704\n"
"v_mfma_f32_16x16x32_bf16 a[36:39], a[184:187], v[20:23], a[36:39]\n"
"v_mfma_f32_16x16x32_bf16 a[72:75], a[188:191], v[24:27], a[72:75]\n"
"ds_read_b128 a[160:163], v246 offset:512\n"
"buffer_load_dwordx4 v[52:55], v1, s[24:27], s96 offen offset:512\n"
"v_mfma_f32_16x16x32_bf16 a[104:107], a[192:195], v[24:27], a[104:107]\n"
"v_mfma_f32_16x16x32_bf16 a[8:11], a[180:183], v[24:27], a[8:11]\n"
"ds_read_b128 a[164:167], v246 offset:8704\n"
"v_mfma_f32_16x16x32_bf16 a[40:43], a[184:187], v[24:27], a[40:43]\n"
"v_mfma_f32_16x16x32_bf16 a[76:79], a[188:191], v[28:31], a[76:79]\n"
"s_waitcnt vmcnt(10)\n"
"s_nop 0\n"
"v_perm_b32 v16, v180, v176, s52\n"
"v_mfma_f32_16x16x32_bf16 a[108:111], a[192:195], v[28:31], a[108:111]\n"
"buffer_load_dwordx4 v[56:59], v1, s[24:27], s96 offen offset:1024\n"
"v_perm_b32 v17, v188, v184, s52\n"
"v_mfma_f32_16x16x32_bf16 a[12:15], a[180:183], v[28:31], a[12:15]\n"
"v_perm_b32 v18, v196, v192, s52\n"
"v_mfma_f32_16x16x32_bf16 a[44:47], a[184:187], v[28:31], a[44:47]\n"
"v_perm_b32 v19, v204, v200, s52\n"
"buffer_load_dwordx4 v[60:63], v1, s[24:27], s96 offen offset:1536\n"
"v_mfma_f32_16x16x32_bf16 a[80:83], a[188:191], v[32:35], a[80:83]\n"
"v_perm_b32 v20, v180, v176, s53\n"
"v_perm_b32 v21, v188, v184, s53\n"
"v_mfma_f32_16x16x32_bf16 a[112:115], a[192:195], v[32:35], a[112:115]\n"
"v_perm_b32 v22, v196, v192, s53\n"
"v_perm_b32 v23, v204, v200, s53\n"
"v_mfma_f32_16x16x32_bf16 a[16:19], a[180:183], v[32:35], a[16:19]\n"
"v_perm_b32 v24, v181, v177, s52\n"
"v_perm_b32 v25, v189, v185, s52\n"
"v_mfma_f32_16x16x32_bf16 a[48:51], a[184:187], v[32:35], a[48:51]\n"
"v_perm_b32 v26, v197, v193, s52\n"
"buffer_load_dwordx4 v[64:67], v1, s[24:27], s96 offen offset:2048\n"
"v_perm_b32 v27, v205, v201, s52\n"
"v_mfma_f32_16x16x32_bf16 a[84:87], a[188:191], v[36:39], a[84:87]\n"
"v_perm_b32 v28, v181, v177, s53\n"
"v_mfma_f32_16x16x32_bf16 a[116:119], a[192:195], v[36:39], a[116:119]\n"
"v_perm_b32 v29, v189, v185, s53\n"
"v_mfma_f32_16x16x32_bf16 a[20:23], a[180:183], v[36:39], a[20:23]\n"
"v_perm_b32 v30, v197, v193, s53\n"
"v_mfma_f32_16x16x32_bf16 a[52:55], a[184:187], v[36:39], a[52:55]\n"
"v_perm_b32 v31, v205, v201, s53\n"
"buffer_load_dwordx4 v[68:71], v1, s[24:27], s96 offen offset:2560\n"
"v_mfma_f32_16x16x32_bf16 a[88:91], a[188:191], v[40:43], a[88:91]\n"
"v_perm_b32 v32, v182, v178, s52\n"
"v_perm_b32 v33, v190, v186, s52\n"
"v_mfma_f32_16x16x32_bf16 a[120:123], a[192:195], v[40:43], a[120:123]\n"
"v_perm_b32 v34, v198, v194, s52\n"
"v_perm_b32 v35, v206, v202, s52\n"
"v_mfma_f32_16x16x32_bf16 a[24:27], a[180:183], v[40:43], a[24:27]\n"
"v_perm_b32 v36, v182, v178, s53\n"
"v_perm_b32 v37, v190, v186, s53\n"
"v_mfma_f32_16x16x32_bf16 a[56:59], a[184:187], v[40:43], a[56:59]\n"
"v_perm_b32 v38, v198, v194, s53\n"
"v_perm_b32 v39, v206, v202, s53\n"
"buffer_load_dwordx4 v[72:75], v1, s[24:27], s96 offen offset:3072\n"
"v_mfma_f32_16x16x32_bf16 a[92:95], a[188:191], v[44:47], a[92:95]\n"
"v_perm_b32 v40, v183, v179, s52\n"
"v_mfma_f32_16x16x32_bf16 a[124:127], a[192:195], v[44:47], a[124:127]\n"
"v_perm_b32 v41, v191, v187, s52\n"
"v_mfma_f32_16x16x32_bf16 a[28:31], a[180:183], v[44:47], a[28:31]\n"
"v_perm_b32 v42, v199, v195, s52\n"
"v_mfma_f32_16x16x32_bf16 a[60:63], a[184:187], v[44:47], a[60:63]\n"
"v_perm_b32 v43, v207, v203, s52\n"
"s_waitcnt lgkmcnt(0)\n"
"s_nop 0\n"
"v_mfma_f32_16x16x32_bf16 a[64:67], a[168:171], v[16:19], a[64:67]\n"
"buffer_load_dwordx4 v[76:79], v1, s[24:27], s96 offen offset:3584\n"
"v_perm_b32 v44, v183, v179, s53\n"
"v_mfma_f32_16x16x32_bf16 a[96:99], a[172:175], v[16:19], a[96:99]\n"
"v_perm_b32 v45, v191, v187, s53\n"
"v_mfma_f32_16x16x32_bf16 a[0:3], a[160:163], v[16:19], a[0:3]\n"
"v_perm_b32 v46, v199, v195, s53\n"
"v_mfma_f32_16x16x32_bf16 a[32:35], a[164:167], v[16:19], a[32:35]\n"
"v_perm_b32 v47, v207, v203, s53\n"
"v_mfma_f32_16x16x32_bf16 a[68:71], a[168:171], v[20:23], a[68:71]\n"
"s_mov_b32 s97, 0x18000\n"
"buffer_load_dwordx4 v[80:83], v1, s[24:27], s97 offen\n"
"ds_read_b128 a[188:191], v251 offset:512\n"
"v_mfma_f32_16x16x32_bf16 a[100:103], a[172:175], v[20:23], a[100:103]\n"
"v_mfma_f32_16x16x32_bf16 a[4:7], a[160:163], v[20:23], a[4:7]\n"
"ds_read_b128 a[192:195], v251 offset:8704\n"
"v_mfma_f32_16x16x32_bf16 a[36:39], a[164:167], v[20:23], a[36:39]\n"
"v_mfma_f32_16x16x32_bf16 a[72:75], a[168:171], v[24:27], a[72:75]\n"
"ds_read_b128 a[180:183], v247 offset:512\n"
"buffer_load_dwordx4 v[84:87], v1, s[24:27], s97 offen offset:512\n"
"v_mfma_f32_16x16x32_bf16 a[104:107], a[172:175], v[24:27], a[104:107]\n"
"v_mfma_f32_16x16x32_bf16 a[8:11], a[160:163], v[24:27], a[8:11]\n"
"ds_read_b128 a[184:187], v247 offset:8704\n"
"v_mfma_f32_16x16x32_bf16 a[40:43], a[164:167], v[24:27], a[40:43]\n"
"v_mfma_f32_16x16x32_bf16 a[76:79], a[168:171], v[28:31], a[76:79]\n"
"s_waitcnt vmcnt(10)\n"
"s_nop 0\n"
"v_perm_b32 v16, v212, v208, s52\n"
"v_mfma_f32_16x16x32_bf16 a[108:111], a[172:175], v[28:31], a[108:111]\n"
"buffer_load_dwordx4 v[88:91], v1, s[24:27], s97 offen offset:1024\n"
"v_perm_b32 v17, v220, v216, s52\n"
"v_mfma_f32_16x16x32_bf16 a[12:15], a[160:163], v[28:31], a[12:15]\n"
"v_perm_b32 v18, v228, v224, s52\n"
"v_mfma_f32_16x16x32_bf16 a[44:47], a[164:167], v[28:31], a[44:47]\n"
"v_perm_b32 v19, v236, v232, s52\n"
"buffer_load_dwordx4 v[92:95], v1, s[24:27], s97 offen offset:1536\n"
"v_mfma_f32_16x16x32_bf16 a[80:83], a[168:171], v[32:35], a[80:83]\n"
"v_perm_b32 v20, v212, v208, s53\n"
"v_perm_b32 v21, v220, v216, s53\n"
"v_mfma_f32_16x16x32_bf16 a[112:115], a[172:175], v[32:35], a[112:115]\n"
"v_perm_b32 v22, v228, v224, s53\n"
"v_perm_b32 v23, v236, v232, s53\n"
"v_mfma_f32_16x16x32_bf16 a[16:19], a[160:163], v[32:35], a[16:19]\n"
"v_perm_b32 v24, v213, v209, s52\n"
"v_perm_b32 v25, v221, v217, s52\n"
"v_mfma_f32_16x16x32_bf16 a[48:51], a[164:167], v[32:35], a[48:51]\n"
"v_perm_b32 v26, v229, v225, s52\n"
"buffer_load_dwordx4 v[96:99], v1, s[24:27], s97 offen offset:2048\n"
"v_perm_b32 v27, v237, v233, s52\n"
"v_mfma_f32_16x16x32_bf16 a[84:87], a[168:171], v[36:39], a[84:87]\n"
"v_perm_b32 v28, v213, v209, s53\n"
"v_mfma_f32_16x16x32_bf16 a[116:119], a[172:175], v[36:39], a[116:119]\n"
"v_perm_b32 v29, v221, v217, s53\n"
"v_mfma_f32_16x16x32_bf16 a[20:23], a[160:163], v[36:39], a[20:23]\n"
"v_perm_b32 v30, v229, v225, s53\n"
"v_mfma_f32_16x16x32_bf16 a[52:55], a[164:167], v[36:39], a[52:55]\n"
"v_perm_b32 v31, v237, v233, s53\n"
"buffer_load_dwordx4 v[100:103], v1, s[24:27], s97 offen offset:2560\n"
"v_mfma_f32_16x16x32_bf16 a[88:91], a[168:171], v[40:43], a[88:91]\n"
"v_perm_b32 v32, v214, v210, s52\n"
"v_perm_b32 v33, v222, v218, s52\n"
"v_mfma_f32_16x16x32_bf16 a[120:123], a[172:175], v[40:43], a[120:123]\n"
"v_perm_b32 v34, v230, v226, s52\n"
"v_perm_b32 v35, v238, v234, s52\n"
"v_mfma_f32_16x16x32_bf16 a[24:27], a[160:163], v[40:43], a[24:27]\n"
"v_perm_b32 v36, v214, v210, s53\n"
"v_perm_b32 v37, v222, v218, s53\n"
"v_mfma_f32_16x16x32_bf16 a[56:59], a[164:167], v[40:43], a[56:59]\n"
"v_perm_b32 v38, v230, v226, s53\n"
"v_perm_b32 v39, v238, v234, s53\n"
"buffer_load_dwordx4 v[104:107], v1, s[24:27], s97 offen offset:3072\n"
"v_mfma_f32_16x16x32_bf16 a[92:95], a[168:171], v[44:47], a[92:95]\n"
"v_perm_b32 v40, v215, v211, s52\n"
"v_mfma_f32_16x16x32_bf16 a[124:127], a[172:175], v[44:47], a[124:127]\n"
"v_perm_b32 v41, v223, v219, s52\n"
"v_mfma_f32_16x16x32_bf16 a[28:31], a[160:163], v[44:47], a[28:31]\n"
"v_perm_b32 v42, v231, v227, s52\n"
"v_mfma_f32_16x16x32_bf16 a[60:63], a[164:167], v[44:47], a[60:63]\n"
"v_perm_b32 v43, v239, v235, s52\n"
"s_waitcnt lgkmcnt(0)\n"
"s_nop 0\n"
"v_mfma_f32_16x16x32_bf16 a[64:67], a[188:191], v[16:19], a[64:67]\n"
"buffer_load_dwordx4 v[108:111], v1, s[24:27], s97 offen offset:3584\n"
"v_perm_b32 v44, v215, v211, s53\n"
"v_perm_b32 v45, v223, v219, s53\n"
"v_mfma_f32_16x16x32_bf16 a[96:99], a[192:195], v[16:19], a[96:99]\n"
"v_perm_b32 v46, v231, v227, s53\n"
"v_mfma_f32_16x16x32_bf16 a[0:3], a[180:183], v[16:19], a[0:3]\n"
"v_perm_b32 v47, v239, v235, s53\n"
"v_mfma_f32_16x16x32_bf16 a[32:35], a[184:187], v[16:19], a[32:35]\n"
"s_barrier\n"
"s_nop 0\n"
"s_mov_b32 s96, 0x30000\n"
"buffer_load_dwordx4 v[112:115], v1, s[24:27], s96 offen\n"
"v_mfma_f32_16x16x32_bf16 a[68:71], a[188:191], v[20:23], a[68:71]\n"
"ds_read_b128 a[168:171], v248 offset:36864\n"
"v_mfma_f32_16x16x32_bf16 a[100:103], a[192:195], v[20:23], a[100:103]\n"
"v_mfma_f32_16x16x32_bf16 a[4:7], a[180:183], v[20:23], a[4:7]\n"
"buffer_load_dwordx4 v[116:119], v1, s[24:27], s96 offen offset:512\n"
"ds_read_b128 a[172:175], v248 offset:45056\n"
"v_mfma_f32_16x16x32_bf16 a[36:39], a[184:187], v[20:23], a[36:39]\n"
"v_mfma_f32_16x16x32_bf16 a[72:75], a[188:191], v[24:27], a[72:75]\n"
"ds_read_b128 a[160:163], v244 offset:36864\n"
"v_mfma_f32_16x16x32_bf16 a[104:107], a[192:195], v[24:27], a[104:107]\n"
"v_mfma_f32_16x16x32_bf16 a[8:11], a[180:183], v[24:27], a[8:11]\n"
"ds_read_b128 a[164:167], v244 offset:45056\n"
"buffer_load_dwordx4 v[120:123], v1, s[24:27], s96 offen offset:1024\n"
"v_mfma_f32_16x16x32_bf16 a[40:43], a[184:187], v[24:27], a[40:43]\n"
"v_mfma_f32_16x16x32_bf16 a[76:79], a[188:191], v[28:31], a[76:79]\n"
"s_waitcnt vmcnt(11)\n"
"s_nop 0\n"
"v_perm_b32 v16, v52, v48, s52\n"
"v_mfma_f32_16x16x32_bf16 a[108:111], a[192:195], v[28:31], a[108:111]\n"
"v_perm_b32 v17, v60, v56, s52\n"
"v_mfma_f32_16x16x32_bf16 a[12:15], a[180:183], v[28:31], a[12:15]\n"
"v_perm_b32 v18, v68, v64, s52\n"
"v_mfma_f32_16x16x32_bf16 a[44:47], a[184:187], v[28:31], a[44:47]\n"
"buffer_load_dwordx4 v[124:127], v1, s[24:27], s96 offen offset:1536\n"
"v_perm_b32 v19, v76, v72, s52\n"
"v_mfma_f32_16x16x32_bf16 a[80:83], a[188:191], v[32:35], a[80:83]\n"
"v_perm_b32 v20, v52, v48, s53\n"
"v_perm_b32 v21, v60, v56, s53\n"
"v_mfma_f32_16x16x32_bf16 a[112:115], a[192:195], v[32:35], a[112:115]\n"
"v_perm_b32 v22, v68, v64, s53\n"
"v_perm_b32 v23, v76, v72, s53\n"
"v_mfma_f32_16x16x32_bf16 a[16:19], a[180:183], v[32:35], a[16:19]\n"
"v_perm_b32 v24, v53, v49, s52\n"
"v_perm_b32 v25, v61, v57, s52\n"
"v_mfma_f32_16x16x32_bf16 a[48:51], a[184:187], v[32:35], a[48:51]\n"
"buffer_load_dwordx4 v[128:131], v1, s[24:27], s96 offen offset:2048\n"
"v_perm_b32 v26, v69, v65, s52\n"
"v_perm_b32 v27, v77, v73, s52\n"
"v_mfma_f32_16x16x32_bf16 a[84:87], a[188:191], v[36:39], a[84:87]\n"
"v_perm_b32 v28, v53, v49, s53\n"
"v_mfma_f32_16x16x32_bf16 a[116:119], a[192:195], v[36:39], a[116:119]\n"
"v_perm_b32 v29, v61, v57, s53\n"
"v_mfma_f32_16x16x32_bf16 a[20:23], a[180:183], v[36:39], a[20:23]\n"
"v_perm_b32 v30, v69, v65, s53\n"
"v_mfma_f32_16x16x32_bf16 a[52:55], a[184:187], v[36:39], a[52:55]\n"
"buffer_load_dwordx4 v[132:135], v1, s[24:27], s96 offen offset:2560\n"
"v_perm_b32 v31, v77, v73, s53\n"
"v_mfma_f32_16x16x32_bf16 a[88:91], a[188:191], v[40:43], a[88:91]\n"
"v_perm_b32 v32, v54, v50, s52\n"
"v_perm_b32 v33, v62, v58, s52\n"
"v_perm_b32 v34, v70, v66, s52\n"
"v_mfma_f32_16x16x32_bf16 a[120:123], a[192:195], v[40:43], a[120:123]\n"
"v_perm_b32 v35, v78, v74, s52\n"
"v_perm_b32 v36, v54, v50, s53\n"
"v_mfma_f32_16x16x32_bf16 a[24:27], a[180:183], v[40:43], a[24:27]\n"
"v_perm_b32 v37, v62, v58, s53\n"
"v_perm_b32 v38, v70, v66, s53\n"
"v_mfma_f32_16x16x32_bf16 a[56:59], a[184:187], v[40:43], a[56:59]\n"
"buffer_load_dwordx4 v[136:139], v1, s[24:27], s96 offen offset:3072\n"
"v_perm_b32 v39, v78, v74, s53\n"
"v_mfma_f32_16x16x32_bf16 a[92:95], a[188:191], v[44:47], a[92:95]\n"
"v_perm_b32 v40, v55, v51, s52\n"
"v_mfma_f32_16x16x32_bf16 a[124:127], a[192:195], v[44:47], a[124:127]\n"
"v_perm_b32 v41, v63, v59, s52\n"
"v_mfma_f32_16x16x32_bf16 a[28:31], a[180:183], v[44:47], a[28:31]\n"
"v_perm_b32 v42, v71, v67, s52\n"
"v_mfma_f32_16x16x32_bf16 a[60:63], a[184:187], v[44:47], a[60:63]\n"
"v_perm_b32 v43, v79, v75, s52\n"
"buffer_load_dwordx4 v[140:143], v1, s[24:27], s96 offen offset:3584\n"
"s_waitcnt lgkmcnt(0)\n"
"s_nop 0\n"
"v_mfma_f32_16x16x32_bf16 a[64:67], a[168:171], v[16:19], a[64:67]\n"
"v_perm_b32 v44, v55, v51, s53\n"
"v_mfma_f32_16x16x32_bf16 a[96:99], a[172:175], v[16:19], a[96:99]\n"
"v_perm_b32 v45, v63, v59, s53\n"
"v_mfma_f32_16x16x32_bf16 a[0:3], a[160:163], v[16:19], a[0:3]\n"
"v_perm_b32 v46, v71, v67, s53\n"
"v_mfma_f32_16x16x32_bf16 a[32:35], a[164:167], v[16:19], a[32:35]\n"
"v_perm_b32 v47, v79, v75, s53\n"
"v_mfma_f32_16x16x32_bf16 a[68:71], a[168:171], v[20:23], a[68:71]\n"
"s_mov_b32 s97, 0x38000\n"
"buffer_load_dwordx4 v[144:147], v1, s[24:27], s97 offen\n"
"ds_read_b128 a[188:191], v249 offset:36864\n"
"v_mfma_f32_16x16x32_bf16 a[100:103], a[172:175], v[20:23], a[100:103]\n"
"v_mfma_f32_16x16x32_bf16 a[4:7], a[160:163], v[20:23], a[4:7]\n"
"ds_read_b128 a[192:195], v249 offset:45056\n"
"v_mfma_f32_16x16x32_bf16 a[36:39], a[164:167], v[20:23], a[36:39]\n"
"v_mfma_f32_16x16x32_bf16 a[72:75], a[168:171], v[24:27], a[72:75]\n"
"buffer_load_dwordx4 v[148:151], v1, s[24:27], s97 offen offset:512\n"
"ds_read_b128 a[180:183], v245 offset:36864\n"
"v_mfma_f32_16x16x32_bf16 a[104:107], a[172:175], v[24:27], a[104:107]\n"
"v_mfma_f32_16x16x32_bf16 a[8:11], a[160:163], v[24:27], a[8:11]\n"
"ds_read_b128 a[184:187], v245 offset:45056\n"
"v_mfma_f32_16x16x32_bf16 a[40:43], a[164:167], v[24:27], a[40:43]\n"
"v_mfma_f32_16x16x32_bf16 a[76:79], a[168:171], v[28:31], a[76:79]\n"
"buffer_load_dwordx4 v[152:155], v1, s[24:27], s97 offen offset:1024\n"
"s_waitcnt vmcnt(11)\n"
"s_nop 0\n"
"v_perm_b32 v16, v84, v80, s52\n"
"v_mfma_f32_16x16x32_bf16 a[108:111], a[172:175], v[28:31], a[108:111]\n"
"v_perm_b32 v17, v92, v88, s52\n"
"v_mfma_f32_16x16x32_bf16 a[12:15], a[160:163], v[28:31], a[12:15]\n"
"v_perm_b32 v18, v100, v96, s52\n"
"v_mfma_f32_16x16x32_bf16 a[44:47], a[164:167], v[28:31], a[44:47]\n"
"v_perm_b32 v19, v108, v104, s52\n"
"buffer_load_dwordx4 v[156:159], v1, s[24:27], s97 offen offset:1536\n"
"v_mfma_f32_16x16x32_bf16 a[80:83], a[168:171], v[32:35], a[80:83]\n"
"v_perm_b32 v20, v84, v80, s53\n"
"v_perm_b32 v21, v92, v88, s53\n"
"v_mfma_f32_16x16x32_bf16 a[112:115], a[172:175], v[32:35], a[112:115]\n"
"v_perm_b32 v22, v100, v96, s53\n"
"v_perm_b32 v23, v108, v104, s53\n"
"v_mfma_f32_16x16x32_bf16 a[16:19], a[160:163], v[32:35], a[16:19]\n"
"v_perm_b32 v24, v85, v81, s52\n"
"v_perm_b32 v25, v93, v89, s52\n"
"v_mfma_f32_16x16x32_bf16 a[48:51], a[164:167], v[32:35], a[48:51]\n"
"v_perm_b32 v26, v101, v97, s52\n"
"buffer_load_dwordx4 v[160:163], v1, s[24:27], s97 offen offset:2048\n"
"v_perm_b32 v27, v109, v105, s52\n"
"v_mfma_f32_16x16x32_bf16 a[84:87], a[168:171], v[36:39], a[84:87]\n"
"v_perm_b32 v28, v85, v81, s53\n"
"v_mfma_f32_16x16x32_bf16 a[116:119], a[172:175], v[36:39], a[116:119]\n"
"v_perm_b32 v29, v93, v89, s53\n"
"v_mfma_f32_16x16x32_bf16 a[20:23], a[160:163], v[36:39], a[20:23]\n"
"v_perm_b32 v30, v101, v97, s53\n"
"v_mfma_f32_16x16x32_bf16 a[52:55], a[164:167], v[36:39], a[52:55]\n"
"v_perm_b32 v31, v109, v105, s53\n"
"buffer_load_dwordx4 v[164:167], v1, s[24:27], s97 offen offset:2560\n"
"v_mfma_f32_16x16x32_bf16 a[88:91], a[168:171], v[40:43], a[88:91]\n"
"v_perm_b32 v32, v86, v82, s52\n"
"v_perm_b32 v33, v94, v90, s52\n"
"v_mfma_f32_16x16x32_bf16 a[120:123], a[172:175], v[40:43], a[120:123]\n"
"v_perm_b32 v34, v102, v98, s52\n"
"v_perm_b32 v35, v110, v106, s52\n"
"v_mfma_f32_16x16x32_bf16 a[24:27], a[160:163], v[40:43], a[24:27]\n"
"v_perm_b32 v36, v86, v82, s53\n"
"v_perm_b32 v37, v94, v90, s53\n"
"v_mfma_f32_16x16x32_bf16 a[56:59], a[164:167], v[40:43], a[56:59]\n"
"v_perm_b32 v38, v102, v98, s53\n"
"v_perm_b32 v39, v110, v106, s53\n"
"buffer_load_dwordx4 v[168:171], v1, s[24:27], s97 offen offset:3072\n"
"v_mfma_f32_16x16x32_bf16 a[92:95], a[168:171], v[44:47], a[92:95]\n"
"v_perm_b32 v40, v87, v83, s52\n"
"v_mfma_f32_16x16x32_bf16 a[124:127], a[172:175], v[44:47], a[124:127]\n"
"v_perm_b32 v41, v95, v91, s52\n"
"v_mfma_f32_16x16x32_bf16 a[28:31], a[160:163], v[44:47], a[28:31]\n"
"v_perm_b32 v42, v103, v99, s52\n"
"v_mfma_f32_16x16x32_bf16 a[60:63], a[164:167], v[44:47], a[60:63]\n"
"v_perm_b32 v43, v111, v107, s52\n"
"s_waitcnt lgkmcnt(0)\n"
"s_nop 0\n"
"v_mfma_f32_16x16x32_bf16 a[64:67], a[188:191], v[16:19], a[64:67]\n"
"buffer_load_dwordx4 v[172:175], v1, s[24:27], s97 offen offset:3584\n"
"v_perm_b32 v44, v87, v83, s53\n"
"v_mfma_f32_16x16x32_bf16 a[96:99], a[192:195], v[16:19], a[96:99]\n"
"v_perm_b32 v45, v95, v91, s53\n"
"v_mfma_f32_16x16x32_bf16 a[0:3], a[180:183], v[16:19], a[0:3]\n"
"v_perm_b32 v46, v103, v99, s53\n"
"v_mfma_f32_16x16x32_bf16 a[32:35], a[184:187], v[16:19], a[32:35]\n"
"v_perm_b32 v47, v111, v107, s53\n"
"v_mfma_f32_16x16x32_bf16 a[68:71], a[188:191], v[20:23], a[68:71]\n"
"buffer_load_dword v5, v4, s[28:31], s37 offen sc1\n"
"ds_read_b128 a[168:171], v248 offset:37120\n"
"v_mfma_f32_16x16x32_bf16 a[100:103], a[192:195], v[20:23], a[100:103]\n"
"v_mfma_f32_16x16x32_bf16 a[4:7], a[180:183], v[20:23], a[4:7]\n"
"ds_read_b128 a[172:175], v248 offset:45312\n"
"v_mfma_f32_16x16x32_bf16 a[36:39], a[184:187], v[20:23], a[36:39]\n"
"v_mfma_f32_16x16x32_bf16 a[72:75], a[188:191], v[24:27], a[72:75]\n"
"ds_read_b128 a[160:163], v244 offset:37120\n"
"v_mfma_f32_16x16x32_bf16 a[104:107], a[192:195], v[24:27], a[104:107]\n"
"buffer_load_dword v11, v4, s[28:31], 0 offen offset:4 sc1\n"
"v_mfma_f32_16x16x32_bf16 a[8:11], a[180:183], v[24:27], a[8:11]\n"
"ds_read_b128 a[164:167], v244 offset:45312\n"
"v_mfma_f32_16x16x32_bf16 a[40:43], a[184:187], v[24:27], a[40:43]\n"
"v_mfma_f32_16x16x32_bf16 a[76:79], a[188:191], v[28:31], a[76:79]\n"
"s_waitcnt vmcnt(10)\n"
"s_nop 0\n"
"v_perm_b32 v16, v116, v112, s52\n"
"v_mfma_f32_16x16x32_bf16 a[108:111], a[192:195], v[28:31], a[108:111]\n"
"s_mov_b32 s96, 0x50000\n"
"buffer_load_dwordx4 v[176:179], v1, s[24:27], s96 offen\n"
"v_perm_b32 v17, v124, v120, s52\n"
"v_mfma_f32_16x16x32_bf16 a[12:15], a[180:183], v[28:31], a[12:15]\n"
"v_perm_b32 v18, v132, v128, s52\n"
"v_mfma_f32_16x16x32_bf16 a[44:47], a[184:187], v[28:31], a[44:47]\n"
"v_perm_b32 v19, v140, v136, s52\n"
"buffer_load_dwordx4 v[180:183], v1, s[24:27], s96 offen offset:512\n"
"v_mfma_f32_16x16x32_bf16 a[80:83], a[188:191], v[32:35], a[80:83]\n"
"v_perm_b32 v20, v116, v112, s53\n"
"v_perm_b32 v21, v124, v120, s53\n"
"v_mfma_f32_16x16x32_bf16 a[112:115], a[192:195], v[32:35], a[112:115]\n"
"v_perm_b32 v22, v132, v128, s53\n"
"v_perm_b32 v23, v140, v136, s53\n"
"v_mfma_f32_16x16x32_bf16 a[16:19], a[180:183], v[32:35], a[16:19]\n"
"v_perm_b32 v24, v117, v113, s52\n"
"v_perm_b32 v25, v125, v121, s52\n"
"v_mfma_f32_16x16x32_bf16 a[48:51], a[184:187], v[32:35], a[48:51]\n"
"v_perm_b32 v26, v133, v129, s52\n"
"buffer_load_dwordx4 v[184:187], v1, s[24:27], s96 offen offset:1024\n"
"v_perm_b32 v27, v141, v137, s52\n"
"v_mfma_f32_16x16x32_bf16 a[84:87], a[188:191], v[36:39], a[84:87]\n"
"v_perm_b32 v28, v117, v113, s53\n"
"v_mfma_f32_16x16x32_bf16 a[116:119], a[192:195], v[36:39], a[116:119]\n"
"v_perm_b32 v29, v125, v121, s53\n"
"v_mfma_f32_16x16x32_bf16 a[20:23], a[180:183], v[36:39], a[20:23]\n"
"v_perm_b32 v30, v133, v129, s53\n"
"v_mfma_f32_16x16x32_bf16 a[52:55], a[184:187], v[36:39], a[52:55]\n"
"v_perm_b32 v31, v141, v137, s53\n"
"buffer_load_dwordx4 v[188:191], v1, s[24:27], s96 offen offset:1536\n"
"v_mfma_f32_16x16x32_bf16 a[88:91], a[188:191], v[40:43], a[88:91]\n"
"v_perm_b32 v32, v118, v114, s52\n"
"v_perm_b32 v33, v126, v122, s52\n"
"v_mfma_f32_16x16x32_bf16 a[120:123], a[192:195], v[40:43], a[120:123]\n"
"v_perm_b32 v34, v134, v130, s52\n"
"v_perm_b32 v35, v142, v138, s52\n"
"v_mfma_f32_16x16x32_bf16 a[24:27], a[180:183], v[40:43], a[24:27]\n"
"v_perm_b32 v36, v118, v114, s53\n"
"v_perm_b32 v37, v126, v122, s53\n"
"v_mfma_f32_16x16x32_bf16 a[56:59], a[184:187], v[40:43], a[56:59]\n"
"v_perm_b32 v38, v134, v130, s53\n"
"v_perm_b32 v39, v142, v138, s53\n"
"buffer_load_dwordx4 v[192:195], v1, s[24:27], s96 offen offset:2048\n"
"v_mfma_f32_16x16x32_bf16 a[92:95], a[188:191], v[44:47], a[92:95]\n"
"v_perm_b32 v40, v119, v115, s52\n"
"v_perm_b32 v41, v127, v123, s52\n"
"v_mfma_f32_16x16x32_bf16 a[124:127], a[192:195], v[44:47], a[124:127]\n"
"v_perm_b32 v42, v135, v131, s52\n"
"v_mfma_f32_16x16x32_bf16 a[28:31], a[180:183], v[44:47], a[28:31]\n"
"v_perm_b32 v43, v143, v139, s52\n"
"v_mfma_f32_16x16x32_bf16 a[60:63], a[184:187], v[44:47], a[60:63]\n"
"s_waitcnt vmcnt(6)\n"
"v_xor_b32 v5, v5, v242\n"
"s_waitcnt vmcnt(5)\n"
"v_xor_b32 v11, v11, v243\n"
"v_or_b32 v5, v5, v11\n"
"v_cmp_ne_u32 vcc, 0, v5\n"
"s_nop 0\n"
"s_and_b32 s58, vcc_lo, s4\n"
"s_cbranch_scc1 .Lspin\n"
".Lacked:\n"
"s_barrier\n"
"buffer_load_dwordx4 v[196:199], v1, s[24:27], s96 offen offset:2560\n"
"s_lshr_b32 s58, s95, 1\n"
"buffer_load_dwordx4 v[200:203], v1, s[24:27], s96 offen offset:3072\n"
"v_lshl_add_u32 v5, v2, 4, s58\n"
"buffer_load_dwordx4 v[204:207], v1, s[24:27], s96 offen offset:3584\n"
"v_and_b32 v11, 7, v2\n"
"s_mov_b32 s97, 0x58000\n"
"buffer_load_dwordx4 v[208:211], v1, s[24:27], s97 offen\n"
"v_lshlrev_b32_e64 v11, 2, v11\n"
"buffer_load_dwordx4 v[212:215], v1, s[24:27], s97 offen offset:512\n"
"v_add_u32 v11, 0x28000, v11\n"
"buffer_load_dwordx4 v[216:219], v1, s[24:27], s97 offen offset:1024\n"
"s_waitcnt lgkmcnt(0)\n"
"s_nop 0\n"
"v_mfma_f32_16x16x32_bf16 a[64:67], a[168:171], v[16:19], a[64:67]\n"
"buffer_load_dwordx4 v[220:223], v1, s[24:27], s97 offen offset:1536\n"
"v_perm_b32 v44, v119, v115, s53\n"
"buffer_load_dwordx4 v[224:227], v1, s[24:27], s97 offen offset:2048\n"
"v_mfma_f32_16x16x32_bf16 a[96:99], a[172:175], v[16:19], a[96:99]\n"
"v_perm_b32 v45, v127, v123, s53\n"
"v_mfma_f32_16x16x32_bf16 a[0:3], a[160:163], v[16:19], a[0:3]\n"
"v_perm_b32 v46, v135, v131, s53\n"
"v_mfma_f32_16x16x32_bf16 a[32:35], a[164:167], v[16:19], a[32:35]\n"
"buffer_load_dwordx4 v[228:231], v1, s[24:27], s97 offen offset:2560\n"
"v_perm_b32 v47, v143, v139, s53\n"
"v_mfma_f32_16x16x32_bf16 a[68:71], a[168:171], v[20:23], a[68:71]\n"
"ds_read_b128 a[188:191], v249 offset:37120\n"
"v_mfma_f32_16x16x32_bf16 a[100:103], a[172:175], v[20:23], a[100:103]\n"
"v_mfma_f32_16x16x32_bf16 a[4:7], a[160:163], v[20:23], a[4:7]\n"
"ds_read_b128 a[192:195], v249 offset:45312\n"
"v_mfma_f32_16x16x32_bf16 a[36:39], a[164:167], v[20:23], a[36:39]\n"
"buffer_load_dwordx4 v[232:235], v1, s[24:27], s97 offen offset:3072\n"
"v_mfma_f32_16x16x32_bf16 a[72:75], a[168:171], v[24:27], a[72:75]\n"
"ds_read_b128 a[180:183], v245 offset:37120\n"
"v_mfma_f32_16x16x32_bf16 a[104:107], a[172:175], v[24:27], a[104:107]\n"
"v_mfma_f32_16x16x32_bf16 a[8:11], a[160:163], v[24:27], a[8:11]\n"
"ds_read_b128 a[184:187], v245 offset:45312\n"
"v_mfma_f32_16x16x32_bf16 a[40:43], a[164:167], v[24:27], a[40:43]\n"
"v_mfma_f32_16x16x32_bf16 a[76:79], a[168:171], v[28:31], a[76:79]\n"
"buffer_load_dwordx4 v[236:239], v1, s[24:27], s97 offen offset:3584\n"
"v_perm_b32 v16, v148, v144, s52\n"
"v_mfma_f32_16x16x32_bf16 a[108:111], a[172:175], v[28:31], a[108:111]\n"
"v_perm_b32 v17, v156, v152, s52\n"
"v_mfma_f32_16x16x32_bf16 a[12:15], a[160:163], v[28:31], a[12:15]\n"
"v_perm_b32 v18, v164, v160, s52\n"
"v_mfma_f32_16x16x32_bf16 a[44:47], a[164:167], v[28:31], a[44:47]\n"
"v_perm_b32 v19, v172, v168, s52\n"
"v_mfma_f32_16x16x32_bf16 a[80:83], a[168:171], v[32:35], a[80:83]\n"
"buffer_load_dword a252, v241, s[28:31], 0 offen sc1\n"
"v_perm_b32 v20, v148, v144, s53\n"
"v_perm_b32 v21, v156, v152, s53\n"
"v_mfma_f32_16x16x32_bf16 a[112:115], a[172:175], v[32:35], a[112:115]\n"
"v_perm_b32 v22, v164, v160, s53\n"
"v_perm_b32 v23, v172, v168, s53\n"
"v_mfma_f32_16x16x32_bf16 a[16:19], a[160:163], v[32:35], a[16:19]\n"
"v_perm_b32 v24, v149, v145, s52\n"
"v_perm_b32 v25, v157, v153, s52\n"
"v_mfma_f32_16x16x32_bf16 a[48:51], a[164:167], v[32:35], a[48:51]\n"
"v_perm_b32 v26, v165, v161, s52\n"
"v_perm_b32 v27, v173, v169, s52\n"
"v_mfma_f32_16x16x32_bf16 a[84:87], a[168:171], v[36:39], a[84:87]\n"
"v_perm_b32 v28, v149, v145, s53\n"
"v_mfma_f32_16x16x32_bf16 a[116:119], a[172:175], v[36:39], a[116:119]\n"
"v_perm_b32 v29, v157, v153, s53\n"
"v_mfma_f32_16x16x32_bf16 a[20:23], a[160:163], v[36:39], a[20:23]\n"
"v_perm_b32 v30, v165, v161, s53\n"
"v_mfma_f32_16x16x32_bf16 a[52:55], a[164:167], v[36:39], a[52:55]\n"
"v_perm_b32 v31, v173, v169, s53\n"
"v_mfma_f32_16x16x32_bf16 a[88:91], a[168:171], v[40:43], a[88:91]\n"
"v_perm_b32 v32, v150, v146, s52\n"
"v_perm_b32 v33, v158, v154, s52\n"
"v_mfma_f32_16x16x32_bf16 a[120:123], a[172:175], v[40:43], a[120:123]\n"
"v_perm_b32 v34, v166, v162, s52\n"
"v_perm_b32 v35, v174, v170, s52\n"
"v_mfma_f32_16x16x32_bf16 a[24:27], a[160:163], v[40:43], a[24:27]\n"
"v_perm_b32 v36, v150, v146, s53\n"
"v_perm_b32 v37, v158, v154, s53\n"
"v_mfma_f32_16x16x32_bf16 a[56:59], a[164:167], v[40:43], a[56:59]\n"
"v_perm_b32 v38, v166, v162, s53\n"
"v_perm_b32 v39, v174, v170, s53\n"
"v_mfma_f32_16x16x32_bf16 a[92:95], a[168:171], v[44:47], a[92:95]\n"
"v_perm_b32 v40, v151, v147, s52\n"
"v_mfma_f32_16x16x32_bf16 a[124:127], a[172:175], v[44:47], a[124:127]\n"
"v_perm_b32 v41, v159, v155, s52\n"
"v_mfma_f32_16x16x32_bf16 a[28:31], a[160:163], v[44:47], a[28:31]\n"
"v_perm_b32 v42, v167, v163, s52\n"
"v_mfma_f32_16x16x32_bf16 a[60:63], a[164:167], v[44:47], a[60:63]\n"
"v_perm_b32 v43, v175, v171, s52\n"
"s_waitcnt lgkmcnt(0)\n"
"s_nop 0\n"
"v_mfma_f32_16x16x32_bf16 a[64:67], a[188:191], v[16:19], a[64:67]\n"
"v_perm_b32 v44, v151, v147, s53\n"
"buffer_load_dwordx4 a[216:219], v5, s[28:31], s38 offen sc1\n"
"v_mfma_f32_16x16x32_bf16 a[96:99], a[192:195], v[16:19], a[96:99]\n"
"v_perm_b32 v45, v159, v155, s53\n"
"v_mfma_f32_16x16x32_bf16 a[0:3], a[180:183], v[16:19], a[0:3]\n"
"v_perm_b32 v46, v167, v163, s53\n"
"v_mfma_f32_16x16x32_bf16 a[32:35], a[184:187], v[16:19], a[32:35]\n"
"v_perm_b32 v47, v175, v171, s53\n"
"v_mfma_f32_16x16x32_bf16 a[68:71], a[188:191], v[20:23], a[68:71]\n"
"ds_read_b128 a[168:171], v250 offset:37376\n"
"v_mfma_f32_16x16x32_bf16 a[100:103], a[192:195], v[20:23], a[100:103]\n"
"buffer_load_dwordx4 a[220:223], v5, s[28:31], s38 offen offset:256 sc1\n"
"v_mfma_f32_16x16x32_bf16 a[4:7], a[180:183], v[20:23], a[4:7]\n"
"ds_read_b128 a[172:175], v250 offset:45568\n"
"v_mfma_f32_16x16x32_bf16 a[36:39], a[184:187], v[20:23], a[36:39]\n"
"v_mfma_f32_16x16x32_bf16 a[72:75], a[188:191], v[24:27], a[72:75]\n"
"ds_read_b128 a[160:163], v246 offset:37376\n"
"v_mfma_f32_16x16x32_bf16 a[104:107], a[192:195], v[24:27], a[104:107]\n"
"v_mfma_f32_16x16x32_bf16 a[8:11], a[180:183], v[24:27], a[8:11]\n"
"buffer_load_dwordx4 a[224:227], v5, s[28:31], s38 offen offset:512 sc1\n"
"ds_read_b128 a[164:167], v246 offset:45568\n"
"v_mfma_f32_16x16x32_bf16 a[40:43], a[184:187], v[24:27], a[40:43]\n"
"v_mfma_f32_16x16x32_bf16 a[76:79], a[188:191], v[28:31], a[76:79]\n"
"s_waitcnt vmcnt(12)\n"
"s_nop 0\n"
"v_perm_b32 v16, v180, v176, s52\n"
"v_mfma_f32_16x16x32_bf16 a[108:111], a[192:195], v[28:31], a[108:111]\n"
"v_perm_b32 v17, v188, v184, s52\n"
"v_mfma_f32_16x16x32_bf16 a[12:15], a[180:183], v[28:31], a[12:15]\n"
"buffer_load_dwordx4 a[228:231], v5, s[28:31], s38 offen offset:768 sc1\n"
"v_perm_b32 v18, v196, v192, s52\n"
"v_mfma_f32_16x16x32_bf16 a[44:47], a[184:187], v[28:31], a[44:47]\n"
"v_perm_b32 v19, v204, v200, s52\n"
"v_mfma_f32_16x16x32_bf16 a[80:83], a[188:191], v[32:35], a[80:83]\n"
"v_perm_b32 v20, v180, v176, s53\n"
"v_perm_b32 v21, v188, v184, s53\n"
"v_mfma_f32_16x16x32_bf16 a[112:115], a[192:195], v[32:35], a[112:115]\n"
"v_perm_b32 v22, v196, v192, s53\n"
"v_perm_b32 v23, v204, v200, s53\n"
"v_mfma_f32_16x16x32_bf16 a[16:19], a[180:183], v[32:35], a[16:19]\n"
"buffer_load_dwordx4 a[232:235], v5, s[28:31], s38 offen offset:1024 sc1\n"
"v_perm_b32 v24, v181, v177, s52\n"
"v_perm_b32 v25, v189, v185, s52\n"
"v_mfma_f32_16x16x32_bf16 a[48:51], a[184:187], v[32:35], a[48:51]\n"
"v_perm_b32 v26, v197, v193, s52\n"
"v_perm_b32 v27, v205, v201, s52\n"
"v_mfma_f32_16x16x32_bf16 a[84:87], a[188:191], v[36:39], a[84:87]\n"
"v_perm_b32 v28, v181, v177, s53\n"
"v_mfma_f32_16x16x32_bf16 a[116:119], a[192:195], v[36:39], a[116:119]\n"
"v_perm_b32 v29, v189, v185, s53\n"
"v_mfma_f32_16x16x32_bf16 a[20:23], a[180:183], v[36:39], a[20:23]\n"
"buffer_load_dwordx4 a[236:239], v5, s[28:31], s38 offen offset:1280 sc1\n"
"v_perm_b32 v30, v197, v193, s53\n"
"v_mfma_f32_16x16x32_bf16 a[52:55], a[184:187], v[36:39], a[52:55]\n"
"v_perm_b32 v31, v205, v201, s53\n"
"v_mfma_f32_16x16x32_bf16 a[88:91], a[188:191], v[40:43], a[88:91]\n"
"v_perm_b32 v32, v182, v178, s52\n"
"v_perm_b32 v33, v190, v186, s52\n"
"v_mfma_f32_16x16x32_bf16 a[120:123], a[192:195], v[40:43], a[120:123]\n"
"v_perm_b32 v34, v198, v194, s52\n"
"v_perm_b32 v35, v206, v202, s52\n"
"v_mfma_f32_16x16x32_bf16 a[24:27], a[180:183], v[40:43], a[24:27]\n"
"v_perm_b32 v36, v182, v178, s53\n"
"buffer_load_dwordx4 a[240:243], v5, s[28:31], s38 offen offset:1536 sc1\n"
"v_perm_b32 v37, v190, v186, s53\n"
"v_mfma_f32_16x16x32_bf16 a[56:59], a[184:187], v[40:43], a[56:59]\n"
"v_perm_b32 v38, v198, v194, s53\n"
"v_perm_b32 v39, v206, v202, s53\n"
"v_mfma_f32_16x16x32_bf16 a[92:95], a[188:191], v[44:47], a[92:95]\n"
"v_perm_b32 v40, v183, v179, s52\n"
"v_mfma_f32_16x16x32_bf16 a[124:127], a[192:195], v[44:47], a[124:127]\n"
"v_perm_b32 v41, v191, v187, s52\n"
"v_mfma_f32_16x16x32_bf16 a[28:31], a[180:183], v[44:47], a[28:31]\n"
"v_perm_b32 v42, v199, v195, s52\n"
"buffer_load_dwordx4 a[244:247], v5, s[28:31], s38 offen offset:1792 sc1\n"
"v_mfma_f32_16x16x32_bf16 a[60:63], a[184:187], v[44:47], a[60:63]\n"
"v_perm_b32 v43, v207, v203, s52\n"
"s_waitcnt lgkmcnt(0)\n"
"s_nop 0\n"
"v_mfma_f32_16x16x32_bf16 a[64:67], a[168:171], v[16:19], a[64:67]\n"
"v_perm_b32 v44, v183, v179, s53\n"
"v_mfma_f32_16x16x32_bf16 a[96:99], a[172:175], v[16:19], a[96:99]\n"
"v_perm_b32 v45, v191, v187, s53\n"
"v_mfma_f32_16x16x32_bf16 a[0:3], a[160:163], v[16:19], a[0:3]\n"
"v_perm_b32 v46, v199, v195, s53\n"
"v_mfma_f32_16x16x32_bf16 a[32:35], a[164:167], v[16:19], a[32:35]\n"
"buffer_load_dword v253, v4, s[28:31], s36 offen offset:12 sc1\n"
"v_perm_b32 v47, v207, v203, s53\n"
"v_mfma_f32_16x16x32_bf16 a[68:71], a[168:171], v[20:23], a[68:71]\n"
"ds_read_b128 a[188:191], v251 offset:37376\n"
"v_mfma_f32_16x16x32_bf16 a[100:103], a[172:175], v[20:23], a[100:103]\n"
"v_mfma_f32_16x16x32_bf16 a[4:7], a[160:163], v[20:23], a[4:7]\n"
"ds_read_b128 a[192:195], v251 offset:45568\n"
"v_mfma_f32_16x16x32_bf16 a[36:39], a[164:167], v[20:23], a[36:39]\n"
"buffer_load_dword v254, v11, s[28:31], 0 offen sc1\n"
"v_mfma_f32_16x16x32_bf16 a[72:75], a[168:171], v[24:27], a[72:75]\n"
"ds_read_b128 a[180:183], v247 offset:37376\n"
"v_mfma_f32_16x16x32_bf16 a[104:107], a[172:175], v[24:27], a[104:107]\n"
"v_mfma_f32_16x16x32_bf16 a[8:11], a[160:163], v[24:27], a[8:11]\n"
"ds_read_b128 a[184:187], v247 offset:45568\n"
"v_mfma_f32_16x16x32_bf16 a[40:43], a[164:167], v[24:27], a[40:43]\n"
"v_mfma_f32_16x16x32_bf16 a[76:79], a[168:171], v[28:31], a[76:79]\n"
"s_mov_b32 s96, 0x60000\n"
"buffer_load_dwordx4 a[200:203], v7, s[28:31], s96 offen sc1\n"
"s_waitcnt vmcnt(12)\n"
"s_nop 0\n"
"v_perm_b32 v16, v212, v208, s52\n"
"v_mfma_f32_16x16x32_bf16 a[108:111], a[172:175], v[28:31], a[108:111]\n"
"v_perm_b32 v17, v220, v216, s52\n"
"v_mfma_f32_16x16x32_bf16 a[12:15], a[160:163], v[28:31], a[12:15]\n"
"v_perm_b32 v18, v228, v224, s52\n"
"v_mfma_f32_16x16x32_bf16 a[44:47], a[164:167], v[28:31], a[44:47]\n"
"v_perm_b32 v19, v236, v232, s52\n"
"v_mfma_f32_16x16x32_bf16 a[80:83], a[168:171], v[32:35], a[80:83]\n"
"v_perm_b32 v20, v212, v208, s53\n"
"v_perm_b32 v21, v220, v216, s53\n"
"v_mfma_f32_16x16x32_bf16 a[112:115], a[172:175], v[32:35], a[112:115]\n"
"v_perm_b32 v22, v228, v224, s53\n"
"v_perm_b32 v23, v236, v232, s53\n"
"v_mfma_f32_16x16x32_bf16 a[16:19], a[160:163], v[32:35], a[16:19]\n"
"v_perm_b32 v24, v213, v209, s52\n"
"v_perm_b32 v25, v221, v217, s52\n"
"v_mfma_f32_16x16x32_bf16 a[48:51], a[164:167], v[32:35], a[48:51]\n"
"v_perm_b32 v26, v229, v225, s52\n"
"v_perm_b32 v27, v237, v233, s52\n"
"v_mfma_f32_16x16x32_bf16 a[84:87], a[168:171], v[36:39], a[84:87]\n"
"v_perm_b32 v28, v213, v209, s53\n"
"v_mfma_f32_16x16x32_bf16 a[116:119], a[172:175], v[36:39], a[116:119]\n"
"v_perm_b32 v29, v221, v217, s53\n"
"v_mfma_f32_16x16x32_bf16 a[20:23], a[160:163], v[36:39], a[20:23]\n"
"v_perm_b32 v30, v229, v225, s53\n"
"v_mfma_f32_16x16x32_bf16 a[52:55], a[164:167], v[36:39], a[52:55]\n"
"v_perm_b32 v31, v237, v233, s53\n"
"v_mfma_f32_16x16x32_bf16 a[88:91], a[168:171], v[40:43], a[88:91]\n"
"v_perm_b32 v32, v214, v210, s52\n"
"v_perm_b32 v33, v222, v218, s52\n"
"v_mfma_f32_16x16x32_bf16 a[120:123], a[172:175], v[40:43], a[120:123]\n"
"v_perm_b32 v34, v230, v226, s52\n"
"v_perm_b32 v35, v238, v234, s52\n"
"v_mfma_f32_16x16x32_bf16 a[24:27], a[160:163], v[40:43], a[24:27]\n"
"v_perm_b32 v36, v214, v210, s53\n"
"v_perm_b32 v37, v222, v218, s53\n"
"v_mfma_f32_16x16x32_bf16 a[56:59], a[164:167], v[40:43], a[56:59]\n"
"v_perm_b32 v38, v230, v226, s53\n"
"v_perm_b32 v39, v238, v234, s53\n"
"v_mfma_f32_16x16x32_bf16 a[92:95], a[168:171], v[44:47], a[92:95]\n"
"v_perm_b32 v40, v215, v211, s52\n"
"v_mfma_f32_16x16x32_bf16 a[124:127], a[172:175], v[44:47], a[124:127]\n"
"v_perm_b32 v41, v223, v219, s52\n"
"v_mfma_f32_16x16x32_bf16 a[28:31], a[160:163], v[44:47], a[28:31]\n"
"v_perm_b32 v42, v231, v227, s52\n"
"v_mfma_f32_16x16x32_bf16 a[60:63], a[164:167], v[44:47], a[60:63]\n"
"v_perm_b32 v43, v239, v235, s52\n"
"s_waitcnt lgkmcnt(0)\n"
"s_nop 0\n"
"v_mfma_f32_16x16x32_bf16 a[64:67], a[188:191], v[16:19], a[64:67]\n"
"v_perm_b32 v44, v215, v211, s53\n"
"v_mfma_f32_16x16x32_bf16 a[96:99], a[192:195], v[16:19], a[96:99]\n"
"v_perm_b32 v45, v223, v219, s53\n"
"v_mfma_f32_16x16x32_bf16 a[0:3], a[180:183], v[16:19], a[0:3]\n"
"v_perm_b32 v46, v231, v227, s53\n"
"v_mfma_f32_16x16x32_bf16 a[32:35], a[184:187], v[16:19], a[32:35]\n"
"v_perm_b32 v47, v239, v235, s53\n"
"v_mfma_f32_16x16x32_bf16 a[68:71], a[188:191], v[20:23], a[68:71]\n"
"v_mfma_f32_16x16x32_bf16 a[100:103], a[192:195], v[20:23], a[100:103]\n"
"v_mfma_f32_16x16x32_bf16 a[4:7], a[180:183], v[20:23], a[4:7]\n"
"v_mfma_f32_16x16x32_bf16 a[36:39], a[184:187], v[20:23], a[36:39]\n"
"v_mfma_f32_16x16x32_bf16 a[72:75], a[188:191], v[24:27], a[72:75]\n"
"ds_write_b128 v6, a[64:67]\n"
"v_mfma_f32_16x16x32_bf16 a[104:107], a[192:195], v[24:27], a[104:107]\n"
"v_mfma_f32_16x16x32_bf16 a[8:11], a[180:183], v[24:27], a[8:11]\n"
"buffer_load_dwordx4 a[204:207], v7, s[28:31], s96 offen offset:1024 sc1\n"
"ds_write_b128 v6, a[96:99] offset:8192\n"
"v_mfma_f32_16x16x32_bf16 a[40:43], a[184:187], v[24:27], a[40:43]\n"
"v_mfma_f32_16x16x32_bf16 a[76:79], a[188:191], v[28:31], a[76:79]\n"
"ds_write_b128 v6, a[68:71] offset:1024\n"
"v_mfma_f32_16x16x32_bf16 a[108:111], a[192:195], v[28:31], a[108:111]\n"
"v_mfma_f32_16x16x32_bf16 a[12:15], a[180:183], v[28:31], a[12:15]\n"
"ds_write_b128 v6, a[100:103] offset:9216\n"
"v_mfma_f32_16x16x32_bf16 a[44:47], a[184:187], v[28:31], a[44:47]\n"
"v_mfma_f32_16x16x32_bf16 a[80:83], a[188:191], v[32:35], a[80:83]\n"
"ds_write_b128 v6, a[72:75] offset:2048\n"
"v_mfma_f32_16x16x32_bf16 a[112:115], a[192:195], v[32:35], a[112:115]\n"
"v_mfma_f32_16x16x32_bf16 a[16:19], a[180:183], v[32:35], a[16:19]\n"
"ds_write_b128 v6, a[104:107] offset:10240\n"
"v_mfma_f32_16x16x32_bf16 a[48:51], a[184:187], v[32:35], a[48:51]\n"
"v_mfma_f32_16x16x32_bf16 a[84:87], a[188:191], v[36:39], a[84:87]\n"
"ds_write_b128 v6, a[76:79] offset:3072\n"
"v_mfma_f32_16x16x32_bf16 a[116:119], a[192:195], v[36:39], a[116:119]\n"
"v_mfma_f32_16x16x32_bf16 a[20:23], a[180:183], v[36:39], a[20:23]\n"
"ds_write_b128 v6, a[108:111] offset:11264\n"
"buffer_load_dwordx4 a[208:211], v7, s[28:31], s96 offen offset:2048 sc1\n"
"v_mfma_f32_16x16x32_bf16 a[52:55], a[184:187], v[36:39], a[52:55]\n"
"v_mfma_f32_16x16x32_bf16 a[88:91], a[188:191], v[40:43], a[88:91]\n"
"ds_write_b128 v6, a[80:83] offset:4096\n"
"v_mfma_f32_16x16x32_bf16 a[120:123], a[192:195], v[40:43], a[120:123]\n"
"v_mfma_f32_16x16x32_bf16 a[24:27], a[180:183], v[40:43], a[24:27]\n"
"buffer_load_dwordx4 a[212:215], v7, s[28:31], s96 offen offset:3072 sc1\n"
"ds_write_b128 v6, a[112:115] offset:12288\n"
"v_mfma_f32_16x16x32_bf16 a[56:59], a[184:187], v[40:43], a[56:59]\n"
"v_mfma_f32_16x16x32_bf16 a[92:95], a[188:191], v[44:47], a[92:95]\n"
"ds_write_b128 v6, a[84:87] offset:5120\n"
"s_mov_b32 s97, 0x64000\n"
"buffer_load_dwordx4 v[48:51], v7, s[28:31], s97 offen sc1\n"
"v_mfma_f32_16x16x32_bf16 a[124:127], a[192:195], v[44:47], a[124:127]\n"
"v_mfma_f32_16x16x32_bf16 a[28:31], a[180:183], v[44:47], a[28:31]\n"
"ds_write_b128 v6, a[116:119] offset:13312\n"
"v_mfma_f32_16x16x32_bf16 a[60:63], a[184:187], v[44:47], a[60:63]\n"
"buffer_load_dwordx4 v[52:55], v7, s[28:31], s97 offen offset:1024 sc1\n"
"ds_write_b128 v6, a[88:91] offset:6144\n"
"ds_write_b128 v6, a[120:123] offset:14336\n"
"ds_write_b128 v6, a[92:95] offset:7168\n"
"s_nop 0\n"
"s_nop 0\n"
"ds_write_b128 v6, a[124:127] offset:15360\n"
"buffer_load_dwordx4 v[56:59], v7, s[28:31], s97 offen offset:2048 sc1\n"
"v_subrev_u32 v1, 0x12000, v6\n"
"v_xor_b32 v1, 0x8000, v1\n"
"v_add_u32 v1, 0x12000, v1\n"
"buffer_load_dwordx4 v[60:63], v7, s[28:31], s97 offen offset:3072 sc1\n"
"s_mov_b32 s96, 0x68000\n"
"buffer_load_dwordx4 v[64:67], v7, s[28:31], s96 offen sc1\n"
"buffer_load_dwordx4 v[68:71], v7, s[28:31], s96 offen offset:1024 sc1\n"
"buffer_load_dwordx4 v[72:75], v7, s[28:31], s96 offen offset:2048 sc1\n"
"s_waitcnt vmcnt(11)\n"
"v_cmp_ne_u32 vcc, 0, v254\n"
"buffer_load_dwordx4 v[76:79], v7, s[28:31], s96 offen offset:3072 sc1\n"
"s_nop 0\n"
"s_mov_b32 s98, vcc_lo\n"
"v_cmp_ne_u32 vcc, v253, v243\n"
"s_nop 0\n"
"s_and_b32 s58, vcc_lo, s4\n"
"s_cbranch_scc1 .Lspinn\n"
".Lgotn:\n"
"v_mov_b32 v6, v252\n"
"s_waitcnt lgkmcnt(0)\n"
"s_barrier\n"
"s_nop 3\n"
".Ljoin:\n"
"s_mov_b32 s97, 0x6c000\n"
"buffer_load_dwordx4 v[80:83], v7, s[28:31], s97 offen sc1\n"
"ds_read_b128 v[32:35], v1 offset:8192\n"
"ds_read_b128 v[36:39], v1 offset:9216\n"
"ds_read_b128 v[40:43], v1 offset:10240\n"
"ds_read_b128 v[44:47], v1 offset:11264\n"
"v_accvgpr_read_b32 v16, a32\n"
"v_accvgpr_read_b32 v17, a33\n"
"v_accvgpr_read_b32 v18, a34\n"
"v_accvgpr_read_b32 v19, a35\n"
"v_accvgpr_read_b32 v20, a36\n"
"v_accvgpr_read_b32 v21, a37\n"
"v_accvgpr_read_b32 v22, a38\n"
"buffer_load_dwordx4 v[84:87], v7, s[28:31], s97 offen offset:1024 sc1\n"
"v_accvgpr_read_b32 v23, a39\n"
"v_add_u32 v10, 1, v15\n"
"v_and_b32 v9, 1, v10\n"
"v_lshrrev_b32 v10, 1, v10\n"
"v_mul_u32_u24 v9, 33, v9\n"
"v_add_u32 v10, v10, v9\n"
"s_lshl_b32 s58, s56, 4\n"
"v_add_u32_e64 v10, s58, v10\n"
"v_mul_u32_u24 v10, 0x210, v10\n"
"v_add_u32 v10, v10, v14\n"
"v_lshlrev_b32_e64 v9, 1, v14\n"
"s_lshl_b32 s58, s56, 1\n"
"s_xor_b32 s58, s58, 6\n"
"s_sub_u32 s58, s58, 2\n"
"v_mov_b32 v11, 0x7ffffff0\n"
"buffer_load_dwordx4 v[88:91], v7, s[28:31], s97 offen offset:2048 sc1\n"
"v_cmp_eq_u32 vcc, s58, v3\n"
"s_nop 1\n"
"v_cndmask_b32 v9, v11, v9, vcc\n"
"s_waitcnt lgkmcnt(2)\n"
"v_pk_add_f32 v[32:33], v[32:33], v[16:17]\n"
"v_pk_add_f32 v[34:35], v[34:35], v[18:19]\n"
"v_pk_add_f32 v[36:37], v[36:37], v[20:21]\n"
"v_pk_add_f32 v[38:39], v[38:39], v[22:23]\n"
"v_pk_mul_f32 v[16:17], v[32:33], v[32:33]\n"
"v_pk_mul_f32 v[18:19], v[34:35], v[34:35]\n"
"v_pk_mul_f32 v[20:21], v[36:37], v[36:37]\n"
"v_pk_mul_f32 v[22:23], v[38:39], v[38:39]\n"
"v_pk_fma_f32 v[16:17], v[16:17], s[60:61], s[60:61] op_sel:[0,0,1] op_sel_hi:[1,0,1]\n"
"v_pk_fma_f32 v[18:19], v[18:19], s[60:61], s[60:61] op_sel:[0,0,1] op_sel_hi:[1,0,1]\n"
"v_pk_fma_f32 v[20:21], v[20:21], s[60:61], s[60:61] op_sel:[0,0,1] op_sel_hi:[1,0,1]\n"
"v_pk_fma_f32 v[22:23], v[22:23], s[60:61], s[60:61] op_sel:[0,0,1] op_sel_hi:[1,0,1]\n"
"v_pk_mul_f32 v[16:17], v[16:17], v[32:33]\n"
"buffer_load_dwordx4 v[92:95], v7, s[28:31], s97 offen offset:3072 sc1\n"
"v_pk_mul_f32 v[18:19], v[18:19], v[34:35]\n"
"v_pk_mul_f32 v[20:21], v[20:21], v[36:37]\n"
"v_pk_mul_f32 v[22:23], v[22:23], v[38:39]\n"
"v_exp_f32_e64 v16, v16\n"
"v_accvgpr_read_b32 v24, a40\n"
"v_exp_f32_e64 v17, v17\n"
"v_accvgpr_read_b32 v25, a41\n"
"v_exp_f32_e64 v18, v18\n"
"v_accvgpr_read_b32 v26, a42\n"
"v_exp_f32_e64 v19, v19\n"
"v_accvgpr_read_b32 v27, a43\n"
"s_mov_b32 s96, 0x70000\n"
"buffer_load_dwordx4 v[96:99], v7, s[28:31], s96 offen sc1\n"
"v_exp_f32_e64 v20, v20\n"
"v_accvgpr_read_b32 v28, a44\n"
"v_exp_f32_e64 v21, v21\n"
"v_accvgpr_read_b32 v29, a45\n"
"v_exp_f32_e64 v22, v22\n"
"v_accvgpr_read_b32 v30, a46\n"
"v_exp_f32_e64 v23, v23\n"
"v_accvgpr_read_b32 v31, a47\n"
"v_pk_add_f32 v[16:17], v[16:17], 1.0 op_sel_hi:[1,0]\n"
"v_pk_add_f32 v[18:19], v[18:19], 1.0 op_sel_hi:[1,0]\n"
"v_pk_add_f32 v[20:21], v[20:21], 1.0 op_sel_hi:[1,0]\n"
"buffer_load_dwordx4 v[100:103], v7, s[28:31], s96 offen offset:1024 sc1\n"
"v_pk_add_f32 v[22:23], v[22:23], 1.0 op_sel_hi:[1,0]\n"
"v_rcp_f32 v16, v16\n"
"s_waitcnt lgkmcnt(0)\n"
"v_pk_add_f32 v[40:41], v[40:41], v[24:25]\n"
"v_pk_add_f32 v[42:43], v[42:43], v[26:27]\n"
"v_rcp_f32_e64 v17, v17\n"
"v_pk_add_f32 v[44:45], v[44:45], v[28:29]\n"
"v_pk_add_f32 v[46:47], v[46:47], v[30:31]\n"
"v_rcp_f32_e64 v18, v18\n"
"v_pk_mul_f32 v[24:25], v[40:41], v[40:41]\n"
"v_pk_mul_f32 v[26:27], v[42:43], v[42:43]\n"
"v_rcp_f32_e64 v19, v19\n"
"buffer_load_dwordx4 v[104:107], v7, s[28:31], s96 offen offset:2048 sc1\n"
"v_pk_mul_f32 v[28:29], v[44:45], v[44:45]\n"
"v_pk_mul_f32 v[30:31], v[46:47], v[46:47]\n"
"v_rcp_f32_e64 v20, v20\n"
"v_pk_fma_f32 v[24:25], v[24:25], s[60:61], s[60:61] op_sel:[0,0,1] op_sel_hi:[1,0,1]\n"
"v_pk_fma_f32 v[26:27], v[26:27], s[60:61], s[60:61] op_sel:[0,0,1] op_sel_hi:[1,0,1]\n"
"v_rcp_f32_e64 v21, v21\n"
"v_pk_fma_f32 v[28:29], v[28:29], s[60:61], s[60:61] op_sel:[0,0,1] op_sel_hi:[1,0,1]\n"
"v_pk_fma_f32 v[30:31], v[30:31], s[60:61], s[60:61] op_sel:[0,0,1] op_sel_hi:[1,0,1]\n"
"v_rcp_f32_e64 v22, v22\n"
"v_pk_mul_f32 v[24:25], v[24:25], v[40:41]\n"
"v_pk_mul_f32 v[26:27], v[26:27], v[42:43]\n"
"v_rcp_f32_e64 v23, v23\n"
"buffer_load_dwordx4 v[108:111], v7, s[28:31], s96 offen offset:3072 sc1\n"
"v_pk_mul_f32 v[28:29], v[28:29], v[44:45]\n"
"v_pk_mul_f32 v[30:31], v[30:31], v[46:47]\n"
"v_pk_mul_f32 v[16:17], v[16:17], v[32:33]\n"
"v_pk_mul_f32 v[18:19], v[18:19], v[34:35]\n"
"v_pk_mul_f32 v[20:21], v[20:21], v[36:37]\n"
"v_pk_mul_f32 v[22:23], v[22:23], v[38:39]\n"
"v_cvt_pk_bf16_f32 v240, v16, v20\n"
"v_cvt_pk_bf16_f32 v244, v17, v21\n"
"v_cvt_pk_bf16_f32 v248, v18, v22\n"
"v_cvt_pk_bf16_f32 v252, v19, v23\n"
"ds_read_b128 v[32:35], v1 offset:12288\n"
"ds_read_b128 v[36:39], v1 offset:13312\n"
"v_exp_f32_e64 v24, v24\n"
"s_mov_b32 s97, 0x74000\n"
"buffer_load_dwordx4 v[112:115], v7, s[28:31], s97 offen sc1\n"
"v_accvgpr_read_b32 v16, a48\n"
"v_exp_f32_e64 v25, v25\n"
"v_accvgpr_read_b32 v17, a49\n"
"v_exp_f32_e64 v26, v26\n"
"v_accvgpr_read_b32 v18, a50\n"
"v_exp_f32_e64 v27, v27\n"
"v_accvgpr_read_b32 v19, a51\n"
"v_exp_f32_e64 v28, v28\n"
"v_accvgpr_read_b32 v20, a52\n"
"v_exp_f32_e64 v29, v29\n"
"buffer_load_dwordx4 v[116:119], v7, s[28:31], s97 offen offset:1024 sc1\n"
"v_accvgpr_read_b32 v21, a53\n"
"v_exp_f32_e64 v30, v30\n"
"v_accvgpr_read_b32 v22, a54\n"
"v_exp_f32_e64 v31, v31\n"
"v_accvgpr_read_b32 v23, a55\n"
"v_pk_add_f32 v[24:25], v[24:25], 1.0 op_sel_hi:[1,0]\n"
"v_pk_add_f32 v[26:27], v[26:27], 1.0 op_sel_hi:[1,0]\n"
"v_pk_add_f32 v[28:29], v[28:29], 1.0 op_sel_hi:[1,0]\n"
"v_pk_add_f32 v[30:31], v[30:31], 1.0 op_sel_hi:[1,0]\n"
"v_rcp_f32 v24, v24\n"
"s_waitcnt lgkmcnt(0)\n"
"v_pk_add_f32 v[32:33], v[32:33], v[16:17]\n"
"v_pk_add_f32 v[34:35], v[34:35], v[18:19]\n"
"buffer_load_dwordx4 v[120:123], v7, s[28:31], s97 offen offset:2048 sc1\n"
"v_rcp_f32_e64 v25, v25\n"
"v_pk_add_f32 v[36:37], v[36:37], v[20:21]\n"
"v_pk_add_f32 v[38:39], v[38:39], v[22:23]\n"
"v_rcp_f32_e64 v26, v26\n"
"v_pk_mul_f32 v[16:17], v[32:33], v[32:33]\n"
"v_pk_mul_f32 v[18:19], v[34:35], v[34:35]\n"
"v_rcp_f32_e64 v27, v27\n"
"v_pk_mul_f32 v[20:21], v[36:37], v[36:37]\n"
"v_pk_mul_f32 v[22:23], v[38:39], v[38:39]\n"
"v_rcp_f32_e64 v28, v28\n"
"v_pk_fma_f32 v[16:17], v[16:17], s[60:61], s[60:61] op_sel:[0,0,1] op_sel_hi:[1,0,1]\n"
"v_pk_fma_f32 v[18:19], v[18:19], s[60:61], s[60:61] op_sel:[0,0,1] op_sel_hi:[1,0,1]\n"
"buffer_load_dwordx4 v[124:127], v7, s[28:31], s97 offen offset:3072 sc1\n"
"v_rcp_f32_e64 v29, v29\n"
"v_pk_fma_f32 v[20:21], v[20:21], s[60:61], s[60:61] op_sel:[0,0,1] op_sel_hi:[1,0,1]\n"
"v_pk_fma_f32 v[22:23], v[22:23], s[60:61], s[60:61] op_sel:[0,0,1] op_sel_hi:[1,0,1]\n"
"v_rcp_f32_e64 v30, v30\n"
"v_pk_mul_f32 v[16:17], v[16:17], v[32:33]\n"
"v_pk_mul_f32 v[18:19], v[18:19], v[34:35]\n"
"v_rcp_f32_e64 v31, v31\n"
"v_pk_mul_f32 v[20:21], v[20:21], v[36:37]\n"
"v_pk_mul_f32 v[22:23], v[22:23], v[38:39]\n"
"v_pk_mul_f32 v[24:25], v[24:25], v[40:41]\n"
"v_pk_mul_f32 v[26:27], v[26:27], v[42:43]\n"
"v_pk_mul_f32 v[28:29], v[28:29], v[44:45]\n"
"s_mov_b32 s96, 0x78000\n"
"buffer_load_dwordx4 v[128:131], v7, s[28:31], s96 offen sc1\n"
"v_pk_mul_f32 v[30:31], v[30:31], v[46:47]\n"
"v_cvt_pk_bf16_f32 v241, v24, v28\n"
"v_cvt_pk_bf16_f32 v245, v25, v29\n"
"v_cvt_pk_bf16_f32 v249, v26, v30\n"
"v_cvt_pk_bf16_f32 v253, v27, v31\n"
"ds_read_b128 v[40:43], v1 offset:14336\n"
"ds_read_b128 v[44:47], v1 offset:15360\n"
"v_exp_f32_e64 v16, v16\n"
"v_accvgpr_read_b32 v24, a56\n"
"v_exp_f32_e64 v17, v17\n"
"v_accvgpr_read_b32 v25, a57\n"
"v_exp_f32_e64 v18, v18\n"
"buffer_load_dwordx4 v[132:135], v7, s[28:31], s96 offen offset:1024 sc1\n"
"v_accvgpr_read_b32 v26, a58\n"
"v_exp_f32_e64 v19, v19\n"
"v_accvgpr_read_b32 v27, a59\n"
"v_exp_f32_e64 v20, v20\n"
"v_accvgpr_read_b32 v28, a60\n"
"v_exp_f32_e64 v21, v21\n"
"v_accvgpr_read_b32 v29, a61\n"
"v_exp_f32_e64 v22, v22\n"
"v_accvgpr_read_b32 v30, a62\n"
"v_exp_f32_e64 v23, v23\n"
"buffer_load_dwordx4 v[136:139], v7, s[28:31], s96 offen offset:2048 sc1\n"
"v_accvgpr_read_b32 v31, a63\n"
"v_pk_add_f32 v[16:17], v[16:17], 1.0 op_sel_hi:[1,0]\n"
"v_pk_add_f32 v[18:19], v[18:19], 1.0 op_sel_hi:[1,0]\n"
"v_pk_add_f32 v[20:21], v[20:21], 1.0 op_sel_hi:[1,0]\n"
"v_pk_add_f32 v[22:23], v[22:23], 1.0 op_sel_hi:[1,0]\n"
"v_rcp_f32 v16, v16\n"
"s_waitcnt lgkmcnt(0)\n"
"v_pk_add_f32 v[40:41], v[40:41], v[24:25]\n"
"v_pk_add_f32 v[42:43], v[42:43], v[26:27]\n"
"v_rcp_f32_e64 v17, v17\n"
"v_pk_add_f32 v[44:45], v[44:45], v[28:29]\n"
"v_pk_add_f32 v[46:47], v[46:47], v[30:31]\n"
"v_rcp_f32_e64 v18, v18\n"
"buffer_load_dwordx4 v[140:143], v7, s[28:31], s96 offen offset:3072 sc1\n"
"v_pk_mul_f32 v[24:25], v[40:41], v[40:41]\n"
"v_pk_mul_f32 v[26:27], v[42:43], v[42:43]\n"
"v_rcp_f32_e64 v19, v19\n"
"v_pk_mul_f32 v[28:29], v[44:45], v[44:45]\n"
"v_pk_mul_f32 v[30:31], v[46:47], v[46:47]\n"
"v_rcp_f32_e64 v20, v20\n"
"v_pk_fma_f32 v[24:25], v[24:25], s[60:61], s[60:61] op_sel:[0,0,1] op_sel_hi:[1,0,1]\n"
"v_pk_fma_f32 v[26:27], v[26:27], s[60:61], s[60:61] op_sel:[0,0,1] op_sel_hi:[1,0,1]\n"
"v_rcp_f32_e64 v21, v21\n"
"v_pk_fma_f32 v[28:29], v[28:29], s[60:61], s[60:61] op_sel:[0,0,1] op_sel_hi:[1,0,1]\n"
"v_pk_fma_f32 v[30:31], v[30:31], s[60:61], s[60:61] op_sel:[0,0,1] op_sel_hi:[1,0,1]\n"
"v_rcp_f32_e64 v22, v22\n"
"s_mov_b32 s97, 0x7c000\n"
"buffer_load_dwordx4 v[144:147], v7, s[28:31], s97 offen sc1\n"
"v_pk_mul_f32 v[24:25], v[24:25], v[40:41]\n"
"v_pk_mul_f32 v[26:27], v[26:27], v[42:43]\n"
"v_rcp_f32_e64 v23, v23\n"
"v_pk_mul_f32 v[28:29], v[28:29], v[44:45]\n"
"v_pk_mul_f32 v[30:31], v[30:31], v[46:47]\n"
"v_pk_mul_f32 v[16:17], v[16:17], v[32:33]\n"
"v_pk_mul_f32 v[18:19], v[18:19], v[34:35]\n"
"v_pk_mul_f32 v[20:21], v[20:21], v[36:37]\n"
"v_pk_mul_f32 v[22:23], v[22:23], v[38:39]\n"
"v_cvt_pk_bf16_f32 v242, v16, v20\n"
"v_cvt_pk_bf16_f32 v246, v17, v21\n"
"v_cvt_pk_bf16_f32 v250, v18, v22\n"
"v_cvt_pk_bf16_f32 v254, v19, v23\n"
"buffer_load_dwordx4 v[148:151], v7, s[28:31], s97 offen offset:1024 sc1\n"
"ds_read_b128 v[32:35], v1\n"
"ds_read_b128 v[36:39], v1 offset:1024\n"
"v_exp_f32_e64 v24, v24\n"
"v_accvgpr_read_b32 v16, a0\n"
"v_exp_f32_e64 v25, v25\n"
"v_accvgpr_read_b32 v17, a1\n"
"v_exp_f32_e64 v26, v26\n"
"v_accvgpr_read_b32 v18, a2\n"
"v_exp_f32_e64 v27, v27\n"
"v_accvgpr_read_b32 v19, a3\n"
"v_exp_f32_e64 v28, v28\n"
"buffer_load_dwordx4 v[152:155], v7, s[28:31], s97 offen offset:2048 sc1\n"
"v_accvgpr_read_b32 v20, a4\n"
"v_exp_f32_e64 v29, v29\n"
"v_accvgpr_read_b32 v21, a5\n"
"v_exp_f32_e64 v30, v30\n"
"v_accvgpr_read_b32 v22, a6\n"
"v_exp_f32_e64 v31, v31\n"
"v_accvgpr_read_b32 v23, a7\n"
"v_pk_add_f32 v[24:25], v[24:25], 1.0 op_sel_hi:[1,0]\n"
"v_pk_add_f32 v[26:27], v[26:27], 1.0 op_sel_hi:[1,0]\n"
"v_pk_add_f32 v[28:29], v[28:29], 1.0 op_sel_hi:[1,0]\n"
"v_pk_add_f32 v[30:31], v[30:31], 1.0 op_sel_hi:[1,0]\n"
"v_rcp_f32_e64 v24, v24\n"
"buffer_load_dwordx4 v[156:159], v7, s[28:31], s97 offen offset:3072 sc1\n"
"s_waitcnt lgkmcnt(0)\n"
"v_pk_add_f32 v[32:33], v[32:33], v[16:17]\n"
"v_pk_add_f32 v[34:35], v[34:35], v[18:19]\n"
"v_rcp_f32 v25, v25\n"
"v_pk_add_f32 v[36:37], v[36:37], v[20:21]\n"
"v_pk_add_f32 v[38:39], v[38:39], v[22:23]\n"
"v_rcp_f32_e64 v26, v26\n"
"v_pk_mul_f32 v[16:17], v[32:33], v[32:33]\n"
"v_pk_mul_f32 v[18:19], v[34:35], v[34:35]\n"
"v_rcp_f32_e64 v27, v27\n"
"v_pk_mul_f32 v[20:21], v[36:37], v[36:37]\n"
"v_pk_mul_f32 v[22:23], v[38:39], v[38:39]\n"
"s_mov_b32 s96, 0x80000\n"
"buffer_load_dwordx4 v[160:163], v7, s[28:31], s96 offen sc1\n"
"v_rcp_f32_e64 v28, v28\n"
"v_pk_fma_f32 v[16:17], v[16:17], s[60:61], s[60:61] op_sel:[0,0,1] op_sel_hi:[1,0,1]\n"
"v_pk_fma_f32 v[18:19], v[18:19], s[60:61], s[60:61] op_sel:[0,0,1] op_sel_hi:[1,0,1]\n"
"v_rcp_f32_e64 v29, v29\n"
"v_pk_fma_f32 v[20:21], v[20:21], s[60:61], s[60:61] op_sel:[0,0,1] op_sel_hi:[1,0,1]\n"
"v_pk_fma_f32 v[22:23], v[22:23], s[60:61], s[60:61] op_sel:[0,0,1] op_sel_hi:[1,0,1]\n"
"v_rcp_f32_e64 v30, v30\n"
"v_pk_mul_f32 v[16:17], v[16:17], v[32:33]\n"
"v_pk_mul_f32 v[18:19], v[18:19], v[34:35]\n"
"v_rcp_f32_e64 v31, v31\n"
"v_pk_mul_f32 v[20:21], v[20:21], v[36:37]\n"
"buffer_load_dwordx4 v[164:167], v7, s[28:31], s96 offen offset:1024 sc1\n"
"v_pk_mul_f32 v[22:23], v[22:23], v[38:39]\n"
"v_pk_mul_f32 v[24:25], v[24:25], v[40:41]\n"
"v_pk_mul_f32 v[26:27], v[26:27], v[42:43]\n"
"v_pk_mul_f32 v[28:29], v[28:29], v[44:45]\n"
"v_pk_mul_f32 v[30:31], v[30:31], v[46:47]\n"
"v_cvt_pk_bf16_f32 v243, v24, v28\n"
"v_cvt_pk_bf16_f32 v247, v25, v29\n"
"v_cvt_pk_bf16_f32 v251, v26, v30\n"
"v_cvt_pk_bf16_f32 v255, v27, v31\n"
"ds_read_b128 v[40:43], v1 offset:2048\n"
"ds_read_b128 v[44:47], v1 offset:3072\n"
"v_mov_b32 v11, 0x5a5a5a5a\n"
"v_accvgpr_write_b32 a128, v252\n"
"v_accvgpr_write_b32 a129, v11\n"
"buffer_load_dwordx4 v[168:171], v7, s[28:31], s96 offen offset:2048 sc1\n"
"v_accvgpr_write_b32 a130, v253\n"
"v_accvgpr_write_b32 a131, v11\n"
"v_accvgpr_write_b32 a132, v254\n"
"v_accvgpr_write_b32 a133, v11\n"
"v_accvgpr_write_b32 a134, v255\n"
"v_accvgpr_write_b32 a135, v11\n"
"buffer_store_dwordx4 a[128:131], v9, s[28:31], s7 offen sc1\n"
"buffer_store_dwordx4 a[132:135], v9, s[28:31], s7 offen offset:16 sc1\n"
"ds_write_b128 v10, v[240:243] offset:4224\n"
"buffer_load_dwordx4 v[172:175], v7, s[28:31], s96 offen offset:3072 sc1\n"
"v_exp_f32_e64 v16, v16\n"
"v_accvgpr_read_b32 v24, a8\n"
"v_exp_f32_e64 v17, v17\n"
"v_accvgpr_read_b32 v25, a9\n"
"v_exp_f32_e64 v18, v18\n"
"v_accvgpr_read_b32 v26, a10\n"
"v_exp_f32_e64 v19, v19\n"
"v_accvgpr_read_b32 v27, a11\n"
"v_exp_f32_e64 v20, v20\n"
"v_accvgpr_read_b32 v28, a12\n"
"v_exp_f32_e64 v21, v21\n"
"s_mov_b32 s97, 0x84000\n"
"buffer_load_dwordx4 v[176:179], v7, s[28:31], s97 offen sc1\n"
"ds_write_b128 v10, v[244:247] offset:4752\n"
"v_accvgpr_read_b32 v29, a13\n"
"v_exp_f32_e64 v22, v22\n"
"v_accvgpr_read_b32 v30, a14\n"
"v_exp_f32_e64 v23, v23\n"
"v_accvgpr_read_b32 v31, a15\n"
"v_pk_add_f32 v[16:17], v[16:17], 1.0 op_sel_hi:[1,0]\n"
"v_pk_add_f32 v[18:19], v[18:19], 1.0 op_sel_hi:[1,0]\n"
"buffer_load_dwordx4 v[180:183], v7, s[28:31], s97 offen offset:1024 sc1\n"
"v_pk_add_f32 v[20:21], v[20:21], 1.0 op_sel_hi:[1,0]\n"
"v_pk_add_f32 v[22:23], v[22:23], 1.0 op_sel_hi:[1,0]\n"
"v_rcp_f32 v16, v16\n"
"s_waitcnt lgkmcnt(2)\n"
"v_pk_add_f32 v[40:41], v[40:41], v[24:25]\n"
"ds_write_b128 v10, v[248:251] offset:5280\n"
"v_pk_add_f32 v[42:43], v[42:43], v[26:27]\n"
"v_rcp_f32_e64 v17, v17\n"
"v_pk_add_f32 v[44:45], v[44:45], v[28:29]\n"
"v_pk_add_f32 v[46:47], v[46:47], v[30:31]\n"
"buffer_load_dwordx4 v[184:187], v7, s[28:31], s97 offen offset:2048 sc1\n"
"v_rcp_f32_e64 v18, v18\n"
"v_pk_mul_f32 v[24:25], v[40:41], v[40:41]\n"
"v_pk_mul_f32 v[26:27], v[42:43], v[42:43]\n"
"v_rcp_f32_e64 v19, v19\n"
"v_pk_mul_f32 v[28:29], v[44:45], v[44:45]\n"
"v_pk_mul_f32 v[30:31], v[46:47], v[46:47]\n"
"v_rcp_f32_e64 v20, v20\n"
"ds_write_b128 v10, v[252:255] offset:5808\n"
"v_pk_fma_f32 v[24:25], v[24:25], s[60:61], s[60:61] op_sel:[0,0,1] op_sel_hi:[1,0,1]\n"
"buffer_load_dwordx4 v[188:191], v7, s[28:31], s97 offen offset:3072 sc1\n"
"v_pk_fma_f32 v[26:27], v[26:27], s[60:61], s[60:61] op_sel:[0,0,1] op_sel_hi:[1,0,1]\n"
"v_rcp_f32_e64 v21, v21\n"
"v_pk_fma_f32 v[28:29], v[28:29], s[60:61], s[60:61] op_sel:[0,0,1] op_sel_hi:[1,0,1]\n"
"v_pk_fma_f32 v[30:31], v[30:31], s[60:61], s[60:61] op_sel:[0,0,1] op_sel_hi:[1,0,1]\n"
"v_rcp_f32_e64 v22, v22\n"
"v_pk_mul_f32 v[24:25], v[24:25], v[40:41]\n"
"v_pk_mul_f32 v[26:27], v[26:27], v[42:43]\n"
"v_rcp_f32_e64 v23, v23\n"
"v_pk_mul_f32 v[28:29], v[28:29], v[44:45]\n"
"v_pk_mul_f32 v[30:31], v[30:31], v[46:47]\n"
"v_pk_mul_f32 v[16:17], v[16:17], v[32:33]\n"
"v_pk_mul_f32 v[18:19], v[18:19], v[34:35]\n"
"s_mov_b32 s96, 0x88000\n"
"buffer_load_dwordx4 v[192:195], v7, s[28:31], s96 offen sc1\n"
"v_pk_mul_f32 v[20:21], v[20:21], v[36:37]\n"
"v_pk_mul_f32 v[22:23], v[22:23], v[38:39]\n"
"v_cvt_pk_bf16_f32 v240, v16, v20\n"
"v_cvt_pk_bf16_f32 v244, v17, v21\n"
"v_cvt_pk_bf16_f32 v248, v18, v22\n"
"v_cvt_pk_bf16_f32 v252, v19, v23\n"
"ds_read_b128 v[32:35], v1 offset:4096\n"
"ds_read_b128 v[36:39], v1 offset:5120\n"
"v_exp_f32_e64 v24, v24\n"
"v_accvgpr_read_b32 v16, a16\n"
"v_exp_f32_e64 v25, v25\n"
"v_accvgpr_read_b32 v17, a17\n"
"v_exp_f32_e64 v26, v26\n"
"buffer_load_dwordx4 v[196:199], v7, s[28:31], s96 offen offset:1024 sc1\n"
"v_accvgpr_read_b32 v18, a18\n"
"v_exp_f32_e64 v27, v27\n"
"v_accvgpr_read_b32 v19, a19\n"
"v_exp_f32_e64 v28, v28\n"
"v_accvgpr_read_b32 v20, a20\n"
"v_exp_f32_e64 v29, v29\n"
"v_accvgpr_read_b32 v21, a21\n"
"v_exp_f32_e64 v30, v30\n"
"v_accvgpr_read_b32 v22, a22\n"
"v_exp_f32_e64 v31, v31\n"
"buffer_load_dwordx4 v[200:203], v7, s[28:31], s96 offen offset:2048 sc1\n"
"v_accvgpr_read_b32 v23, a23\n"
"v_pk_add_f32 v[24:25], v[24:25], 1.0 op_sel_hi:[1,0]\n"
"v_pk_add_f32 v[26:27], v[26:27], 1.0 op_sel_hi:[1,0]\n"
"v_pk_add_f32 v[28:29], v[28:29], 1.0 op_sel_hi:[1,0]\n"
"v_pk_add_f32 v[30:31], v[30:31], 1.0 op_sel_hi:[1,0]\n"
"v_rcp_f32 v24, v24\n"
"s_waitcnt lgkmcnt(0)\n"
"v_pk_add_f32 v[32:33], v[32:33], v[16:17]\n"
"v_pk_add_f32 v[34:35], v[34:35], v[18:19]\n"
"v_rcp_f32_e64 v25, v25\n"
"v_pk_add_f32 v[36:37], v[36:37], v[20:21]\n"
"v_pk_add_f32 v[38:39], v[38:39], v[22:23]\n"
"v_rcp_f32_e64 v26, v26\n"
"buffer_load_dwordx4 v[204:207], v7, s[28:31], s96 offen offset:3072 sc1\n"
"v_pk_mul_f32 v[16:17], v[32:33], v[32:33]\n"
"v_pk_mul_f32 v[18:19], v[34:35], v[34:35]\n"
"v_rcp_f32_e64 v27, v27\n"
"v_pk_mul_f32 v[20:21], v[36:37], v[36:37]\n"
"v_pk_mul_f32 v[22:23], v[38:39], v[38:39]\n"
"v_rcp_f32_e64 v28, v28\n"
"v_pk_fma_f32 v[16:17], v[16:17], s[60:61], s[60:61] op_sel:[0,0,1] op_sel_hi:[1,0,1]\n"
"v_pk_fma_f32 v[18:19], v[18:19], s[60:61], s[60:61] op_sel:[0,0,1] op_sel_hi:[1,0,1]\n"
"v_rcp_f32_e64 v29, v29\n"
"v_pk_fma_f32 v[20:21], v[20:21], s[60:61], s[60:61] op_sel:[0,0,1] op_sel_hi:[1,0,1]\n"
"v_pk_fma_f32 v[22:23], v[22:23], s[60:61], s[60:61] op_sel:[0,0,1] op_sel_hi:[1,0,1]\n"
"v_rcp_f32_e64 v30, v30\n"
"s_mov_b32 s97, 0x8c000\n"
"buffer_load_dwordx4 v[208:211], v7, s[28:31], s97 offen sc1\n"
"v_pk_mul_f32 v[16:17], v[16:17], v[32:33]\n"
"v_pk_mul_f32 v[18:19], v[18:19], v[34:35]\n"
"v_rcp_f32_e64 v31, v31\n"
"v_pk_mul_f32 v[20:21], v[20:21], v[36:37]\n"
"v_pk_mul_f32 v[22:23], v[22:23], v[38:39]\n"
"v_pk_mul_f32 v[24:25], v[24:25], v[40:41]\n"
"v_pk_mul_f32 v[26:27], v[26:27], v[42:43]\n"
"v_pk_mul_f32 v[28:29], v[28:29], v[44:45]\n"
"v_pk_mul_f32 v[30:31], v[30:31], v[46:47]\n"
"v_cvt_pk_bf16_f32 v241, v24, v28\n"
"v_cvt_pk_bf16_f32 v245, v25, v29\n"
"v_cvt_pk_bf16_f32 v249, v26, v30\n"
"buffer_load_dwordx4 v[212:215], v7, s[28:31], s97 offen offset:1024 sc1\n"
"v_cvt_pk_bf16_f32 v253, v27, v31\n"
"ds_read_b128 v[40:43], v1 offset:6144\n"
"ds_read_b128 v[44:47], v1 offset:7168\n"
"v_exp_f32_e64 v16, v16\n"
"v_accvgpr_read_b32 v24, a24\n"
"v_exp_f32_e64 v17, v17\n"
"v_accvgpr_read_b32 v25, a25\n"
"v_exp_f32_e64 v18, v18\n"
"v_accvgpr_read_b32 v26, a26\n"
"v_exp_f32_e64 v19, v19\n"
"v_accvgpr_read_b32 v27, a27\n"
"v_exp_f32_e64 v20, v20\n"
"buffer_load_dwordx4 v[216:219], v7, s[28:31], s97 offen offset:2048 sc1\n"
"v_accvgpr_read_b32 v28, a28\n"
"v_exp_f32_e64 v21, v21\n"
"v_accvgpr_read_b32 v29, a29\n"
"v_exp_f32_e64 v22, v22\n"
"v_accvgpr_read_b32 v30, a30\n"
"v_exp_f32_e64 v23, v23\n"
"v_accvgpr_read_b32 v31, a31\n"
"v_pk_add_f32 v[16:17], v[16:17], 1.0 op_sel_hi:[1,0]\n"
"v_pk_add_f32 v[18:19], v[18:19], 1.0 op_sel_hi:[1,0]\n"
"v_pk_add_f32 v[20:21], v[20:21], 1.0 op_sel_hi:[1,0]\n"
"v_pk_add_f32 v[22:23], v[22:23], 1.0 op_sel_hi:[1,0]\n"
"buffer_load_dwordx4 v[220:223], v7, s[28:31], s97 offen offset:3072 sc1\n"
"v_rcp_f32 v16, v16\n"
"s_waitcnt lgkmcnt(0)\n"
"v_pk_add_f32 v[40:41], v[40:41], v[24:25]\n"
"v_pk_add_f32 v[42:43], v[42:43], v[26:27]\n"
"v_rcp_f32_e64 v17, v17\n"
"v_pk_add_f32 v[44:45], v[44:45], v[28:29]\n"
"v_pk_add_f32 v[46:47], v[46:47], v[30:31]\n"
"v_rcp_f32_e64 v18, v18\n"
"v_pk_mul_f32 v[24:25], v[40:41], v[40:41]\n"
"v_pk_mul_f32 v[26:27], v[42:43], v[42:43]\n"
"v_rcp_f32_e64 v19, v19\n"
"v_pk_mul_f32 v[28:29], v[44:45], v[44:45]\n"
"s_mov_b32 s96, 0x90000\n"
"buffer_load_dwordx4 v[224:227], v7, s[28:31], s96 offen sc1\n"
"v_pk_mul_f32 v[30:31], v[46:47], v[46:47]\n"
"v_rcp_f32_e64 v20, v20\n"
"v_pk_fma_f32 v[24:25], v[24:25], s[60:61], s[60:61] op_sel:[0,0,1] op_sel_hi:[1,0,1]\n"
"v_pk_fma_f32 v[26:27], v[26:27], s[60:61], s[60:61] op_sel:[0,0,1] op_sel_hi:[1,0,1]\n"
"v_rcp_f32_e64 v21, v21\n"
"v_pk_fma_f32 v[28:29], v[28:29], s[60:61], s[60:61] op_sel:[0,0,1] op_sel_hi:[1,0,1]\n"
"v_pk_fma_f32 v[30:31], v[30:31], s[60:61], s[60:61] op_sel:[0,0,1] op_sel_hi:[1,0,1]\n"
"v_rcp_f32_e64 v22, v22\n"
"v_pk_mul_f32 v[24:25], v[24:25], v[40:41]\n"
"v_pk_mul_f32 v[26:27], v[26:27], v[42:43]\n"
"v_rcp_f32_e64 v23, v23\n"
"buffer_load_dwordx4 v[228:231], v7, s[28:31], s96 offen offset:1024 sc1\n"
"v_pk_mul_f32 v[28:29], v[28:29], v[44:45]\n"
"v_pk_mul_f32 v[30:31], v[30:31], v[46:47]\n"
"v_pk_mul_f32 v[16:17], v[16:17], v[32:33]\n"
"v_pk_mul_f32 v[18:19], v[18:19], v[34:35]\n"
"v_pk_mul_f32 v[20:21], v[20:21], v[36:37]\n"
"v_pk_mul_f32 v[22:23], v[22:23], v[38:39]\n"
"v_cvt_pk_bf16_f32 v242, v16, v20\n"
"v_cvt_pk_bf16_f32 v246, v17, v21\n"
"v_cvt_pk_bf16_f32 v250, v18, v22\n"
"v_cvt_pk_bf16_f32 v254, v19, v23\n"
"v_exp_f32 v24, v24\n"
"v_exp_f32 v25, v25\n"
"v_exp_f32_e64 v26, v26\n"
"buffer_load_dwordx4 v[232:235], v7, s[28:31], s96 offen offset:2048 sc1\n"
"v_exp_f32 v27, v27\n"
"v_exp_f32 v28, v28\n"
"v_exp_f32 v29, v29\n"
"v_exp_f32 v30, v30\n"
"v_exp_f32_e64 v31, v31\n"
"v_pk_add_f32 v[24:25], v[24:25], 1.0 op_sel_hi:[1,0]\n"
"v_pk_add_f32 v[26:27], v[26:27], 1.0 op_sel_hi:[1,0]\n"
"v_pk_add_f32 v[28:29], v[28:29], 1.0 op_sel_hi:[1,0]\n"
"v_pk_add_f32 v[30:31], v[30:31], 1.0 op_sel_hi:[1,0]\n"
"v_rcp_f32_e64 v24, v24\n"
"buffer_load_dwordx4 v[236:239], v7, s[28:31], s96 offen offset:3072 sc1\n"
"v_rcp_f32 v25, v25\n"
"v_rcp_f32 v26, v26\n"
"v_rcp_f32 v27, v27\n"
"v_rcp_f32 v28, v28\n"
"v_rcp_f32 v29, v29\n"
"v_rcp_f32 v30, v30\n"
"v_rcp_f32_e64 v31, v31\n"
"v_pk_mul_f32 v[24:25], v[24:25], v[40:41]\n"
"s_mov_b32 s97, 0x94000\n"
"buffer_load_dwordx4 a[64:67], v7, s[28:31], s97 offen sc1\n"
"v_pk_mul_f32 v[26:27], v[26:27], v[42:43]\n"
"v_pk_mul_f32 v[28:29], v[28:29], v[44:45]\n"
"v_pk_mul_f32 v[30:31], v[30:31], v[46:47]\n"
"v_cvt_pk_bf16_f32 v243, v24, v28\n"
"v_cvt_pk_bf16_f32 v247, v25, v29\n"
"v_cvt_pk_bf16_f32 v251, v26, v30\n"
"v_cvt_pk_bf16_f32 v255, v27, v31\n"
"ds_write_b128 v10, v[240:243]\n"
"s_lshl_b32 s58, s55, 8\n"
"s_lshl_b32 s99, s56, 3\n"
"ds_write_b128 v10, v[244:247] offset:528\n"
"buffer_load_dwordx4 a[68:71], v7, s[28:31], s97 offen offset:1024 sc1\n"
"s_add_u32 s59, s58, s99\n"
"v_lshlrev_b32 v5, 2, v3\n"
"ds_write_b128 v10, v[248:251] offset:1056\n"
"v_xor_b32 v5, v2, v5\n"
"v_lshlrev_b32 v5, 4, v5\n"
"ds_write_b128 v10, v[252:255] offset:1584\n"
"buffer_load_dwordx4 a[72:75], v7, s[28:31], s97 offen offset:2048 sc1\n"
"v_lshl_add_u32 v5, v3, 11, v5\n"
"s_add_u32 s99, s59, 0x24000\n"
"v_add_u32 v5, s99, v5\n"
"v_mov_b32 v8, v5\n"
"v_xor_b32 v9, 16, v5\n"
"v_xor_b32 v10, 32, v5\n"
"v_xor_b32_e64 v11, 48, v5\n"
"s_waitcnt lgkmcnt(0)\n"
"s_barrier\n"
"buffer_load_dwordx4 a[76:79], v7, s[28:31], s97 offen offset:3072 sc1\n"
"ds_read_b128 a[192:195], v13 offset:17424\n"
"ds_read_b128 a[196:199], v13 offset:25872\n"
"s_mov_b32 s96, 0x98000\n"
"buffer_load_dwordx4 a[80:83], v7, s[28:31], s96 offen sc1\n"
"s_waitcnt vmcnt(55) lgkmcnt(0)\n"
"s_nop 0\n"
"v_mfma_f32_16x16x32_bf16 v[16:19], a[192:195], a[200:203], 0\n"
"buffer_load_dwordx4 a[84:87], v7, s[28:31], s96 offen offset:1024 sc1\n"
"ds_read_b128 a[248:251], v13 offset:17488\n"
"v_mfma_f32_16x16x32_bf16 v[32:35], a[196:199], a[200:203], 0\n"
"v_mfma_f32_16x16x32_bf16 v[20:23], a[192:195], a[204:207], 0\n"
"ds_read_b128 a[252:255], v13 offset:25936\n"
"v_mfma_f32_16x16x32_bf16 v[36:39], a[196:199], a[204:207], 0\n"
"v_mfma_f32_16x16x32_bf16 v[24:27], a[192:195], a[208:211], 0\n"
"buffer_load_dwordx4 a[88:91], v7, s[28:31], s96 offen offset:2048 sc1\n"
"v_and_b32_e64 v240, 63, v0\n"
"v_mfma_f32_16x16x32_bf16 v[40:43], a[196:199], a[208:211], 0\n"
"v_mfma_f32_16x16x32_bf16 v[28:31], a[192:195], a[212:215], 0\n"
"v_lshlrev_b32_e64 v240, 4, v240\n"
"v_mfma_f32_16x16x32_bf16 v[44:47], a[196:199], a[212:215], 0\n"
"s_waitcnt vmcnt(53) lgkmcnt(0)\n"
"s_nop 0\n"
"v_mfma_f32_16x16x32_bf16 v[16:19], a[248:251], v[48:51], v[16:19]\n"
"buffer_load_dwordx4 a[92:95], v7, s[28:31], s96 offen offset:3072 sc1\n"
"ds_read_b128 a[192:195], v13 offset:17552\n"
"v_mfma_f32_16x16x32_bf16 v[32:35], a[252:255], v[48:51], v[32:35]\n"
"v_mfma_f32_16x16x32_bf16 v[20:23], a[248:251], v[52:55], v[20:23]\n"
"ds_read_b128 a[196:199], v13 offset:26000\n"
"v_mfma_f32_16x16x32_bf16 v[36:39], a[252:255], v[52:55], v[36:39]\n"
"v_mfma_f32_16x16x32_bf16 v[24:27], a[248:251], v[56:59], v[24:27]\n"
"s_mov_b32 s97, 0x9c000\n"
"buffer_load_dwordx4 a[96:99], v7, s[28:31], s97 offen sc1\n"
"s_lshl_b32 s58, s55, 13\n"
"v_mfma_f32_16x16x32_bf16 v[40:43], a[252:255], v[56:59], v[40:43]\n"
"v_mfma_f32_16x16x32_bf16 v[28:31], a[248:251], v[60:63], v[28:31]\n"
"v_add_u32 v240, s58, v240\n"
"v_mfma_f32_16x16x32_bf16 v[44:47], a[252:255], v[60:63], v[44:47]\n"
"s_waitcnt vmcnt(51) lgkmcnt(0)\n"
"s_nop 0\n"
"v_mfma_f32_16x16x32_bf16 v[16:19], a[192:195], v[64:67], v[16:19]\n"
"buffer_load_dwordx4 a[100:103], v7, s[28:31], s97 offen offset:1024 sc1\n"
"ds_read_b128 a[248:251], v13 offset:17616\n"
"v_mfma_f32_16x16x32_bf16 v[32:35], a[196:199], v[64:67], v[32:35]\n"
"v_mfma_f32_16x16x32_bf16 v[20:23], a[192:195], v[68:71], v[20:23]\n"
"ds_read_b128 a[252:255], v13 offset:26064\n"
"v_mfma_f32_16x16x32_bf16 v[36:39], a[196:199], v[68:71], v[36:39]\n"
"v_mfma_f32_16x16x32_bf16 v[24:27], a[192:195], v[72:75], v[24:27]\n"
"buffer_load_dwordx4 a[104:107], v7, s[28:31], s97 offen offset:2048 sc1\n"
"v_add_u32 v241, 0x10000, v240\n"
"v_mfma_f32_16x16x32_bf16 v[40:43], a[196:199], v[72:75], v[40:43]\n"
"v_mfma_f32_16x16x32_bf16 v[28:31], a[192:195], v[76:79], v[28:31]\n"
"v_xor_b32_e64 v244, v2, v3\n"
"v_mfma_f32_16x16x32_bf16 v[44:47], a[196:199], v[76:79], v[44:47]\n"
"s_waitcnt vmcnt(49) lgkmcnt(0)\n"
"s_nop 0\n"
"v_mfma_f32_16x16x32_bf16 v[16:19], a[248:251], v[80:83], v[16:19]\n"
"buffer_load_dwordx4 a[108:111], v7, s[28:31], s97 offen offset:3072 sc1\n"
"ds_read_b128 a[192:195], v13 offset:17680\n"
"v_mfma_f32_16x16x32_bf16 v[32:35], a[252:255], v[80:83], v[32:35]\n"
"v_mfma_f32_16x16x32_bf16 v[20:23], a[248:251], v[84:87], v[20:23]\n"
"ds_read_b128 a[196:199], v13 offset:26128\n"
"v_mfma_f32_16x16x32_bf16 v[36:39], a[252:255], v[84:87], v[36:39]\n"
"v_mfma_f32_16x16x32_bf16 v[24:27], a[248:251], v[88:91], v[24:27]\n"
"s_mov_b32 s96, 0x40000\n"
"buffer_load_dwordx4 a[112:115], v7, s[28:31], s96 offen sc1\n"
"v_lshlrev_b32_e64 v244, 4, v244\n"
"v_mfma_f32_16x16x32_bf16 v[40:43], a[252:255], v[88:91], v[40:43]\n"
"v_mfma_f32_16x16x32_bf16 v[28:31], a[248:251], v[92:95], v[28:31]\n"
"v_lshl_add_u32 v244, v3, 9, v244\n"
"v_mfma_f32_16x16x32_bf16 v[44:47], a[252:255], v[92:95], v[44:47]\n"
"s_waitcnt vmcnt(47) lgkmcnt(0)\n"
"s_nop 0\n"
"v_mfma_f32_16x16x32_bf16 v[16:19], a[192:195], v[96:99], v[16:19]\n"
"buffer_load_dwordx4 a[116:119], v7, s[28:31], s96 offen offset:1024 sc1\n"
"ds_read_b128 a[248:251], v13 offset:17744\n"
"v_mfma_f32_16x16x32_bf16 v[32:35], a[196:199], v[96:99], v[32:35]\n"
"v_mfma_f32_16x16x32_bf16 v[20:23], a[192:195], v[100:103], v[20:23]\n"
"ds_read_b128 a[252:255], v13 offset:26192\n"
"v_mfma_f32_16x16x32_bf16 v[36:39], a[196:199], v[100:103], v[36:39]\n"
"buffer_load_dwordx4 a[120:123], v7, s[28:31], s96 offen offset:2048 sc1\n"
"v_mfma_f32_16x16x32_bf16 v[24:27], a[192:195], v[104:107], v[24:27]\n"
"v_add_u32_e64 v244, s59, v244\n"
"v_mfma_f32_16x16x32_bf16 v[40:43], a[196:199], v[104:107], v[40:43]\n"
"v_mfma_f32_16x16x32_bf16 v[28:31], a[192:195], v[108:111], v[28:31]\n"
"v_xor_b32_e64 v245, 64, v244\n"
"v_mfma_f32_16x16x32_bf16 v[44:47], a[196:199], v[108:111], v[44:47]\n"
"s_waitcnt vmcnt(45) lgkmcnt(0)\n"
"s_nop 0\n"
"v_mfma_f32_16x16x32_bf16 v[16:19], a[248:251], v[112:115], v[16:19]\n"
"buffer_load_dwordx4 a[124:127], v7, s[28:31], s96 offen offset:3072 sc1\n"
"ds_read_b128 a[192:195], v13 offset:17808\n"
"v_mfma_f32_16x16x32_bf16 v[32:35], a[252:255], v[112:115], v[32:35]\n"
"v_mfma_f32_16x16x32_bf16 v[20:23], a[248:251], v[116:119], v[20:23]\n"
"ds_read_b128 a[196:199], v13 offset:26256\n"
"v_mfma_f32_16x16x32_bf16 v[36:39], a[252:255], v[116:119], v[36:39]\n"
"s_mov_b32 s97, 0x44000\n"
"buffer_load_dwordx4 a[128:131], v7, s[28:31], s97 offen sc1\n"
"v_mfma_f32_16x16x32_bf16 v[24:27], a[248:251], v[120:123], v[24:27]\n"
"v_xor_b32 v246, 0x80, v244\n"
"v_mfma_f32_16x16x32_bf16 v[40:43], a[252:255], v[120:123], v[40:43]\n"
"v_mfma_f32_16x16x32_bf16 v[28:31], a[248:251], v[124:127], v[28:31]\n"
"v_xor_b32 v247, 0xc0, v244\n"
"v_mfma_f32_16x16x32_bf16 v[44:47], a[252:255], v[124:127], v[44:47]\n"
"buffer_load_dwordx4 a[132:135], v7, s[28:31], s97 offen offset:1024 sc1\n"
"s_waitcnt vmcnt(44) lgkmcnt(0)\n"
"s_nop 0\n"
"v_mfma_f32_16x16x32_bf16 v[16:19], a[192:195], v[128:131], v[16:19]\n"
"ds_read_b128 a[248:251], v13 offset:17872\n"
"v_mfma_f32_16x16x32_bf16 v[32:35], a[196:199], v[128:131], v[32:35]\n"
"v_mfma_f32_16x16x32_bf16 v[20:23], a[192:195], v[132:135], v[20:23]\n"
"ds_read_b128 a[252:255], v13 offset:26320\n"
"buffer_load_dwordx4 a[136:139], v7, s[28:31], s97 offen offset:2048 sc1\n"
"v_mfma_f32_16x16x32_bf16 v[36:39], a[196:199], v[132:135], v[36:39]\n"
"v_mfma_f32_16x16x32_bf16 v[24:27], a[192:195], v[136:139], v[24:27]\n"
"v_xor_b32_e64 v248, v2, v12\n"
"v_mfma_f32_16x16x32_bf16 v[40:43], a[196:199], v[136:139], v[40:43]\n"
"v_mfma_f32_16x16x32_bf16 v[28:31], a[192:195], v[140:143], v[28:31]\n"
"v_lshlrev_b32_e64 v248, 4, v248\n"
"v_mfma_f32_16x16x32_bf16 v[44:47], a[196:199], v[140:143], v[44:47]\n"
"buffer_load_dwordx4 a[140:143], v7, s[28:31], s97 offen offset:3072 sc1\n"
"s_waitcnt vmcnt(42) lgkmcnt(0)\n"
"s_nop 0\n"
"v_mfma_f32_16x16x32_bf16 v[16:19], a[248:251], v[144:147], v[16:19]\n"
"ds_read_b128 a[192:195], v13 offset:528\n"
"v_mfma_f32_16x16x32_bf16 v[32:35], a[252:255], v[144:147], v[32:35]\n"
"v_mfma_f32_16x16x32_bf16 v[20:23], a[248:251], v[148:151], v[20:23]\n"
"s_mov_b32 s96, 0x48000\n"
"buffer_load_dwordx4 a[144:147], v7, s[28:31], s96 offen sc1\n"
"ds_read_b128 a[196:199], v13 offset:8976\n"
"v_mfma_f32_16x16x32_bf16 v[36:39], a[252:255], v[148:151], v[36:39]\n"
"v_mfma_f32_16x16x32_bf16 v[24:27], a[248:251], v[152:155], v[24:27]\n"
"v_lshl_add_u32 v248, v2, 9, v248\n"
"v_mfma_f32_16x16x32_bf16 v[40:43], a[252:255], v[152:155], v[40:43]\n"
"v_mfma_f32_16x16x32_bf16 v[28:31], a[248:251], v[156:159], v[28:31]\n"
"v_xor_b32_e64 v249, 64, v248\n"
"buffer_load_dwordx4 a[148:151], v7, s[28:31], s96 offen offset:1024 sc1\n"
"v_mfma_f32_16x16x32_bf16 v[44:47], a[252:255], v[156:159], v[44:47]\n"
"s_waitcnt vmcnt(38) lgkmcnt(0)\n"
"s_nop 0\n"
"v_mfma_f32_16x16x32_bf16 v[16:19], a[192:195], v[160:163], v[16:19]\n"
"ds_read_b128 a[248:251], v13 offset:592\n"
"v_mfma_f32_16x16x32_bf16 v[32:35], a[196:199], v[160:163], v[32:35]\n"
"v_mfma_f32_16x16x32_bf16 v[20:23], a[192:195], v[164:167], v[20:23]\n"
"buffer_load_dwordx4 a[152:155], v7, s[28:31], s96 offen offset:2048 sc1\n"
"ds_read_b128 a[252:255], v13 offset:9040\n"
"v_mfma_f32_16x16x32_bf16 v[36:39], a[196:199], v[164:167], v[36:39]\n"
"v_mfma_f32_16x16x32_bf16 v[24:27], a[192:195], v[168:171], v[24:27]\n"
"v_xor_b32 v250, 0x80, v248\n"
"v_mfma_f32_16x16x32_bf16 v[40:43], a[196:199], v[168:171], v[40:43]\n"
"v_mfma_f32_16x16x32_bf16 v[28:31], a[192:195], v[172:175], v[28:31]\n"
"buffer_load_dwordx4 a[156:159], v7, s[28:31], s96 offen offset:3072 sc1\n"
"v_xor_b32 v251, 0xc0, v248\n"
"v_mfma_f32_16x16x32_bf16 v[44:47], a[196:199], v[172:175], v[44:47]\n"
"s_waitcnt vmcnt(36) lgkmcnt(0)\n"
"s_nop 0\n"
"v_mfma_f32_16x16x32_bf16 v[16:19], a[248:251], v[176:179], v[16:19]\n"
"ds_read_b128 a[192:195], v13 offset:656\n"
"v_mfma_f32_16x16x32_bf16 v[32:35], a[252:255], v[176:179], v[32:35]\n"
"s_mov_b32 s97, 0x4c000\n"
"buffer_load_dwordx4 a[160:163], v7, s[28:31], s97 offen sc1\n"
"v_mfma_f32_16x16x32_bf16 v[20:23], a[248:251], v[180:183], v[20:23]\n"
"ds_read_b128 a[196:199], v13 offset:9104\n"
"v_mfma_f32_16x16x32_bf16 v[36:39], a[252:255], v[180:183], v[36:39]\n"
"v_mfma_f32_16x16x32_bf16 v[24:27], a[248:251], v[184:187], v[24:27]\n"
"s_mul_i32 s58, s56, 0x10800\n"
"v_mfma_f32_16x16x32_bf16 v[40:43], a[252:255], v[184:187], v[40:43]\n"
"buffer_load_dwordx4 a[164:167], v7, s[28:31], s97 offen offset:1024 sc1\n"
"v_mfma_f32_16x16x32_bf16 v[28:31], a[248:251], v[188:191], v[28:31]\n"
"v_lshl_add_u32 v242, v3, 9, v14\n"
"v_mfma_f32_16x16x32_bf16 v[44:47], a[252:255], v[188:191], v[44:47]\n"
"s_waitcnt vmcnt(34) lgkmcnt(0)\n"
"s_nop 0\n"
"v_mfma_f32_16x16x32_bf16 v[16:19], a[192:195], v[192:195], v[16:19]\n"
"ds_read_b128 a[248:251], v13 offset:720\n"
"v_mfma_f32_16x16x32_bf16 v[32:35], a[196:199], v[192:195], v[32:35]\n"
"buffer_load_dwordx4 a[168:171], v7, s[28:31], s97 offen offset:2048 sc1\n"
"v_mfma_f32_16x16x32_bf16 v[20:23], a[192:195], v[196:199], v[20:23]\n"
"ds_read_b128 a[252:255], v13 offset:9168\n"
"v_mfma_f32_16x16x32_bf16 v[36:39], a[196:199], v[196:199], v[36:39]\n"
"v_mfma_f32_16x16x32_bf16 v[24:27], a[192:195], v[200:203], v[24:27]\n"
"v_add_u32_e64 v242, s58, v242\n"
"v_mfma_f32_16x16x32_bf16 v[40:43], a[196:199], v[200:203], v[40:43]\n"
"buffer_load_dwordx4 a[172:175], v7, s[28:31], s97 offen offset:3072 sc1\n"
"v_mfma_f32_16x16x32_bf16 v[28:31], a[192:195], v[204:207], v[28:31]\n"
"s_lshr_b32 s58, s95, 1\n"
"v_mfma_f32_16x16x32_bf16 v[44:47], a[196:199], v[204:207], v[44:47]\n"
"s_waitcnt vmcnt(32) lgkmcnt(0)\n"
"v_mfma_f32_16x16x32_bf16 v[16:19], a[248:251], v[208:211], v[16:19]\n"
"ds_read_b128 a[192:195], v13 offset:784\n"
"v_mfma_f32_16x16x32_bf16 v[32:35], a[252:255], v[208:211], v[32:35]\n"
"buffer_load_dwordx4 v[252:255], v6, s[28:31], s6 offen sc1\n"
"v_mfma_f32_16x16x32_bf16 v[20:23], a[248:251], v[212:215], v[20:23]\n"
"ds_read_b128 a[196:199], v13 offset:9232\n"
"v_mfma_f32_16x16x32_bf16 v[36:39], a[252:255], v[212:215], v[36:39]\n"
"v_mfma_f32_16x16x32_bf16 v[24:27], a[248:251], v[216:219], v[24:27]\n"
"v_mfma_f32_16x16x32_bf16 v[40:43], a[252:255], v[216:219], v[40:43]\n"
"s_mov_b32 s96, 0x50000\n"
"buffer_load_dwordx4 a[176:179], v7, s[28:31], s96 offen sc1\n"
"v_lshl_add_u32 v243, v2, 4, s58\n"
"v_mfma_f32_16x16x32_bf16 v[28:31], a[248:251], v[220:223], v[28:31]\n"
"v_mfma_f32_16x16x32_bf16 v[44:47], a[252:255], v[220:223], v[44:47]\n"
"s_waitcnt vmcnt(30) lgkmcnt(0)\n"
"s_nop 0\n"
"v_mfma_f32_16x16x32_bf16 v[16:19], a[192:195], v[224:227], v[16:19]\n"
"ds_read_b128 a[248:251], v13 offset:848\n"
"buffer_load_dwordx4 a[180:183], v7, s[28:31], s96 offen offset:1024 sc1\n"
"v_mfma_f32_16x16x32_bf16 v[32:35], a[196:199], v[224:227], v[32:35]\n"
"v_mfma_f32_16x16x32_bf16 v[20:23], a[192:195], v[228:231], v[20:23]\n"
"v_mfma_f32_16x16x32_bf16 v[36:39], a[196:199], v[228:231], v[36:39]\n"
"v_mfma_f32_16x16x32_bf16 v[24:27], a[192:195], v[232:235], v[24:27]\n"
"ds_read_b128 a[252:255], v13 offset:9296\n"
"buffer_load_dwordx4 a[184:187], v7, s[28:31], s96 offen offset:2048 sc1\n"
"v_mfma_f32_16x16x32_bf16 v[40:43], a[196:199], v[232:235], v[40:43]\n"
"v_mfma_f32_16x16x32_bf16 v[28:31], a[192:195], v[236:239], v[28:31]\n"
"v_mfma_f32_16x16x32_bf16 v[44:47], a[196:199], v[236:239], v[44:47]\n"
"s_waitcnt vmcnt(28) lgkmcnt(0)\n"
"s_nop 0\n"
"v_mfma_f32_16x16x32_bf16 v[16:19], a[248:251], a[64:67], v[16:19]\n"
"ds_read_b128 a[192:195], v13 offset:912\n"
"buffer_load_dwordx4 a[188:191], v7, s[28:31], s96 offen offset:3072 sc1\n"
"v_mfma_f32_16x16x32_bf16 v[32:35], a[252:255], a[64:67], v[32:35]\n"
"v_mfma_f32_16x16x32_bf16 v[20:23], a[248:251], a[68:71], v[20:23]\n"
"s_mov_b32 s97, 0x54000\n"
"buffer_load_dwordx4 a[0:3], v7, s[28:31], s97 offen sc1\n"
"v_mfma_f32_16x16x32_bf16 v[36:39], a[252:255], a[68:71], v[36:39]\n"
"v_mfma_f32_16x16x32_bf16 v[24:27], a[248:251], a[72:75], v[24:27]\n"
"ds_read_b128 a[196:199], v13 offset:9360\n"
"v_mfma_f32_16x16x32_bf16 v[40:43], a[252:255], a[72:75], v[40:43]\n"
"v_mfma_f32_16x16x32_bf16 v[28:31], a[248:251], a[76:79], v[28:31]\n"
"v_mfma_f32_16x16x32_bf16 v[44:47], a[252:255], a[76:79], v[44:47]\n"
"buffer_load_dwordx4 a[4:7], v7, s[28:31], s97 offen offset:1024 sc1\n"
"s_waitcnt vmcnt(27) lgkmcnt(0)\n"
"s_nop 0\n"
"v_mfma_f32_16x16x32_bf16 v[16:19], a[192:195], a[80:83], v[16:19]\n"
"ds_read_b128 a[248:251], v13 offset:976\n"
"buffer_load_dwordx4 a[8:11], v7, s[28:31], s97 offen offset:2048 sc1\n"
"v_mfma_f32_16x16x32_bf16 v[32:35], a[196:199], a[80:83], v[32:35]\n"
"v_mfma_f32_16x16x32_bf16 v[20:23], a[192:195], a[84:87], v[20:23]\n"
"v_mfma_f32_16x16x32_bf16 v[36:39], a[196:199], a[84:87], v[36:39]\n"
"v_mfma_f32_16x16x32_bf16 v[24:27], a[192:195], a[88:91], v[24:27]\n"
"buffer_load_dwordx4 a[12:15], v7, s[28:31], s97 offen offset:3072 sc1\n"
"ds_read_b128 a[252:255], v13 offset:9424\n"
"v_mfma_f32_16x16x32_bf16 v[40:43], a[196:199], a[88:91], v[40:43]\n"
"v_mfma_f32_16x16x32_bf16 v[28:31], a[192:195], a[92:95], v[28:31]\n"
"v_mfma_f32_16x16x32_bf16 v[44:47], a[196:199], a[92:95], v[44:47]\n"
"s_waitcnt vmcnt(8)\n"
"v_xor_b32 v253, 0x5a5a5a5a, v253\n"
"v_xor_b32 v255, 0x5a5a5a5a, v255\n"
"v_or_b32 v253, v253, v255\n"
"v_cmp_ne_u32 vcc, 0, v253\n"
"s_cmp_eq_u32 s94, 0\n"
"s_cselect_b32 s62, 0, -1\n"
"s_or_b32 s58, vcc_lo, vcc_hi\n"
"s_and_b32 s58, s58, s62\n"
"s_cbranch_scc1 .Lspinh\n"
".Lgoth:\n"
"s_mov_b32 s96, 0x58000\n"
"buffer_load_dwordx4 a[16:19], v7, s[28:31], s96 offen sc1\n"
"v_and_b32_e64 v252, s62, v252\n"
"buffer_load_dwordx4 a[20:23], v7, s[28:31], s96 offen offset:1024 sc1\n"
"v_and_b32_e64 v253, s62, v254\n"
"buffer_load_dwordx4 a[24:27], v7, s[28:31], s96 offen offset:2048 sc1\n"
"v_lshrrev_b32_e64 v254, 1, v6\n"
"buffer_load_dwordx4 a[28:31], v7, s[28:31], s96 offen offset:3072 sc1\n"
"ds_write_b64 v254, v[252:253]\n"
"s_mov_b32 s97, 0x5c000\n"
"buffer_load_dwordx4 a[32:35], v7, s[28:31], s97 offen sc1\n"
"s_waitcnt lgkmcnt(1)\n"
"s_nop 0\n"
"v_mfma_f32_16x16x32_bf16 v[16:19], a[248:251], a[96:99], v[16:19]\n"
"ds_read_b128 a[192:195], v13\n"
"v_mfma_f32_16x16x32_bf16 v[32:35], a[252:255], a[96:99], v[32:35]\n"
"v_mfma_f32_16x16x32_bf16 v[20:23], a[248:251], a[100:103], v[20:23]\n"
"v_mfma_f32_16x16x32_bf16 v[36:39], a[252:255], a[100:103], v[36:39]\n"
"buffer_load_dwordx4 a[36:39], v7, s[28:31], s97 offen offset:1024 sc1\n"
"v_mfma_f32_16x16x32_bf16 v[24:27], a[248:251], a[104:107], v[24:27]\n"
"ds_read_b128 a[196:199], v13 offset:8448\n"
"v_mfma_f32_16x16x32_bf16 v[40:43], a[252:255], a[104:107], v[40:43]\n"
"v_mfma_f32_16x16x32_bf16 v[28:31], a[248:251], a[108:111], v[28:31]\n"
"v_mfma_f32_16x16x32_bf16 v[44:47], a[252:255], a[108:111], v[44:47]\n"
"buffer_load_dwordx4 a[40:43], v7, s[28:31], s97 offen offset:2048 sc1\n"
"s_waitcnt lgkmcnt(0)\n"
"s_nop 0\n"
"v_mfma_f32_16x16x32_bf16 v[16:19], a[192:195], a[112:115], v[16:19]\n"
"ds_read_b128 a[248:251], v13 offset:64\n"
"buffer_load_dwordx4 a[44:47], v7, s[28:31], s97 offen offset:3072 sc1\n"
"v_mfma_f32_16x16x32_bf16 v[32:35], a[196:199], a[112:115], v[32:35]\n"
"ds_read_b128 a[252:255], v13 offset:8512\n"
"v_mfma_f32_16x16x32_bf16 v[20:23], a[192:195], a[116:119], v[20:23]\n"
"v_accvgpr_read_b32 v144, a216\n"
"v_mfma_f32_16x16x32_bf16 v[36:39], a[196:199], a[116:119], v[36:39]\n"
"v_mfma_f32_16x16x32_bf16 v[24:27], a[192:195], a[120:123], v[24:27]\n"
"v_accvgpr_read_b32 v145, a217\n"
"v_mfma_f32_16x16x32_bf16 v[40:43], a[196:199], a[120:123], v[40:43]\n"
"s_mov_b32 s96, 0xa0000\n"
"buffer_load_dwordx4 a[48:51], v7, s[28:31], s96 offen sc1\n"
"v_accvgpr_read_b32 v146, a218\n"
"v_mfma_f32_16x16x32_bf16 v[28:31], a[192:195], a[124:127], v[28:31]\n"
"v_accvgpr_read_b32 v147, a219\n"
"v_mfma_f32_16x16x32_bf16 v[44:47], a[196:199], a[124:127], v[44:47]\n"
"s_waitcnt lgkmcnt(0)\n"
"s_nop 0\n"
"v_mfma_f32_16x16x32_bf16 v[16:19], a[248:251], a[128:131], v[16:19]\n"
"ds_read_b128 a[192:195], v13 offset:128\n"
"v_mfma_f32_16x16x32_bf16 v[32:35], a[252:255], a[128:131], v[32:35]\n"
"buffer_load_dwordx4 a[52:55], v7, s[28:31], s96 offen offset:1024 sc1\n"
"ds_read_b128 a[196:199], v13 offset:8576\n"
"v_mfma_f32_16x16x32_bf16 v[20:23], a[248:251], a[132:135], v[20:23]\n"
"v_accvgpr_read_b32 v148, a220\n"
"v_mfma_f32_16x16x32_bf16 v[36:39], a[252:255], a[132:135], v[36:39]\n"
"v_mfma_f32_16x16x32_bf16 v[24:27], a[248:251], a[136:139], v[24:27]\n"
"v_accvgpr_read_b32 v149, a221\n"
"v_mfma_f32_16x16x32_bf16 v[40:43], a[252:255], a[136:139], v[40:43]\n"
"buffer_load_dwordx4 a[56:59], v7, s[28:31], s96 offen offset:2048 sc1\n"
"v_accvgpr_read_b32 v150, a222\n"
"v_mfma_f32_16x16x32_bf16 v[28:31], a[248:251], a[140:143], v[28:31]\n"
"v_accvgpr_read_b32 v151, a223\n"
"v_mfma_f32_16x16x32_bf16 v[44:47], a[252:255], a[140:143], v[44:47]\n"
"s_waitcnt lgkmcnt(0)\n"
"s_nop 0\n"
"v_mfma_f32_16x16x32_bf16 v[16:19], a[192:195], a[144:147], v[16:19]\n"
"ds_read_b128 a[248:251], v13 offset:192\n"
"v_mfma_f32_16x16x32_bf16 v[32:35], a[196:199], a[144:147], v[32:35]\n"
"buffer_load_dwordx4 a[60:63], v7, s[28:31], s96 offen offset:3072 sc1\n"
"ds_read_b128 a[252:255], v13 offset:8640\n"
"v_mfma_f32_16x16x32_bf16 v[20:23], a[192:195], a[148:151], v[20:23]\n"
"v_accvgpr_read_b32 v152, a224\n"
"v_mfma_f32_16x16x32_bf16 v[36:39], a[196:199], a[148:151], v[36:39]\n"
"v_mfma_f32_16x16x32_bf16 v[24:27], a[192:195], a[152:155], v[24:27]\n"
"v_accvgpr_read_b32 v153, a225\n"
"v_mfma_f32_16x16x32_bf16 v[40:43], a[196:199], a[152:155], v[40:43]\n"
"v_accvgpr_read_b32 v154, a226\n"
"v_mfma_f32_16x16x32_bf16 v[28:31], a[192:195], a[156:159], v[28:31]\n"
"s_mov_b32 s97, 0xa4000\n"
"buffer_load_dwordx4 a[200:203], v7, s[28:31], s97 offen sc1\n"
"v_accvgpr_read_b32 v155, a227\n"
"v_mfma_f32_16x16x32_bf16 v[44:47], a[196:199], a[156:159], v[44:47]\n"
"s_waitcnt lgkmcnt(0)\n"
"s_nop 0\n"
"v_mfma_f32_16x16x32_bf16 v[16:19], a[248:251], a[160:163], v[16:19]\n"
"ds_read_b128 a[192:195], v13 offset:256\n"
"v_mfma_f32_16x16x32_bf16 v[32:35], a[252:255], a[160:163], v[32:35]\n"
"ds_read_b128 a[196:199], v13 offset:8704\n"
"v_mfma_f32_16x16x32_bf16 v[20:23], a[248:251], a[164:167], v[20:23]\n"
"buffer_load_dwordx4 a[204:207], v7, s[28:31], s97 offen offset:1024 sc1\n"
"v_accvgpr_read_b32 v156, a228\n"
"v_mfma_f32_16x16x32_bf16 v[36:39], a[252:255], a[164:167], v[36:39]\n"
"v_mfma_f32_16x16x32_bf16 v[24:27], a[248:251], a[168:171], v[24:27]\n"
"v_accvgpr_read_b32 v157, a229\n"
"v_mfma_f32_16x16x32_bf16 v[40:43], a[252:255], a[168:171], v[40:43]\n"
"v_accvgpr_read_b32 v158, a230\n"
"v_mfma_f32_16x16x32_bf16 v[28:31], a[248:251], a[172:175], v[28:31]\n"
"buffer_load_dwordx4 a[208:211], v7, s[28:31], s97 offen offset:2048 sc1\n"
"v_accvgpr_read_b32 v159, a231\n"
"v_mfma_f32_16x16x32_bf16 v[44:47], a[252:255], a[172:175], v[44:47]\n"
"s_waitcnt vmcnt(19) lgkmcnt(0)\n"
"s_nop 0\n"
"v_mfma_f32_16x16x32_bf16 v[16:19], a[192:195], a[176:179], v[16:19]\n"
"ds_read_b128 a[248:251], v13 offset:320\n"
"v_mfma_f32_16x16x32_bf16 v[32:35], a[196:199], a[176:179], v[32:35]\n"
"ds_read_b128 a[252:255], v13 offset:8768\n"
"v_mfma_f32_16x16x32_bf16 v[20:23], a[192:195], a[180:183], v[20:23]\n"
"v_accvgpr_read_b32 v160, a232\n"
"buffer_load_dwordx4 a[212:215], v7, s[28:31], s97 offen offset:3072 sc1\n"
"v_mfma_f32_16x16x32_bf16 v[36:39], a[196:199], a[180:183], v[36:39]\n"
"v_mfma_f32_16x16x32_bf16 v[24:27], a[192:195], a[184:187], v[24:27]\n"
"v_accvgpr_read_b32 v161, a233\n"
"v_mfma_f32_16x16x32_bf16 v[40:43], a[196:199], a[184:187], v[40:43]\n"
"v_accvgpr_read_b32 v162, a234\n"
"v_mfma_f32_16x16x32_bf16 v[28:31], a[192:195], a[188:191], v[28:31]\n"
"v_accvgpr_read_b32 v163, a235\n"
"v_mfma_f32_16x16x32_bf16 v[44:47], a[196:199], a[188:191], v[44:47]\n"
"s_mov_b32 s96, 0xa8000\n"
"buffer_load_dwordx4 v[48:51], v7, s[28:31], s96 offen sc1\n"
"s_waitcnt vmcnt(17) lgkmcnt(0)\n"
"s_nop 0\n"
"v_mfma_f32_16x16x32_bf16 v[16:19], a[248:251], a[0:3], v[16:19]\n"
"ds_read_b128 a[192:195], v13 offset:384\n"
"v_mfma_f32_16x16x32_bf16 v[32:35], a[252:255], a[0:3], v[32:35]\n"
"ds_read_b128 a[196:199], v13 offset:8832\n"
"v_mfma_f32_16x16x32_bf16 v[20:23], a[248:251], a[4:7], v[20:23]\n"
"v_accvgpr_read_b32 v164, a236\n"
"v_mfma_f32_16x16x32_bf16 v[36:39], a[252:255], a[4:7], v[36:39]\n"
"buffer_load_dwordx4 v[52:55], v7, s[28:31], s96 offen offset:1024 sc1\n"
"v_mfma_f32_16x16x32_bf16 v[24:27], a[248:251], a[8:11], v[24:27]\n"
"v_accvgpr_read_b32 v165, a237\n"
"v_mfma_f32_16x16x32_bf16 v[40:43], a[252:255], a[8:11], v[40:43]\n"
"v_accvgpr_read_b32 v166, a238\n"
"v_mfma_f32_16x16x32_bf16 v[28:31], a[248:251], a[12:15], v[28:31]\n"
"v_accvgpr_read_b32 v167, a239\n"
"v_mfma_f32_16x16x32_bf16 v[44:47], a[252:255], a[12:15], v[44:47]\n"
"s_waitcnt vmcnt(14) lgkmcnt(0)\n"
"s_nop 0\n"
"v_mfma_f32_16x16x32_bf16 v[16:19], a[192:195], a[16:19], v[16:19]\n"
"buffer_load_dwordx4 v[56:59], v7, s[28:31], s96 offen offset:2048 sc1\n"
"ds_read_b128 a[248:251], v13 offset:448\n"
"v_mfma_f32_16x16x32_bf16 v[32:35], a[196:199], a[16:19], v[32:35]\n"
"ds_read_b128 a[252:255], v13 offset:8896\n"
"v_mfma_f32_16x16x32_bf16 v[20:23], a[192:195], a[20:23], v[20:23]\n"
"v_accvgpr_read_b32 v168, a240\n"
"v_mfma_f32_16x16x32_bf16 v[36:39], a[196:199], a[20:23], v[36:39]\n"
"v_mfma_f32_16x16x32_bf16 v[24:27], a[192:195], a[24:27], v[24:27]\n"
"buffer_load_dwordx4 v[60:63], v7, s[28:31], s96 offen offset:3072 sc1\n"
"v_accvgpr_read_b32 v169, a241\n"
"v_mfma_f32_16x16x32_bf16 v[40:43], a[196:199], a[24:27], v[40:43]\n"
"v_accvgpr_read_b32 v170, a242\n"
"v_mfma_f32_16x16x32_bf16 v[28:31], a[192:195], a[28:31], v[28:31]\n"
"v_accvgpr_read_b32 v171, a243\n"
"v_mfma_f32_16x16x32_bf16 v[44:47], a[196:199], a[28:31], v[44:47]\n"
"s_waitcnt vmcnt(12) lgkmcnt(0)\n"
"s_nop 0\n"
"v_mfma_f32_16x16x32_bf16 v[16:19], a[248:251], a[32:35], v[16:19]\n"
"s_mov_b32 s97, 0xac000\n"
"buffer_load_dwordx4 v[64:67], v7, s[28:31], s97 offen sc1\n"
"v_accvgpr_read_b32 v172, a244\n"
"v_mfma_f32_16x16x32_bf16 v[32:35], a[252:255], a[32:35], v[32:35]\n"
"v_mfma_f32_16x16x32_bf16 v[20:23], a[248:251], a[36:39], v[20:23]\n"
"v_accvgpr_read_b32 v173, a245\n"
"v_mfma_f32_16x16x32_bf16 v[36:39], a[252:255], a[36:39], v[36:39]\n"
"v_mfma_f32_16x16x32_bf16 v[24:27], a[248:251], a[40:43], v[24:27]\n"
"buffer_load_dwordx4 v[68:71], v7, s[28:31], s97 offen offset:1024 sc1\n"
"v_accvgpr_read_b32 v174, a246\n"
"v_mfma_f32_16x16x32_bf16 v[40:43], a[252:255], a[40:43], v[40:43]\n"
"v_mfma_f32_16x16x32_bf16 v[28:31], a[248:251], a[44:47], v[28:31]\n"
"v_accvgpr_read_b32 v175, a247\n"
"v_mfma_f32_16x16x32_bf16 v[44:47], a[252:255], a[44:47], v[44:47]\n"
"s_mov_b32 s9, 0\n"
"s_nop 0\n"
"v_add_u32 v208, 0x24000, v248\n"
"v_add_u32 v209, 0x24000, v249\n"
"v_add_u32 v210, 0x24000, v250\n"
"buffer_load_dwordx4 v[72:75], v7, s[28:31], s97 offen offset:2048 sc1\n"
"v_add_u32 v211, 0x24000, v251\n"
"v_pk_mul_f32 v[176:177], v[16:17], v[16:17]\n"
"v_pk_mul_f32 v[178:179], v[18:19], v[18:19]\n"
"v_pk_mul_f32 v[180:181], v[20:21], v[20:21]\n"
"v_pk_mul_f32 v[182:183], v[22:23], v[22:23]\n"
"v_pk_fma_f32 v[176:177], v[176:177], s[60:61], s[60:61] op_sel:[0,0,1] op_sel_hi:[1,0,1]\n"
"v_pk_fma_f32 v[178:179], v[178:179], s[60:61], s[60:61] op_sel:[0,0,1] op_sel_hi:[1,0,1]\n"
"v_pk_fma_f32 v[180:181], v[180:181], s[60:61], s[60:61] op_sel:[0,0,1] op_sel_hi:[1,0,1]\n"
"v_pk_fma_f32 v[182:183], v[182:183], s[60:61], s[60:61] op_sel:[0,0,1] op_sel_hi:[1,0,1]\n"
"v_pk_mul_f32 v[176:177], v[176:177], v[16:17]\n"
"v_pk_mul_f32 v[178:179], v[178:179], v[18:19]\n"
"v_pk_mul_f32 v[180:181], v[180:181], v[20:21]\n"
"v_pk_mul_f32 v[182:183], v[182:183], v[22:23]\n"
"v_exp_f32_e64 v176, v176\n"
"buffer_load_dwordx4 v[76:79], v7, s[28:31], s97 offen offset:3072 sc1\n"
"v_pk_mul_f32 v[184:185], v[24:25], v[24:25]\n"
"v_exp_f32 v177, v177\n"
"v_exp_f32 v178, v178\n"
"v_pk_mul_f32 v[186:187], v[26:27], v[26:27]\n"
"v_exp_f32 v179, v179\n"
"v_exp_f32 v180, v180\n"
"v_pk_mul_f32 v[188:189], v[28:29], v[28:29]\n"
"v_exp_f32 v181, v181\n"
"v_exp_f32 v182, v182\n"
"v_pk_mul_f32 v[190:191], v[30:31], v[30:31]\n"
"s_mov_b32 s96, 0xb0000\n"
"buffer_load_dwordx4 v[80:83], v7, s[28:31], s96 offen sc1\n"
"v_exp_f32_e64 v183, v183\n"
"v_pk_add_f32 v[176:177], v[176:177], 1.0 op_sel_hi:[1,0]\n"
"v_pk_add_f32 v[178:179], v[178:179], 1.0 op_sel_hi:[1,0]\n"
"v_pk_add_f32 v[180:181], v[180:181], 1.0 op_sel_hi:[1,0]\n"
"v_pk_add_f32 v[182:183], v[182:183], 1.0 op_sel_hi:[1,0]\n"
"v_rcp_f32_e64 v176, v176\n"
"v_pk_fma_f32 v[184:185], v[184:185], s[60:61], s[60:61] op_sel:[0,0,1] op_sel_hi:[1,0,1]\n"
"v_rcp_f32_e64 v177, v177\n"
"v_pk_fma_f32 v[186:187], v[186:187], s[60:61], s[60:61] op_sel:[0,0,1] op_sel_hi:[1,0,1]\n"
"v_rcp_f32_e64 v178, v178\n"
"buffer_load_dwordx4 v[84:87], v7, s[28:31], s96 offen offset:1024 sc1\n"
"v_pk_fma_f32 v[188:189], v[188:189], s[60:61], s[60:61] op_sel:[0,0,1] op_sel_hi:[1,0,1]\n"
"v_rcp_f32_e64 v179, v179\n"
"v_pk_fma_f32 v[190:191], v[190:191], s[60:61], s[60:61] op_sel:[0,0,1] op_sel_hi:[1,0,1]\n"
"v_rcp_f32_e64 v180, v180\n"
"v_pk_mul_f32 v[184:185], v[184:185], v[24:25]\n"
"v_rcp_f32_e64 v181, v181\n"
"v_pk_mul_f32 v[186:187], v[186:187], v[26:27]\n"
"v_rcp_f32_e64 v182, v182\n"
"v_pk_mul_f32 v[188:189], v[188:189], v[28:29]\n"
"v_rcp_f32_e64 v183, v183\n"
"v_pk_mul_f32 v[190:191], v[190:191], v[30:31]\n"
"buffer_load_dwordx4 v[88:91], v7, s[28:31], s96 offen offset:2048 sc1\n"
"v_pk_mul_f32 v[176:177], v[176:177], v[16:17]\n"
"v_pk_mul_f32 v[178:179], v[178:179], v[18:19]\n"
"v_pk_mul_f32 v[180:181], v[180:181], v[20:21]\n"
"v_pk_mul_f32 v[182:183], v[182:183], v[22:23]\n"
"v_cvt_pk_bf16_f32 v192, v176, v180\n"
"v_cvt_pk_bf16_f32 v194, v177, v181\n"
"v_cvt_pk_bf16_f32 v196, v178, v182\n"
"v_cvt_pk_bf16_f32 v198, v179, v183\n"
"v_exp_f32_e64 v184, v184\n"
"v_pk_mul_f32 v[176:177], v[32:33], v[32:33]\n"
"v_exp_f32 v185, v185\n"
"v_exp_f32 v186, v186\n"
"buffer_load_dwordx4 v[92:95], v7, s[28:31], s96 offen offset:3072 sc1\n"
"v_pk_mul_f32 v[178:179], v[34:35], v[34:35]\n"
"v_exp_f32 v187, v187\n"
"v_exp_f32 v188, v188\n"
"v_pk_mul_f32 v[180:181], v[36:37], v[36:37]\n"
"v_exp_f32 v189, v189\n"
"v_exp_f32 v190, v190\n"
"v_pk_mul_f32 v[182:183], v[38:39], v[38:39]\n"
"v_exp_f32_e64 v191, v191\n"
"v_pk_add_f32 v[184:185], v[184:185], 1.0 op_sel_hi:[1,0]\n"
"v_pk_add_f32 v[186:187], v[186:187], 1.0 op_sel_hi:[1,0]\n"
"v_pk_add_f32 v[188:189], v[188:189], 1.0 op_sel_hi:[1,0]\n"
"s_mov_b32 s97, 0xb4000\n"
"buffer_load_dwordx4 v[96:99], v7, s[28:31], s97 offen sc1\n"
"v_pk_add_f32 v[190:191], v[190:191], 1.0 op_sel_hi:[1,0]\n"
"v_rcp_f32_e64 v184, v184\n"
"v_pk_fma_f32 v[176:177], v[176:177], s[60:61], s[60:61] op_sel:[0,0,1] op_sel_hi:[1,0,1]\n"
"v_rcp_f32_e64 v185, v185\n"
"v_pk_fma_f32 v[178:179], v[178:179], s[60:61], s[60:61] op_sel:[0,0,1] op_sel_hi:[1,0,1]\n"
"v_rcp_f32_e64 v186, v186\n"
"v_pk_fma_f32 v[180:181], v[180:181], s[60:61], s[60:61] op_sel:[0,0,1] op_sel_hi:[1,0,1]\n"
"v_rcp_f32_e64 v187, v187\n"
"v_pk_fma_f32 v[182:183], v[182:183], s[60:61], s[60:61] op_sel:[0,0,1] op_sel_hi:[1,0,1]\n"
"v_rcp_f32_e64 v188, v188\n"
"buffer_load_dwordx4 v[100:103], v7, s[28:31], s97 offen offset:1024 sc1\n"
"v_pk_mul_f32 v[176:177], v[176:177], v[32:33]\n"
"v_rcp_f32_e64 v189, v189\n"
"v_pk_mul_f32 v[178:179], v[178:179], v[34:35]\n"
"v_rcp_f32_e64 v190, v190\n"
"v_pk_mul_f32 v[180:181], v[180:181], v[36:37]\n"
"v_rcp_f32_e64 v191, v191\n"
"v_pk_mul_f32 v[182:183], v[182:183], v[38:39]\n"
"v_pk_mul_f32 v[184:185], v[184:185], v[24:25]\n"
"v_pk_mul_f32 v[186:187], v[186:187], v[26:27]\n"
"v_pk_mul_f32 v[188:189], v[188:189], v[28:29]\n"
"v_pk_mul_f32 v[190:191], v[190:191], v[30:31]\n"
"v_cvt_pk_bf16_f32 v193, v184, v188\n"
"buffer_load_dwordx4 v[104:107], v7, s[28:31], s97 offen offset:2048 sc1\n"
"v_cvt_pk_bf16_f32 v195, v185, v189\n"
"v_cvt_pk_bf16_f32 v197, v186, v190\n"
"v_cvt_pk_bf16_f32 v199, v187, v191\n"
"ds_write_b64 v8, v[192:193]\n"
"v_exp_f32_e64 v176, v176\n"
"v_pk_mul_f32 v[184:185], v[40:41], v[40:41]\n"
"v_exp_f32 v177, v177\n"
"v_exp_f32 v178, v178\n"
"v_pk_mul_f32 v[186:187], v[42:43], v[42:43]\n"
"v_exp_f32_e64 v179, v179\n"
"buffer_load_dwordx4 v[108:111], v7, s[28:31], s97 offen offset:3072 sc1\n"
"v_exp_f32_e64 v180, v180\n"
"v_pk_mul_f32 v[188:189], v[44:45], v[44:45]\n"
"ds_write_b64 v9, v[194:195] offset:512\n"
"v_exp_f32 v181, v181\n"
"v_exp_f32 v182, v182\n"
"v_pk_mul_f32 v[190:191], v[46:47], v[46:47]\n"
"v_exp_f32_e64 v183, v183\n"
"v_pk_add_f32 v[176:177], v[176:177], 1.0 op_sel_hi:[1,0]\n"
"v_pk_add_f32 v[178:179], v[178:179], 1.0 op_sel_hi:[1,0]\n"
"s_mov_b32 s96, 0xb8000\n"
"buffer_load_dwordx4 v[112:115], v7, s[28:31], s96 offen sc1\n"
"v_pk_add_f32 v[180:181], v[180:181], 1.0 op_sel_hi:[1,0]\n"
"v_pk_add_f32 v[182:183], v[182:183], 1.0 op_sel_hi:[1,0]\n"
"ds_write_b64 v10, v[196:197] offset:1024\n"
"v_rcp_f32_e64 v176, v176\n"
"v_pk_fma_f32 v[184:185], v[184:185], s[60:61], s[60:61] op_sel:[0,0,1] op_sel_hi:[1,0,1]\n"
"v_rcp_f32_e64 v177, v177\n"
"v_pk_fma_f32 v[186:187], v[186:187], s[60:61], s[60:61] op_sel:[0,0,1] op_sel_hi:[1,0,1]\n"
"v_rcp_f32_e64 v178, v178\n"
"v_pk_fma_f32 v[188:189], v[188:189], s[60:61], s[60:61] op_sel:[0,0,1] op_sel_hi:[1,0,1]\n"
"v_rcp_f32_e64 v179, v179\n"
"buffer_load_dwordx4 v[116:119], v7, s[28:31], s96 offen offset:1024 sc1\n"
"v_pk_fma_f32 v[190:191], v[190:191], s[60:61], s[60:61] op_sel:[0,0,1] op_sel_hi:[1,0,1]\n"
"ds_write_b64 v11, v[198:199] offset:1536\n"
"v_rcp_f32_e64 v180, v180\n"
"v_pk_mul_f32 v[184:185], v[184:185], v[40:41]\n"
"v_rcp_f32_e64 v181, v181\n"
"v_pk_mul_f32 v[186:187], v[186:187], v[42:43]\n"
"v_rcp_f32_e64 v182, v182\n"
"v_pk_mul_f32 v[188:189], v[188:189], v[44:45]\n"
"v_rcp_f32_e64 v183, v183\n"
"buffer_load_dwordx4 v[120:123], v7, s[28:31], s96 offen offset:2048 sc1\n"
"v_pk_mul_f32 v[190:191], v[190:191], v[46:47]\n"
"v_pk_mul_f32 v[176:177], v[176:177], v[32:33]\n"
"v_pk_mul_f32 v[178:179], v[178:179], v[34:35]\n"
"v_pk_mul_f32 v[180:181], v[180:181], v[36:37]\n"
"v_pk_mul_f32 v[182:183], v[182:183], v[38:39]\n"
"v_cvt_pk_bf16_f32 v200, v176, v180\n"
"v_cvt_pk_bf16_f32 v202, v177, v181\n"
"v_cvt_pk_bf16_f32 v204, v178, v182\n"
"v_cvt_pk_bf16_f32 v206, v179, v183\n"
"v_exp_f32 v184, v184\n"
"v_exp_f32 v185, v185\n"
"v_exp_f32_e64 v186, v186\n"
"buffer_load_dwordx4 v[124:127], v7, s[28:31], s96 offen offset:3072 sc1\n"
"v_exp_f32 v187, v187\n"
"v_exp_f32 v188, v188\n"
"v_exp_f32 v189, v189\n"
"v_exp_f32 v190, v190\n"
"v_exp_f32_e64 v191, v191\n"
"v_pk_add_f32 v[184:185], v[184:185], 1.0 op_sel_hi:[1,0]\n"
"v_pk_add_f32 v[186:187], v[186:187], 1.0 op_sel_hi:[1,0]\n"
"v_pk_add_f32 v[188:189], v[188:189], 1.0 op_sel_hi:[1,0]\n"
"v_pk_add_f32 v[190:191], v[190:191], 1.0 op_sel_hi:[1,0]\n"
"v_rcp_f32_e64 v184, v184\n"
"s_mov_b32 s97, 0xbc000\n"
"buffer_load_dwordx4 v[128:131], v7, s[28:31], s97 offen sc1\n"
"v_rcp_f32 v185, v185\n"
"v_rcp_f32 v186, v186\n"
"v_rcp_f32 v187, v187\n"
"v_rcp_f32 v188, v188\n"
"v_rcp_f32 v189, v189\n"
"v_rcp_f32 v190, v190\n"
"v_rcp_f32_e64 v191, v191\n"
"v_pk_mul_f32 v[184:185], v[184:185], v[40:41]\n"
"buffer_load_dwordx4 v[132:135], v7, s[28:31], s97 offen offset:1024 sc1\n"
"v_pk_mul_f32 v[186:187], v[186:187], v[42:43]\n"
"v_pk_mul_f32 v[188:189], v[188:189], v[44:45]\n"
"v_pk_mul_f32 v[190:191], v[190:191], v[46:47]\n"
"v_cvt_pk_bf16_f32 v201, v184, v188\n"
"ds_write_b64 v8, v[200:201] offset:8192\n"
"v_cvt_pk_bf16_f32 v203, v185, v189\n"
"ds_write_b64 v9, v[202:203] offset:8704\n"
"v_cvt_pk_bf16_f32 v205, v186, v190\n"
"ds_write_b64 v10, v[204:205] offset:9216\n"
"buffer_load_dwordx4 v[136:139], v7, s[28:31], s97 offen offset:2048 sc1\n"
"v_cvt_pk_bf16_f32 v207, v187, v191\n"
"ds_write_b64 v11, v[206:207] offset:9728\n"
"s_waitcnt lgkmcnt(0)\n"
"s_barrier\n"
"ds_read_b128 a[64:67], v208\n"
"ds_read_b128 a[68:71], v208 offset:8192\n"
"buffer_load_dwordx4 v[140:143], v7, s[28:31], s97 offen offset:3072 sc1\n"
"ds_read_b128 a[72:75], v209\n"
"ds_read_b128 a[76:79], v209 offset:8192\n"
"ds_read_b128 a[80:83], v210\n"
"ds_read_b128 a[84:87], v210 offset:8192\n"
"ds_read_b128 a[88:91], v211\n"
"ds_read_b128 a[92:95], v211 offset:8192\n"
"ds_read_b128 a[96:99], v208 offset:256\n"
"ds_read_b128 a[100:103], v208 offset:8448\n"
"ds_read_b128 a[104:107], v209 offset:256\n"
"ds_read_b128 a[108:111], v209 offset:8448\n"
"ds_read_b128 a[112:115], v210 offset:256\n"
"ds_read_b128 a[116:119], v210 offset:8448\n"
"ds_read_b128 a[120:123], v211 offset:256\n"
"ds_read_b128 a[124:127], v211 offset:8448\n"
"s_lshl_b32 s58, s56, 13\n"
"s_add_u32 m0, s95, 0xc000\n"
"s_mov_b32 s90, 0xcc000\n"
"buffer_load_dwordx4 v7, s[28:31], s90 offen sc1 lds\n"
"v_add_u32 v248, s58, v248\n"
"v_add_u32 v249, s58, v249\n"
"v_add_u32 v250, s58, v250\n"
"v_add_u32_e64 v251, s58, v251\n"
"v_add_u32 v8, 0x20000, v244\n"
"v_add_u32 v9, 0x20000, v245\n"
"v_add_u32 v10, 0x20000, v246\n"
"v_add_u32 v11, 0x20000, v247\n"
"v_add_u32 v12, 0x20000, v248\n"
"v_add_u32 v13, 0x20000, v249\n"
"v_add_u32 v14, 0x20000, v250\n"
"v_add_u32 v15, 0x20000, v251\n"
"s_mov_b32 s60, 0x4038aa3b\n"
"buffer_load_dwordx4 v7, s[28:31], s90 offen offset:1024 sc1 lds\n"
"s_mov_b32 s61, 0xbf800000\n"
"s_waitcnt vmcnt(30) lgkmcnt(12)\n"
"v_mfma_f32_16x16x32_bf16 v[208:211], a[64:67], a[48:51], 0\n"
"v_mfma_f32_16x16x32_bf16 v[224:227], a[68:71], a[48:51], 0\n"
"v_mfma_f32_16x16x32_bf16 v[212:215], a[64:67], a[52:55], 0\n"
"v_mfma_f32_16x16x32_bf16 v[228:231], a[68:71], a[52:55], 0\n"
"v_mfma_f32_16x16x32_bf16 v[216:219], a[64:67], a[56:59], 0\n"
"buffer_load_dwordx4 v7, s[28:31], s90 offen offset:2048 sc1 lds\n"
"v_mfma_f32_16x16x32_bf16 v[232:235], a[68:71], a[56:59], 0\n"
"v_mfma_f32_16x16x32_bf16 v[220:223], a[64:67], a[60:63], 0\n"
"v_mfma_f32_16x16x32_bf16 v[236:239], a[68:71], a[60:63], 0\n"
"s_waitcnt vmcnt(27)\n"
"s_nop 0\n"
"v_mfma_f32_16x16x32_bf16 v[208:211], a[72:75], a[200:203], v[208:211]\n"
"buffer_load_dwordx4 v7, s[28:31], s90 offen offset:3072 sc1 lds\n"
"v_mfma_f32_16x16x32_bf16 v[224:227], a[76:79], a[200:203], v[224:227]\n"
"v_mfma_f32_16x16x32_bf16 v[212:215], a[72:75], a[204:207], v[212:215]\n"
"v_mfma_f32_16x16x32_bf16 v[228:231], a[76:79], a[204:207], v[228:231]\n"
"v_mfma_f32_16x16x32_bf16 v[216:219], a[72:75], a[208:211], v[216:219]\n"
"v_mfma_f32_16x16x32_bf16 v[232:235], a[76:79], a[208:211], v[232:235]\n"
"s_add_u32 m0, s95, 0x10000\n"
"s_mov_b32 s90, 0xd0000\n"
"buffer_load_dwordx4 v7, s[28:31], s90 offen sc1 lds\n"
"v_mfma_f32_16x16x32_bf16 v[220:223], a[72:75], a[212:215], v[220:223]\n"
"v_mfma_f32_16x16x32_bf16 v[236:239], a[76:79], a[212:215], v[236:239]\n"
"s_waitcnt vmcnt(25) lgkmcnt(8)\n"
"s_nop 0\n"
"v_mfma_f32_16x16x32_bf16 v[208:211], a[80:83], v[48:51], v[208:211]\n"
"v_mfma_f32_16x16x32_bf16 v[224:227], a[84:87], v[48:51], v[224:227]\n"
"buffer_load_dwordx4 v7, s[28:31], s90 offen offset:1024 sc1 lds\n"
"v_mfma_f32_16x16x32_bf16 v[212:215], a[80:83], v[52:55], v[212:215]\n"
"v_mfma_f32_16x16x32_bf16 v[228:231], a[84:87], v[52:55], v[228:231]\n"
"v_mfma_f32_16x16x32_bf16 v[216:219], a[80:83], v[56:59], v[216:219]\n"
"v_mfma_f32_16x16x32_bf16 v[232:235], a[84:87], v[56:59], v[232:235]\n"
"buffer_load_dwordx4 v7, s[28:31], s90 offen offset:2048 sc1 lds\n"
"v_mfma_f32_16x16x32_bf16 v[220:223], a[80:83], v[60:63], v[220:223]\n"
"v_mfma_f32_16x16x32_bf16 v[236:239], a[84:87], v[60:63], v[236:239]\n"
"s_waitcnt vmcnt(23)\n"
"v_mfma_f32_16x16x32_bf16 v[208:211], a[88:91], v[64:67], v[208:211]\n"
"s_waitcnt lgkmcnt(0)\n"
"s_barrier\n"
"s_nop 0\n"
"buffer_load_dwordx4 v7, s[28:31], s90 offen offset:3072 sc1 lds\n"
"v_mfma_f32_16x16x32_bf16 v[224:227], a[92:95], v[64:67], v[224:227]\n"
"v_mfma_f32_16x16x32_bf16 v[212:215], a[88:91], v[68:71], v[212:215]\n"
"v_mfma_f32_16x16x32_bf16 v[228:231], a[92:95], v[68:71], v[228:231]\n"
"v_mfma_f32_16x16x32_bf16 v[216:219], a[88:91], v[72:75], v[216:219]\n"
"s_add_u32 m0, s95, 0x14000\n"
"s_mov_b32 s90, 0xd4000\n"
"buffer_load_dwordx4 v7, s[28:31], s90 offen sc1 lds\n"
"v_mfma_f32_16x16x32_bf16 v[232:235], a[92:95], v[72:75], v[232:235]\n"
"v_mfma_f32_16x16x32_bf16 v[220:223], a[88:91], v[76:79], v[220:223]\n"
"v_mfma_f32_16x16x32_bf16 v[236:239], a[92:95], v[76:79], v[236:239]\n"
"s_waitcnt vmcnt(21)\n"
"s_nop 0\n"
"v_mfma_f32_16x16x32_bf16 v[208:211], a[96:99], v[80:83], v[208:211]\n"
"buffer_load_dwordx4 v7, s[28:31], s90 offen offset:1024 sc1 lds\n"
"v_mfma_f32_16x16x32_bf16 v[224:227], a[100:103], v[80:83], v[224:227]\n"
"v_mfma_f32_16x16x32_bf16 v[212:215], a[96:99], v[84:87], v[212:215]\n"
"v_mfma_f32_16x16x32_bf16 v[228:231], a[100:103], v[84:87], v[228:231]\n"
"v_mfma_f32_16x16x32_bf16 v[216:219], a[96:99], v[88:91], v[216:219]\n"
"v_mfma_f32_16x16x32_bf16 v[232:235], a[100:103], v[88:91], v[232:235]\n"
"buffer_load_dwordx4 v7, s[28:31], s90 offen offset:2048 sc1 lds\n"
"v_mfma_f32_16x16x32_bf16 v[220:223], a[96:99], v[92:95], v[220:223]\n"
"v_mfma_f32_16x16x32_bf16 v[236:239], a[100:103], v[92:95], v[236:239]\n"
"s_waitcnt vmcnt(19)\n"
"s_nop 0\n"
"v_mfma_f32_16x16x32_bf16 v[208:211], a[104:107], v[96:99], v[208:211]\n"
"v_mfma_f32_16x16x32_bf16 v[224:227], a[108:111], v[96:99], v[224:227]\n"
"buffer_load_dwordx4 v7, s[28:31], s90 offen offset:3072 sc1 lds\n"
"v_mfma_f32_16x16x32_bf16 v[212:215], a[104:107], v[100:103], v[212:215]\n"
"v_mfma_f32_16x16x32_bf16 v[228:231], a[108:111], v[100:103], v[228:231]\n"
"v_mfma_f32_16x16x32_bf16 v[216:219], a[104:107], v[104:107], v[216:219]\n"
"v_mfma_f32_16x16x32_bf16 v[232:235], a[108:111], v[104:107], v[232:235]\n"
"v_mfma_f32_16x16x32_bf16 v[220:223], a[104:107], v[108:111], v[220:223]\n"
"s_add_u32 m0, s95, 0x18000\n"
"s_mov_b32 s90, 0xd8000\n"
"buffer_load_dwordx4 v7, s[28:31], s90 offen sc1 lds\n"
"v_mfma_f32_16x16x32_bf16 v[236:239], a[108:111], v[108:111], v[236:239]\n"
"s_waitcnt vmcnt(17)\n"
"s_nop 0\n"
"v_mfma_f32_16x16x32_bf16 v[208:211], a[112:115], v[112:115], v[208:211]\n"
"v_mfma_f32_16x16x32_bf16 v[224:227], a[116:119], v[112:115], v[224:227]\n"
"v_mfma_f32_16x16x32_bf16 v[212:215], a[112:115], v[116:119], v[212:215]\n"
"buffer_load_dwordx4 v7, s[28:31], s90 offen offset:1024 sc1 lds\n"
"v_mfma_f32_16x16x32_bf16 v[228:231], a[116:119], v[116:119], v[228:231]\n"
"v_mfma_f32_16x16x32_bf16 v[216:219], a[112:115], v[120:123], v[216:219]\n"
"v_mfma_f32_16x16x32_bf16 v[232:235], a[116:119], v[120:123], v[232:235]\n"
"v_mfma_f32_16x16x32_bf16 v[220:223], a[112:115], v[124:127], v[220:223]\n"
"buffer_load_dwordx4 v7, s[28:31], s90 offen offset:2048 sc1 lds\n"
"v_mfma_f32_16x16x32_bf16 v[236:239], a[116:119], v[124:127], v[236:239]\n"
"s_waitcnt vmcnt(15)\n"
"s_nop 0\n"
"v_mfma_f32_16x16x32_bf16 v[208:211], a[120:123], v[128:131], v[208:211]\n"
"v_mfma_f32_16x16x32_bf16 v[224:227], a[124:127], v[128:131], v[224:227]\n"
"v_mfma_f32_16x16x32_bf16 v[212:215], a[120:123], v[132:135], v[212:215]\n"
"v_mfma_f32_16x16x32_bf16 v[228:231], a[124:127], v[132:135], v[228:231]\n"
"buffer_load_dwordx4 v7, s[28:31], s90 offen offset:3072 sc1 lds\n"
"v_mfma_f32_16x16x32_bf16 v[216:219], a[120:123], v[136:139], v[216:219]\n"
"v_mfma_f32_16x16x32_bf16 v[232:235], a[124:127], v[136:139], v[232:235]\n"
"v_mfma_f32_16x16x32_bf16 v[220:223], a[120:123], v[140:143], v[220:223]\n"
"v_mfma_f32_16x16x32_bf16 v[236:239], a[124:127], v[140:143], v[236:239]\n"
"s_add_u32 m0, s95, 0x1c000\n"
"s_mov_b32 s90, 0xdc000\n"
"buffer_load_dwordx4 v7, s[28:31], s90 offen sc1 lds\n"
"v_pk_fma_f32 v[208:209], v[208:209], s[60:61], s[60:61] op_sel:[0,0,1] op_sel_hi:[1,0,1]\n"
"v_pk_fma_f32 v[210:211], v[210:211], s[60:61], s[60:61] op_sel:[0,0,1] op_sel_hi:[1,0,1]\n"
"v_pk_fma_f32 v[212:213], v[212:213], s[60:61], s[60:61] op_sel:[0,0,1] op_sel_hi:[1,0,1]\n"
"v_pk_fma_f32 v[214:215], v[214:215], s[60:61], s[60:61] op_sel:[0,0,1] op_sel_hi:[1,0,1]\n"
"v_pk_fma_f32 v[216:217], v[216:217], s[60:61], s[60:61] op_sel:[0,0,1] op_sel_hi:[1,0,1]\n"
"v_pk_fma_f32 v[218:219], v[218:219], s[60:61], s[60:61] op_sel:[0,0,1] op_sel_hi:[1,0,1]\n"
"v_pk_fma_f32 v[220:221], v[220:221], s[60:61], s[60:61] op_sel:[0,0,1] op_sel_hi:[1,0,1]\n"
"v_pk_fma_f32 v[222:223], v[222:223], s[60:61], s[60:61] op_sel:[0,0,1] op_sel_hi:[1,0,1]\n"
"v_pk_fma_f32 v[224:225], v[224:225], s[60:61], s[60:61] op_sel:[0,0,1] op_sel_hi:[1,0,1]\n"
"v_pk_fma_f32 v[226:227], v[226:227], s[60:61], s[60:61] op_sel:[0,0,1] op_sel_hi:[1,0,1]\n"
"v_pk_fma_f32 v[228:229], v[228:229], s[60:61], s[60:61] op_sel:[0,0,1] op_sel_hi:[1,0,1]\n"
"v_pk_fma_f32 v[230:231], v[230:231], s[60:61], s[60:61] op_sel:[0,0,1] op_sel_hi:[1,0,1]\n"
"v_pk_fma_f32 v[232:233], v[232:233], s[60:61], s[60:61] op_sel:[0,0,1] op_sel_hi:[1,0,1]\n"
"buffer_load_dwordx4 v7, s[28:31], s90 offen offset:1024 sc1 lds\n"
"v_pk_fma_f32 v[234:235], v[234:235], s[60:61], s[60:61] op_sel:[0,0,1] op_sel_hi:[1,0,1]\n"
"v_pk_fma_f32 v[236:237], v[236:237], s[60:61], s[60:61] op_sel:[0,0,1] op_sel_hi:[1,0,1]\n"
"v_pk_fma_f32 v[238:239], v[238:239], s[60:61], s[60:61] op_sel:[0,0,1] op_sel_hi:[1,0,1]\n"
"v_max3_f32 v254, |v208|, |v209|, |v209|\n"
"v_max3_f32 v254, |v210|, |v211|, v254\n"
"v_max3_f32 v254, |v212|, |v213|, v254\n"
"v_max3_f32 v254, |v214|, |v215|, v254\n"
"v_max3_f32 v254, |v216|, |v217|, v254\n"
"v_max3_f32 v254, |v218|, |v219|, v254\n"
"v_max3_f32 v254, |v220|, |v221|, v254\n"
"v_max3_f32 v254, |v222|, |v223|, v254\n"
"v_max3_f32 v254, |v224|, |v225|, v254\n"
"v_max3_f32 v254, |v226|, |v227|, v254\n"
"v_max3_f32 v254, |v228|, |v229|, v254\n"
"v_max3_f32 v254, |v230|, |v231|, v254\n"
"buffer_load_dwordx4 v7, s[28:31], s90 offen offset:2048 sc1 lds\n"
"v_max3_f32 v254, |v232|, |v233|, v254\n"
"v_max3_f32 v254, |v234|, |v235|, v254\n"
"v_max3_f32 v254, |v236|, |v237|, v254\n"
"v_max3_f32 v254, |v238|, |v239|, v254\n"
"v_cmp_lt_f32 vcc, 0x42f00000, v254\n"
"s_nop 0\n"
"s_or_b32 s59, vcc_lo, vcc_hi\n"
"s_or_b32 s98, s98, s59\n"
"s_cbranch_scc1 .Lslow_path\n"
"v_exp_f32 v208, v208\n"
"v_exp_f32 v212, v212\n"
"v_exp_f32 v216, v216\n"
"v_exp_f32 v220, v220\n"
"buffer_load_dwordx4 v7, s[28:31], s90 offen offset:3072 sc1 lds\n"
"v_exp_f32 v224, v224\n"
"v_exp_f32 v228, v228\n"
"v_exp_f32 v232, v232\n"
"v_exp_f32 v236, v236\n"
"v_pk_fma_f32 v[16:17], v[144:145], v[208:209], -0.5 op_sel:[0,0,0] op_sel_hi:[1,0,0] neg_lo:[0,1,0] neg_hi:[0,1,0]\n"
"v_pk_fma_f32 v[18:19], v[146:147], v[208:209], -0.5 op_sel:[0,0,0] op_sel_hi:[1,0,0] neg_lo:[0,1,0] neg_hi:[0,1,0]\n"
"v_pk_fma_f32 v[20:21], v[152:153], v[212:213], -0.5 op_sel:[0,0,0] op_sel_hi:[1,0,0] neg_lo:[0,1,0] neg_hi:[0,1,0]\n"
"v_pk_fma_f32 v[22:23], v[154:155], v[212:213], -0.5 op_sel:[0,0,0] op_sel_hi:[1,0,0] neg_lo:[0,1,0] neg_hi:[0,1,0]\n"
"v_pk_fma_f32 v[24:25], v[160:161], v[216:217], -0.5 op_sel:[0,0,0] op_sel_hi:[1,0,0] neg_lo:[0,1,0] neg_hi:[0,1,0]\n"
"v_pk_fma_f32 v[26:27], v[162:163], v[216:217], -0.5 op_sel:[0,0,0] op_sel_hi:[1,0,0] neg_lo:[0,1,0] neg_hi:[0,1,0]\n"
"s_add_u32 m0, s95, 0\n"
"s_nop 0\n"
"s_mov_b32 s90, 0xc0000\n"
"buffer_load_dwordx4 v7, s[28:31], s90 offen sc1 lds\n"
"v_pk_fma_f32 v[28:29], v[168:169], v[220:221], -0.5 op_sel:[0,0,0] op_sel_hi:[1,0,0] neg_lo:[0,1,0] neg_hi:[0,1,0]\n"
"v_pk_fma_f32 v[30:31], v[170:171], v[220:221], -0.5 op_sel:[0,0,0] op_sel_hi:[1,0,0] neg_lo:[0,1,0] neg_hi:[0,1,0]\n"
"v_pk_fma_f32 v[32:33], v[148:149], v[224:225], -0.5 op_sel:[0,0,0] op_sel_hi:[1,0,0] neg_lo:[0,1,0] neg_hi:[0,1,0]\n"
"v_pk_fma_f32 v[34:35], v[150:151], v[224:225], -0.5 op_sel:[0,0,0] op_sel_hi:[1,0,0] neg_lo:[0,1,0] neg_hi:[0,1,0]\n"
"v_pk_fma_f32 v[36:37], v[156:157], v[228:229], -0.5 op_sel:[0,0,0] op_sel_hi:[1,0,0] neg_lo:[0,1,0] neg_hi:[0,1,0]\n"
"v_pk_fma_f32 v[38:39], v[158:159], v[228:229], -0.5 op_sel:[0,0,0] op_sel_hi:[1,0,0] neg_lo:[0,1,0] neg_hi:[0,1,0]\n"
"v_pk_fma_f32 v[40:41], v[164:165], v[232:233], -0.5 op_sel:[0,0,0] op_sel_hi:[1,0,0] neg_lo:[0,1,0] neg_hi:[0,1,0]\n"
"v_pk_fma_f32 v[42:43], v[166:167], v[232:233], -0.5 op_sel:[0,0,0] op_sel_hi:[1,0,0] neg_lo:[0,1,0] neg_hi:[0,1,0]\n"
"v_pk_fma_f32 v[44:45], v[172:173], v[236:237], -0.5 op_sel:[0,0,0] op_sel_hi:[1,0,0] neg_lo:[0,1,0] neg_hi:[0,1,0]\n"
"v_pk_fma_f32 v[46:47], v[174:175], v[236:237], -0.5 op_sel:[0,0,0] op_sel_hi:[1,0,0] neg_lo:[0,1,0] neg_hi:[0,1,0]\n"
"v_rcp_f32 v16, v16\n"
"v_rcp_f32 v20, v20\n"
"buffer_load_dwordx4 v7, s[28:31], s90 offen offset:1024 sc1 lds\n"
"v_rcp_f32 v24, v24\n"
"v_rcp_f32 v28, v28\n"
"v_rcp_f32 v17, v17\n"
"v_rcp_f32 v21, v21\n"
"v_rcp_f32 v25, v25\n"
"v_rcp_f32 v29, v29\n"
"v_rcp_f32 v18, v18\n"
"v_rcp_f32 v22, v22\n"
"buffer_load_dwordx4 v7, s[28:31], s90 offen offset:2048 sc1 lds\n"
"v_rcp_f32 v26, v26\n"
"v_rcp_f32 v30, v30\n"
"v_rcp_f32 v19, v19\n"
"v_rcp_f32 v23, v23\n"
"v_rcp_f32 v27, v27\n"
"v_rcp_f32 v31, v31\n"
"v_rcp_f32 v32, v32\n"
"v_rcp_f32 v36, v36\n"
"buffer_load_dwordx4 v7, s[28:31], s90 offen offset:3072 sc1 lds\n"
"v_rcp_f32 v40, v40\n"
"v_rcp_f32 v44, v44\n"
"v_rcp_f32 v33, v33\n"
"v_rcp_f32 v37, v37\n"
"v_rcp_f32 v41, v41\n"
"v_rcp_f32 v45, v45\n"
"v_rcp_f32 v34, v34\n"
"v_rcp_f32 v38, v38\n"
"s_add_u32 m0, s95, 0x4000\n"
"s_mov_b32 s90, 0xc4000\n"
"buffer_load_dwordx4 v7, s[28:31], s90 offen sc1 lds\n"
"v_rcp_f32 v42, v42\n"
"v_rcp_f32 v46, v46\n"
"v_rcp_f32 v35, v35\n"
"v_rcp_f32 v39, v39\n"
"v_rcp_f32 v43, v43\n"
"v_rcp_f32 v47, v47\n"
"v_pk_add_f32 v[16:17], v[16:17], 1.0 op_sel_hi:[1,0]\n"
"v_pk_add_f32 v[18:19], v[18:19], 1.0 op_sel_hi:[1,0]\n"
"v_pk_add_f32 v[20:21], v[20:21], 1.0 op_sel_hi:[1,0]\n"
"buffer_load_dwordx4 v7, s[28:31], s90 offen offset:1024 sc1 lds\n"
"v_pk_add_f32 v[22:23], v[22:23], 1.0 op_sel_hi:[1,0]\n"
"v_pk_add_f32 v[24:25], v[24:25], 1.0 op_sel_hi:[1,0]\n"
"v_pk_add_f32 v[26:27], v[26:27], 1.0 op_sel_hi:[1,0]\n"
"v_pk_add_f32 v[28:29], v[28:29], 1.0 op_sel_hi:[1,0]\n"
"v_pk_add_f32 v[30:31], v[30:31], 1.0 op_sel_hi:[1,0]\n"
"v_pk_add_f32 v[32:33], v[32:33], 1.0 op_sel_hi:[1,0]\n"
"v_pk_add_f32 v[34:35], v[34:35], 1.0 op_sel_hi:[1,0]\n"
"v_pk_add_f32 v[36:37], v[36:37], 1.0 op_sel_hi:[1,0]\n"
"v_pk_add_f32 v[38:39], v[38:39], 1.0 op_sel_hi:[1,0]\n"
"v_pk_add_f32 v[40:41], v[40:41], 1.0 op_sel_hi:[1,0]\n"
"v_pk_add_f32 v[42:43], v[42:43], 1.0 op_sel_hi:[1,0]\n"
"v_pk_add_f32 v[44:45], v[44:45], 1.0 op_sel_hi:[1,0]\n"
"v_pk_add_f32 v[46:47], v[46:47], 1.0 op_sel_hi:[1,0]\n"
"v_cvt_pk_bf16_f32 v176, v16, v20\n"
"buffer_load_dwordx4 v7, s[28:31], s90 offen offset:2048 sc1 lds\n"
"v_cvt_pk_bf16_f32 v177, v24, v28\n"
"ds_write_b64 v8, v[176:177]\n"
"v_exp_f32_e64 v209, v209\n"
"v_cvt_pk_bf16_f32 v178, v17, v21\n"
"v_cvt_pk_bf16_f32 v179, v25, v29\n"
"ds_write_b64 v9, v[178:179] offset:2048\n"
"v_exp_f32_e64 v213, v213\n"
"v_cvt_pk_bf16_f32 v180, v18, v22\n"
"v_cvt_pk_bf16_f32 v181, v26, v30\n"
"buffer_load_dwordx4 v7, s[28:31], s90 offen offset:3072 sc1 lds\n"
"ds_write_b64 v10, v[180:181] offset:4096\n"
"v_exp_f32_e64 v217, v217\n"
"v_cvt_pk_bf16_f32 v182, v19, v23\n"
"v_cvt_pk_bf16_f32 v183, v27, v31\n"
"ds_write_b64 v11, v[182:183] offset:6144\n"
"v_exp_f32_e64 v221, v221\n"
"v_cvt_pk_bf16_f32 v184, v32, v36\n"
"v_cvt_pk_bf16_f32 v185, v40, v44\n"
"ds_write_b64 v8, v[184:185] offset:8192\n"
"s_add_u32 m0, s95, 0x8000\n"
"s_mov_b32 s90, 0xc8000\n"
"buffer_load_dwordx4 v7, s[28:31], s90 offen sc1 lds\n"
"v_exp_f32_e64 v225, v225\n"
"v_cvt_pk_bf16_f32 v186, v33, v37\n"
"v_cvt_pk_bf16_f32 v187, v41, v45\n"
"ds_write_b64 v9, v[186:187] offset:10240\n"
"v_exp_f32_e64 v229, v229\n"
"v_cvt_pk_bf16_f32 v188, v34, v38\n"
"v_cvt_pk_bf16_f32 v189, v42, v46\n"
"ds_write_b64 v10, v[188:189] offset:12288\n"
"buffer_load_dwordx4 v7, s[28:31], s90 offen offset:1024 sc1 lds\n"
"v_exp_f32_e64 v233, v233\n"
"v_cvt_pk_bf16_f32 v190, v35, v39\n"
"v_cvt_pk_bf16_f32 v191, v43, v47\n"
"ds_write_b64 v11, v[190:191] offset:14336\n"
"v_exp_f32_e64 v237, v237\n"
"v_pk_fma_f32 v[16:17], v[144:145], v[208:209], -0.5 op_sel:[0,1,0] op_sel_hi:[1,1,0] neg_lo:[0,1,0] neg_hi:[0,1,0]\n"
"v_pk_fma_f32 v[18:19], v[146:147], v[208:209], -0.5 op_sel:[0,1,0] op_sel_hi:[1,1,0] neg_lo:[0,1,0] neg_hi:[0,1,0]\n"
"v_pk_fma_f32 v[20:21], v[152:153], v[212:213], -0.5 op_sel:[0,1,0] op_sel_hi:[1,1,0] neg_lo:[0,1,0] neg_hi:[0,1,0]\n"
"v_pk_fma_f32 v[22:23], v[154:155], v[212:213], -0.5 op_sel:[0,1,0] op_sel_hi:[1,1,0] neg_lo:[0,1,0] neg_hi:[0,1,0]\n"
"v_pk_fma_f32 v[24:25], v[160:161], v[216:217], -0.5 op_sel:[0,1,0] op_sel_hi:[1,1,0] neg_lo:[0,1,0] neg_hi:[0,1,0]\n"
"buffer_load_dwordx4 v7, s[28:31], s90 offen offset:2048 sc1 lds\n"
"v_pk_fma_f32 v[26:27], v[162:163], v[216:217], -0.5 op_sel:[0,1,0] op_sel_hi:[1,1,0] neg_lo:[0,1,0] neg_hi:[0,1,0]\n"
"v_pk_fma_f32 v[28:29], v[168:169], v[220:221], -0.5 op_sel:[0,1,0] op_sel_hi:[1,1,0] neg_lo:[0,1,0] neg_hi:[0,1,0]\n"
"v_pk_fma_f32 v[30:31], v[170:171], v[220:221], -0.5 op_sel:[0,1,0] op_sel_hi:[1,1,0] neg_lo:[0,1,0] neg_hi:[0,1,0]\n"
"v_pk_fma_f32 v[32:33], v[148:149], v[224:225], -0.5 op_sel:[0,1,0] op_sel_hi:[1,1,0] neg_lo:[0,1,0] neg_hi:[0,1,0]\n"
"v_pk_fma_f32 v[34:35], v[150:151], v[224:225], -0.5 op_sel:[0,1,0] op_sel_hi:[1,1,0] neg_lo:[0,1,0] neg_hi:[0,1,0]\n"
"v_pk_fma_f32 v[36:37], v[156:157], v[228:229], -0.5 op_sel:[0,1,0] op_sel_hi:[1,1,0] neg_lo:[0,1,0] neg_hi:[0,1,0]\n"
"v_pk_fma_f32 v[38:39], v[158:159], v[228:229], -0.5 op_sel:[0,1,0] op_sel_hi:[1,1,0] neg_lo:[0,1,0] neg_hi:[0,1,0]\n"
"v_pk_fma_f32 v[40:41], v[164:165], v[232:233], -0.5 op_sel:[0,1,0] op_sel_hi:[1,1,0] neg_lo:[0,1,0] neg_hi:[0,1,0]\n"
"v_pk_fma_f32 v[42:43], v[166:167], v[232:233], -0.5 op_sel:[0,1,0] op_sel_hi:[1,1,0] neg_lo:[0,1,0] neg_hi:[0,1,0]\n"
"v_pk_fma_f32 v[44:45], v[172:173], v[236:237], -0.5 op_sel:[0,1,0] op_sel_hi:[1,1,0] neg_lo:[0,1,0] neg_hi:[0,1,0]\n"
"v_pk_fma_f32 v[46:47], v[174:175], v[236:237], -0.5 op_sel:[0,1,0] op_sel_hi:[1,1,0] neg_lo:[0,1,0] neg_hi:[0,1,0]\n"
"v_rcp_f32 v16, v16\n"
"v_rcp_f32 v20, v20\n"
"buffer_load_dwordx4 v7, s[28:31], s90 offen offset:3072 sc1 lds\n"
"v_rcp_f32 v24, v24\n"
"v_rcp_f32 v28, v28\n"
"v_rcp_f32 v17, v17\n"
"v_rcp_f32 v21, v21\n"
"v_rcp_f32 v25, v25\n"
"v_rcp_f32 v29, v29\n"
"v_rcp_f32 v18, v18\n"
"v_rcp_f32 v22, v22\n"
"v_rcp_f32 v26, v26\n"
"v_rcp_f32 v30, v30\n"
"v_rcp_f32 v19, v19\n"
"v_rcp_f32 v23, v23\n"
"v_rcp_f32 v27, v27\n"
"v_rcp_f32 v31, v31\n"
"v_rcp_f32 v32, v32\n"
"v_rcp_f32 v36, v36\n"
"v_rcp_f32 v40, v40\n"
"v_rcp_f32 v44, v44\n"
"v_rcp_f32 v33, v33\n"
"v_rcp_f32 v37, v37\n"
"v_rcp_f32 v41, v41\n"
"v_rcp_f32 v45, v45\n"
"v_rcp_f32 v34, v34\n"
"v_rcp_f32 v38, v38\n"
"v_rcp_f32 v42, v42\n"
"v_rcp_f32 v46, v46\n"
"v_rcp_f32 v35, v35\n"
"v_rcp_f32 v39, v39\n"
"v_rcp_f32 v43, v43\n"
"v_rcp_f32 v47, v47\n"
"v_pk_add_f32 v[16:17], v[16:17], 1.0 op_sel_hi:[1,0]\n"
"v_pk_add_f32 v[18:19], v[18:19], 1.0 op_sel_hi:[1,0]\n"
"v_pk_add_f32 v[20:21], v[20:21], 1.0 op_sel_hi:[1,0]\n"
"v_pk_add_f32 v[22:23], v[22:23], 1.0 op_sel_hi:[1,0]\n"
"v_pk_add_f32 v[24:25], v[24:25], 1.0 op_sel_hi:[1,0]\n"
"v_pk_add_f32 v[26:27], v[26:27], 1.0 op_sel_hi:[1,0]\n"
"v_pk_add_f32 v[28:29], v[28:29], 1.0 op_sel_hi:[1,0]\n"
"v_pk_add_f32 v[30:31], v[30:31], 1.0 op_sel_hi:[1,0]\n"
"v_pk_add_f32 v[32:33], v[32:33], 1.0 op_sel_hi:[1,0]\n"
"v_pk_add_f32 v[34:35], v[34:35], 1.0 op_sel_hi:[1,0]\n"
"v_pk_add_f32 v[36:37], v[36:37], 1.0 op_sel_hi:[1,0]\n"
"v_pk_add_f32 v[38:39], v[38:39], 1.0 op_sel_hi:[1,0]\n"
"v_pk_add_f32 v[40:41], v[40:41], 1.0 op_sel_hi:[1,0]\n"
"v_pk_add_f32 v[42:43], v[42:43], 1.0 op_sel_hi:[1,0]\n"
"v_pk_add_f32 v[44:45], v[44:45], 1.0 op_sel_hi:[1,0]\n"
"v_pk_add_f32 v[46:47], v[46:47], 1.0 op_sel_hi:[1,0]\n"
"v_cvt_pk_bf16_f32 v176, v16, v20\n"
"v_cvt_pk_bf16_f32 v177, v24, v28\n"
"ds_write_b64 v8, v[176:177] offset:16384\n"
"v_exp_f32_e64 v210, v210\n"
"v_cvt_pk_bf16_f32 v178, v17, v21\n"
"v_cvt_pk_bf16_f32 v179, v25, v29\n"
"ds_write_b64 v9, v[178:179] offset:18432\n"
"v_exp_f32_e64 v214, v214\n"
"v_cvt_pk_bf16_f32 v180, v18, v22\n"
"v_cvt_pk_bf16_f32 v181, v26, v30\n"
"ds_write_b64 v10, v[180:181] offset:20480\n"
"v_exp_f32_e64 v218, v218\n"
"v_cvt_pk_bf16_f32 v182, v19, v23\n"
"v_cvt_pk_bf16_f32 v183, v27, v31\n"
"ds_write_b64 v11, v[182:183] offset:22528\n"
"v_exp_f32_e64 v222, v222\n"
"v_cvt_pk_bf16_f32 v184, v32, v36\n"
"v_cvt_pk_bf16_f32 v185, v40, v44\n"
"ds_write_b64 v8, v[184:185] offset:24576\n"
"v_exp_f32_e64 v226, v226\n"
"v_cvt_pk_bf16_f32 v186, v33, v37\n"
"v_cvt_pk_bf16_f32 v187, v41, v45\n"
"ds_write_b64 v9, v[186:187] offset:26624\n"
"v_exp_f32_e64 v230, v230\n"
"v_cvt_pk_bf16_f32 v188, v34, v38\n"
"v_cvt_pk_bf16_f32 v189, v42, v46\n"
"ds_write_b64 v10, v[188:189] offset:28672\n"
"v_exp_f32_e64 v234, v234\n"
"v_cvt_pk_bf16_f32 v190, v35, v39\n"
"v_cvt_pk_bf16_f32 v191, v43, v47\n"
"ds_write_b64 v11, v[190:191] offset:30720\n"
"v_exp_f32_e64 v238, v238\n"
"v_pk_fma_f32 v[16:17], v[144:145], v[210:211], -0.5 op_sel:[0,0,0] op_sel_hi:[1,0,0] neg_lo:[0,1,0] neg_hi:[0,1,0]\n"
"v_pk_fma_f32 v[18:19], v[146:147], v[210:211], -0.5 op_sel:[0,0,0] op_sel_hi:[1,0,0] neg_lo:[0,1,0] neg_hi:[0,1,0]\n"
"v_pk_fma_f32 v[20:21], v[152:153], v[214:215], -0.5 op_sel:[0,0,0] op_sel_hi:[1,0,0] neg_lo:[0,1,0] neg_hi:[0,1,0]\n"
"v_pk_fma_f32 v[22:23], v[154:155], v[214:215], -0.5 op_sel:[0,0,0] op_sel_hi:[1,0,0] neg_lo:[0,1,0] neg_hi:[0,1,0]\n"
"v_pk_fma_f32 v[24:25], v[160:161], v[218:219], -0.5 op_sel:[0,0,0] op_sel_hi:[1,0,0] neg_lo:[0,1,0] neg_hi:[0,1,0]\n"
"v_pk_fma_f32 v[26:27], v[162:163], v[218:219], -0.5 op_sel:[0,0,0] op_sel_hi:[1,0,0] neg_lo:[0,1,0] neg_hi:[0,1,0]\n"
"v_pk_fma_f32 v[28:29], v[168:169], v[222:223], -0.5 op_sel:[0,0,0] op_sel_hi:[1,0,0] neg_lo:[0,1,0] neg_hi:[0,1,0]\n"
"v_pk_fma_f32 v[30:31], v[170:171], v[222:223], -0.5 op_sel:[0,0,0] op_sel_hi:[1,0,0] neg_lo:[0,1,0] neg_hi:[0,1,0]\n"
"v_pk_fma_f32 v[32:33], v[148:149], v[226:227], -0.5 op_sel:[0,0,0] op_sel_hi:[1,0,0] neg_lo:[0,1,0] neg_hi:[0,1,0]\n"
"v_pk_fma_f32 v[34:35], v[150:151], v[226:227], -0.5 op_sel:[0,0,0] op_sel_hi:[1,0,0] neg_lo:[0,1,0] neg_hi:[0,1,0]\n"
"v_pk_fma_f32 v[36:37], v[156:157], v[230:231], -0.5 op_sel:[0,0,0] op_sel_hi:[1,0,0] neg_lo:[0,1,0] neg_hi:[0,1,0]\n"
"v_pk_fma_f32 v[38:39], v[158:159], v[230:231], -0.5 op_sel:[0,0,0] op_sel_hi:[1,0,0] neg_lo:[0,1,0] neg_hi:[0,1,0]\n"
"v_pk_fma_f32 v[40:41], v[164:165], v[234:235], -0.5 op_sel:[0,0,0] op_sel_hi:[1,0,0] neg_lo:[0,1,0] neg_hi:[0,1,0]\n"
"v_pk_fma_f32 v[42:43], v[166:167], v[234:235], -0.5 op_sel:[0,0,0] op_sel_hi:[1,0,0] neg_lo:[0,1,0] neg_hi:[0,1,0]\n"
"v_pk_fma_f32 v[44:45], v[172:173], v[238:239], -0.5 op_sel:[0,0,0] op_sel_hi:[1,0,0] neg_lo:[0,1,0] neg_hi:[0,1,0]\n"
"v_pk_fma_f32 v[46:47], v[174:175], v[238:239], -0.5 op_sel:[0,0,0] op_sel_hi:[1,0,0] neg_lo:[0,1,0] neg_hi:[0,1,0]\n"
"v_rcp_f32 v16, v16\n"
"v_rcp_f32 v20, v20\n"
"v_rcp_f32 v24, v24\n"
"v_rcp_f32 v28, v28\n"
"v_rcp_f32 v17, v17\n"
"v_rcp_f32 v21, v21\n"
"v_rcp_f32 v25, v25\n"
"v_rcp_f32 v29, v29\n"
"v_rcp_f32 v18, v18\n"
"v_rcp_f32 v22, v22\n"
"v_rcp_f32 v26, v26\n"
"v_rcp_f32 v30, v30\n"
"v_rcp_f32 v19, v19\n"
"v_rcp_f32 v23, v23\n"
"v_rcp_f32 v27, v27\n"
"v_rcp_f32 v31, v31\n"
"v_rcp_f32 v32, v32\n"
"v_rcp_f32 v36, v36\n"
"v_rcp_f32 v40, v40\n"
"v_rcp_f32 v44, v44\n"
"v_rcp_f32 v33, v33\n"
"v_rcp_f32 v37, v37\n"
"v_rcp_f32 v41, v41\n"
"v_rcp_f32 v45, v45\n"
"v_rcp_f32 v34, v34\n"
"v_rcp_f32 v38, v38\n"
"v_rcp_f32 v42, v42\n"
"v_rcp_f32 v46, v46\n"
"v_rcp_f32 v35, v35\n"
"v_rcp_f32 v39, v39\n"
"v_rcp_f32 v43, v43\n"
"v_rcp_f32 v47, v47\n"
"v_pk_add_f32 v[16:17], v[16:17], 1.0 op_sel_hi:[1,0]\n"
"v_pk_add_f32 v[18:19], v[18:19], 1.0 op_sel_hi:[1,0]\n"
"v_pk_add_f32 v[20:21], v[20:21], 1.0 op_sel_hi:[1,0]\n"
"v_pk_add_f32 v[22:23], v[22:23], 1.0 op_sel_hi:[1,0]\n"
"v_pk_add_f32 v[24:25], v[24:25], 1.0 op_sel_hi:[1,0]\n"
"v_pk_add_f32 v[26:27], v[26:27], 1.0 op_sel_hi:[1,0]\n"
"v_pk_add_f32 v[28:29], v[28:29], 1.0 op_sel_hi:[1,0]\n"
"v_pk_add_f32 v[30:31], v[30:31], 1.0 op_sel_hi:[1,0]\n"
"v_pk_add_f32 v[32:33], v[32:33], 1.0 op_sel_hi:[1,0]\n"
"v_pk_add_f32 v[34:35], v[34:35], 1.0 op_sel_hi:[1,0]\n"
"v_pk_add_f32 v[36:37], v[36:37], 1.0 op_sel_hi:[1,0]\n"
"v_pk_add_f32 v[38:39], v[38:39], 1.0 op_sel_hi:[1,0]\n"
"v_pk_add_f32 v[40:41], v[40:41], 1.0 op_sel_hi:[1,0]\n"
"v_pk_add_f32 v[42:43], v[42:43], 1.0 op_sel_hi:[1,0]\n"
"v_pk_add_f32 v[44:45], v[44:45], 1.0 op_sel_hi:[1,0]\n"
"v_pk_add_f32 v[46:47], v[46:47], 1.0 op_sel_hi:[1,0]\n"
"v_cvt_pk_bf16_f32 v176, v16, v20\n"
"v_cvt_pk_bf16_f32 v177, v24, v28\n"
"v_exp_f32_e64 v211, v211\n"
"v_cvt_pk_bf16_f32 v178, v17, v21\n"
"v_cvt_pk_bf16_f32 v179, v25, v29\n"
"v_exp_f32_e64 v215, v215\n"
"v_cvt_pk_bf16_f32 v180, v18, v22\n"
"v_cvt_pk_bf16_f32 v181, v26, v30\n"
"v_exp_f32_e64 v219, v219\n"
"v_cvt_pk_bf16_f32 v182, v19, v23\n"
"v_cvt_pk_bf16_f32 v183, v27, v31\n"
"v_exp_f32_e64 v223, v223\n"
"v_cvt_pk_bf16_f32 v184, v32, v36\n"
"v_cvt_pk_bf16_f32 v185, v40, v44\n"
"v_exp_f32_e64 v227, v227\n"
"v_cvt_pk_bf16_f32 v186, v33, v37\n"
"v_cvt_pk_bf16_f32 v187, v41, v45\n"
"v_exp_f32_e64 v231, v231\n"
"v_cvt_pk_bf16_f32 v188, v34, v38\n"
"v_cvt_pk_bf16_f32 v189, v42, v46\n"
"v_exp_f32_e64 v235, v235\n"
"v_cvt_pk_bf16_f32 v190, v35, v39\n"
"v_cvt_pk_bf16_f32 v191, v43, v47\n"
"v_exp_f32 v239, v239\n"
"s_waitcnt vmcnt(0)\n"
"s_waitcnt lgkmcnt(0)\n"
"s_barrier\n"
"ds_read_b128 v[48:51], v12\n"
"ds_read_b128 v[52:55], v13\n"
"ds_read_b128 a[0:3], v240\n"
"ds_read_b128 a[4:7], v240 offset:1024\n"
"ds_read_b128 a[8:11], v240 offset:2048\n"
"ds_read_b128 a[12:15], v240 offset:3072\n"
"ds_read_b128 a[16:19], v240 offset:4096\n"
"ds_read_b128 a[20:23], v240 offset:5120\n"
"ds_read_b128 a[24:27], v240 offset:6144\n"
"ds_read_b128 a[28:31], v240 offset:7168\n"
"ds_read_b128 v[56:59], v14\n"
"ds_read_b128 v[60:63], v15\n"
"ds_read_b128 a[32:35], v240 offset:16384\n"
"s_waitcnt lgkmcnt(7)\n"
"s_nop 0\n"
"v_mfma_f32_16x16x32_bf16 v[80:83], v[48:51], a[0:3], 0\n"
"ds_read_b128 a[36:39], v240 offset:17408\n"
"v_mfma_f32_16x16x32_bf16 v[84:87], v[48:51], a[4:7], 0\n"
"ds_read_b128 a[40:43], v240 offset:18432\n"
"v_mfma_f32_16x16x32_bf16 v[88:91], v[48:51], a[8:11], 0\n"
"ds_read_b128 a[44:47], v240 offset:19456\n"
"v_mfma_f32_16x16x32_bf16 v[92:95], v[48:51], a[12:15], 0\n"
"ds_read_b128 a[48:51], v240 offset:20480\n"
"ds_read_b128 v[64:67], v12 offset:256\n"
"s_waitcnt lgkmcnt(8)\n"
"s_nop 0\n"
"v_mfma_f32_16x16x32_bf16 v[96:99], v[48:51], a[16:19], 0\n"
"ds_read_b128 a[52:55], v240 offset:21504\n"
"v_mfma_f32_16x16x32_bf16 v[100:103], v[48:51], a[20:23], 0\n"
"ds_read_b128 a[56:59], v240 offset:22528\n"
"v_mfma_f32_16x16x32_bf16 v[104:107], v[48:51], a[24:27], 0\n"
"ds_read_b128 a[60:63], v240 offset:23552\n"
"v_mfma_f32_16x16x32_bf16 v[108:111], v[48:51], a[28:31], 0\n"
"ds_read_b128 a[64:67], v240 offset:32768\n"
"ds_read_b128 v[68:71], v13 offset:256\n"
"s_waitcnt lgkmcnt(7)\n"
"s_nop 0\n"
"v_mfma_f32_16x16x32_bf16 v[80:83], v[52:55], a[32:35], v[80:83]\n"
"ds_read_b128 a[68:71], v240 offset:33792\n"
"v_mfma_f32_16x16x32_bf16 v[84:87], v[52:55], a[36:39], v[84:87]\n"
"ds_read_b128 a[72:75], v240 offset:34816\n"
"v_mfma_f32_16x16x32_bf16 v[88:91], v[52:55], a[40:43], v[88:91]\n"
"ds_read_b128 a[76:79], v240 offset:35840\n"
"v_mfma_f32_16x16x32_bf16 v[92:95], v[52:55], a[44:47], v[92:95]\n"
"ds_read_b128 a[80:83], v240 offset:36864\n"
"ds_read_b128 v[72:75], v14 offset:256\n"
"s_waitcnt lgkmcnt(7)\n"
"s_nop 0\n"
"v_mfma_f32_16x16x32_bf16 v[96:99], v[52:55], a[48:51], v[96:99]\n"
"ds_read_b128 a[84:87], v240 offset:37888\n"
"v_mfma_f32_16x16x32_bf16 v[100:103], v[52:55], a[52:55], v[100:103]\n"
"ds_read_b128 a[88:91], v240 offset:38912\n"
"v_mfma_f32_16x16x32_bf16 v[104:107], v[52:55], a[56:59], v[104:107]\n"
"ds_read_b128 a[92:95], v240 offset:39936\n"
"v_mfma_f32_16x16x32_bf16 v[108:111], v[52:55], a[60:63], v[108:111]\n"
"ds_read_b128 a[96:99], v240 offset:49152\n"
"ds_read_b128 v[76:79], v15 offset:256\n"
"s_waitcnt lgkmcnt(7)\n"
"v_mfma_f32_16x16x32_bf16 v[80:83], v[56:59], a[64:67], v[80:83]\n"
"s_barrier\n"
"ds_read_b128 a[100:103], v240 offset:50176\n"
"v_mfma_f32_16x16x32_bf16 v[84:87], v[56:59], a[68:71], v[84:87]\n"
"ds_read_b128 a[104:107], v240 offset:51200\n"
"v_mfma_f32_16x16x32_bf16 v[88:91], v[56:59], a[72:75], v[88:91]\n"
"ds_read_b128 a[108:111], v240 offset:52224\n"
"v_mfma_f32_16x16x32_bf16 v[92:95], v[56:59], a[76:79], v[92:95]\n"
"ds_read_b128 a[112:115], v240 offset:53248\n"
"s_waitcnt lgkmcnt(6)\n"
"s_nop 0\n"
"v_mfma_f32_16x16x32_bf16 v[96:99], v[56:59], a[80:83], v[96:99]\n"
"ds_write_b64 v244, v[176:177]\n"
"ds_read_b128 a[116:119], v240 offset:54272\n"
"v_mfma_f32_16x16x32_bf16 v[100:103], v[56:59], a[84:87], v[100:103]\n"
"ds_read_b128 a[120:123], v240 offset:55296\n"
"v_mfma_f32_16x16x32_bf16 v[104:107], v[56:59], a[88:91], v[104:107]\n"
"ds_read_b128 a[124:127], v240 offset:56320\n"
"v_mfma_f32_16x16x32_bf16 v[108:111], v[56:59], a[92:95], v[108:111]\n"
"ds_read_b128 a[128:131], v241\n"
"s_waitcnt lgkmcnt(6)\n"
"s_nop 0\n"
"v_mfma_f32_16x16x32_bf16 v[80:83], v[60:63], a[96:99], v[80:83]\n"
"ds_write_b64 v245, v[178:179] offset:2048\n"
"ds_read_b128 a[132:135], v241 offset:1024\n"
"v_mfma_f32_16x16x32_bf16 v[84:87], v[60:63], a[100:103], v[84:87]\n"
"ds_read_b128 a[136:139], v241 offset:2048\n"
"v_mfma_f32_16x16x32_bf16 v[88:91], v[60:63], a[104:107], v[88:91]\n"
"ds_read_b128 a[140:143], v241 offset:3072\n"
"v_mfma_f32_16x16x32_bf16 v[92:95], v[60:63], a[108:111], v[92:95]\n"
"ds_read_b128 a[144:147], v241 offset:4096\n"
"s_waitcnt lgkmcnt(6)\n"
"s_nop 0\n"
"v_mfma_f32_16x16x32_bf16 v[96:99], v[60:63], a[112:115], v[96:99]\n"
"ds_write_b64 v246, v[180:181] offset:4096\n"
"ds_read_b128 a[148:151], v241 offset:5120\n"
"v_mfma_f32_16x16x32_bf16 v[100:103], v[60:63], a[116:119], v[100:103]\n"
"ds_read_b128 a[152:155], v241 offset:6144\n"
"v_mfma_f32_16x16x32_bf16 v[104:107], v[60:63], a[120:123], v[104:107]\n"
"ds_read_b128 a[156:159], v241 offset:7168\n"
"v_mfma_f32_16x16x32_bf16 v[108:111], v[60:63], a[124:127], v[108:111]\n"
"ds_read_b128 a[160:163], v241 offset:16384\n"
"s_waitcnt lgkmcnt(6)\n"
"s_nop 0\n"
"v_mfma_f32_16x16x32_bf16 v[80:83], v[64:67], a[128:131], v[80:83]\n"
"ds_write_b64 v247, v[182:183] offset:6144\n"
"ds_read_b128 a[164:167], v241 offset:17408\n"
"v_mfma_f32_16x16x32_bf16 v[84:87], v[64:67], a[132:135], v[84:87]\n"
"ds_read_b128 a[168:171], v241 offset:18432\n"
"v_mfma_f32_16x16x32_bf16 v[88:91], v[64:67], a[136:139], v[88:91]\n"
"ds_read_b128 a[172:175], v241 offset:19456\n"
"v_mfma_f32_16x16x32_bf16 v[92:95], v[64:67], a[140:143], v[92:95]\n"
"ds_read_b128 a[176:179], v241 offset:20480\n"
"s_waitcnt lgkmcnt(6)\n"
"s_nop 0\n"
"v_mfma_f32_16x16x32_bf16 v[96:99], v[64:67], a[144:147], v[96:99]\n"
"ds_write_b64 v244, v[184:185] offset:8192\n"
"ds_read_b128 a[180:183], v241 offset:21504\n"
"v_mfma_f32_16x16x32_bf16 v[100:103], v[64:67], a[148:151], v[100:103]\n"
"ds_read_b128 a[184:187], v241 offset:22528\n"
"v_mfma_f32_16x16x32_bf16 v[104:107], v[64:67], a[152:155], v[104:107]\n"
"ds_read_b128 a[188:191], v241 offset:23552\n"
"v_mfma_f32_16x16x32_bf16 v[108:111], v[64:67], a[156:159], v[108:111]\n"
"ds_read_b128 a[192:195], v241 offset:32768\n"
"s_waitcnt lgkmcnt(6)\n"
"s_nop 0\n"
"v_mfma_f32_16x16x32_bf16 v[80:83], v[68:71], a[160:163], v[80:83]\n"
"ds_write_b64 v245, v[186:187] offset:10240\n"
"ds_read_b128 a[196:199], v241 offset:33792\n"
"v_mfma_f32_16x16x32_bf16 v[84:87], v[68:71], a[164:167], v[84:87]\n"
"ds_read_b128 a[200:203], v241 offset:34816\n"
"v_mfma_f32_16x16x32_bf16 v[88:91], v[68:71], a[168:171], v[88:91]\n"
"ds_read_b128 a[204:207], v241 offset:35840\n"
"v_mfma_f32_16x16x32_bf16 v[92:95], v[68:71], a[172:175], v[92:95]\n"
"ds_read_b128 a[208:211], v241 offset:36864\n"
"s_waitcnt lgkmcnt(6)\n"
"s_nop 0\n"
"v_mfma_f32_16x16x32_bf16 v[96:99], v[68:71], a[176:179], v[96:99]\n"
"ds_write_b64 v246, v[188:189] offset:12288\n"
"ds_read_b128 a[212:215], v241 offset:37888\n"
"v_mfma_f32_16x16x32_bf16 v[100:103], v[68:71], a[180:183], v[100:103]\n"
"ds_read_b128 a[216:219], v241 offset:38912\n"
"v_mfma_f32_16x16x32_bf16 v[104:107], v[68:71], a[184:187], v[104:107]\n"
"ds_read_b128 a[220:223], v241 offset:39936\n"
"v_mfma_f32_16x16x32_bf16 v[108:111], v[68:71], a[188:191], v[108:111]\n"
"ds_read_b128 a[224:227], v241 offset:49152\n"
"s_waitcnt lgkmcnt(6)\n"
"s_nop 0\n"
"v_mfma_f32_16x16x32_bf16 v[80:83], v[72:75], a[192:195], v[80:83]\n"
"ds_write_b64 v247, v[190:191] offset:14336\n"
"ds_read_b128 a[228:231], v241 offset:50176\n"
"v_mfma_f32_16x16x32_bf16 v[84:87], v[72:75], a[196:199], v[84:87]\n"
"ds_read_b128 a[232:235], v241 offset:51200\n"
"v_mfma_f32_16x16x32_bf16 v[88:91], v[72:75], a[200:203], v[88:91]\n"
"ds_read_b128 a[236:239], v241 offset:52224\n"
"v_mfma_f32_16x16x32_bf16 v[92:95], v[72:75], a[204:207], v[92:95]\n"
"ds_read_b128 a[240:243], v241 offset:53248\n"
"s_waitcnt lgkmcnt(6)\n"
"s_nop 0\n"
"v_mfma_f32_16x16x32_bf16 v[96:99], v[72:75], a[208:211], v[96:99]\n"
"ds_read_b128 a[244:247], v241 offset:54272\n"
"v_mfma_f32_16x16x32_bf16 v[100:103], v[72:75], a[212:215], v[100:103]\n"
"ds_read_b128 a[248:251], v241 offset:55296\n"
"v_mfma_f32_16x16x32_bf16 v[104:107], v[72:75], a[216:219], v[104:107]\n"
"ds_read_b128 a[252:255], v241 offset:56320\n"
"v_mfma_f32_16x16x32_bf16 v[108:111], v[72:75], a[220:223], v[108:111]\n"
"s_waitcnt lgkmcnt(4)\n"
"s_nop 0\n"
"v_mfma_f32_16x16x32_bf16 v[80:83], v[76:79], a[224:227], v[80:83]\n"
"ds_read_b128 v[48:51], v12 offset:16640\n"
"v_mfma_f32_16x16x32_bf16 v[84:87], v[76:79], a[228:231], v[84:87]\n"
"ds_read_b128 v[52:55], v13 offset:16640\n"
"v_mfma_f32_16x16x32_bf16 v[88:91], v[76:79], a[232:235], v[88:91]\n"
"ds_read_b128 v[56:59], v14 offset:16640\n"
"v_mfma_f32_16x16x32_bf16 v[92:95], v[76:79], a[236:239], v[92:95]\n"
"ds_read_b128 v[60:63], v15 offset:16640\n"
"s_waitcnt lgkmcnt(4)\n"
"s_nop 0\n"
"v_mfma_f32_16x16x32_bf16 v[96:99], v[76:79], a[240:243], v[96:99]\n"
"ds_read_b128 v[0:3], v12 offset:16384\n"
"v_mfma_f32_16x16x32_bf16 v[100:103], v[76:79], a[244:247], v[100:103]\n"
"ds_read_b128 v[4:7], v13 offset:16384\n"
"v_mfma_f32_16x16x32_bf16 v[104:107], v[76:79], a[248:251], v[104:107]\n"
"ds_read_b128 v[8:11], v14 offset:16384\n"
"v_mfma_f32_16x16x32_bf16 v[108:111], v[76:79], a[252:255], v[108:111]\n"
"ds_read_b128 v[12:15], v15 offset:16384\n"
"v_pk_fma_f32 v[16:17], v[144:145], v[210:211], -0.5 op_sel:[0,1,0] op_sel_hi:[1,1,0] neg_lo:[0,1,0] neg_hi:[0,1,0]\n"
"v_pk_fma_f32 v[18:19], v[146:147], v[210:211], -0.5 op_sel:[0,1,0] op_sel_hi:[1,1,0] neg_lo:[0,1,0] neg_hi:[0,1,0]\n"
"v_pk_fma_f32 v[20:21], v[152:153], v[214:215], -0.5 op_sel:[0,1,0] op_sel_hi:[1,1,0] neg_lo:[0,1,0] neg_hi:[0,1,0]\n"
"v_pk_fma_f32 v[22:23], v[154:155], v[214:215], -0.5 op_sel:[0,1,0] op_sel_hi:[1,1,0] neg_lo:[0,1,0] neg_hi:[0,1,0]\n"
"v_pk_fma_f32 v[24:25], v[160:161], v[218:219], -0.5 op_sel:[0,1,0] op_sel_hi:[1,1,0] neg_lo:[0,1,0] neg_hi:[0,1,0]\n"
"v_pk_fma_f32 v[26:27], v[162:163], v[218:219], -0.5 op_sel:[0,1,0] op_sel_hi:[1,1,0] neg_lo:[0,1,0] neg_hi:[0,1,0]\n"
"v_pk_fma_f32 v[28:29], v[168:169], v[222:223], -0.5 op_sel:[0,1,0] op_sel_hi:[1,1,0] neg_lo:[0,1,0] neg_hi:[0,1,0]\n"
"v_pk_fma_f32 v[30:31], v[170:171], v[222:223], -0.5 op_sel:[0,1,0] op_sel_hi:[1,1,0] neg_lo:[0,1,0] neg_hi:[0,1,0]\n"
"v_pk_fma_f32 v[32:33], v[148:149], v[226:227], -0.5 op_sel:[0,1,0] op_sel_hi:[1,1,0] neg_lo:[0,1,0] neg_hi:[0,1,0]\n"
"v_pk_fma_f32 v[34:35], v[150:151], v[226:227], -0.5 op_sel:[0,1,0] op_sel_hi:[1,1,0] neg_lo:[0,1,0] neg_hi:[0,1,0]\n"
"v_pk_fma_f32 v[36:37], v[156:157], v[230:231], -0.5 op_sel:[0,1,0] op_sel_hi:[1,1,0] neg_lo:[0,1,0] neg_hi:[0,1,0]\n"
"v_pk_fma_f32 v[38:39], v[158:159], v[230:231], -0.5 op_sel:[0,1,0] op_sel_hi:[1,1,0] neg_lo:[0,1,0] neg_hi:[0,1,0]\n"
"v_pk_fma_f32 v[40:41], v[164:165], v[234:235], -0.5 op_sel:[0,1,0] op_sel_hi:[1,1,0] neg_lo:[0,1,0] neg_hi:[0,1,0]\n"
"v_pk_fma_f32 v[42:43], v[166:167], v[234:235], -0.5 op_sel:[0,1,0] op_sel_hi:[1,1,0] neg_lo:[0,1,0] neg_hi:[0,1,0]\n"
"v_pk_fma_f32 v[44:45], v[172:173], v[238:239], -0.5 op_sel:[0,1,0] op_sel_hi:[1,1,0] neg_lo:[0,1,0] neg_hi:[0,1,0]\n"
"v_pk_fma_f32 v[46:47], v[174:175], v[238:239], -0.5 op_sel:[0,1,0] op_sel_hi:[1,1,0] neg_lo:[0,1,0] neg_hi:[0,1,0]\n"
"s_waitcnt lgkmcnt(0)\n"
"v_mfma_f32_16x16x32_bf16 v[112:115], v[0:3], a[0:3], 0\n"
"v_rcp_f32 v16, v16\n"
"v_mfma_f32_16x16x32_bf16 v[116:119], v[0:3], a[4:7], 0\n"
"v_rcp_f32_e64 v20, v20\n"
"v_mfma_f32_16x16x32_bf16 v[120:123], v[0:3], a[8:11], 0\n"
"v_cvt_pk_bf16_f32 v192, v80, v84\n"
"v_cvt_pk_bf16_f32 v193, v88, v92\n"
"v_cvt_pk_bf16_f32 v194, v96, v100\n"
"v_cvt_pk_bf16_f32 v195, v104, v108\n"
"v_mfma_f32_16x16x32_bf16 v[124:127], v[0:3], a[12:15], 0\n"
"v_rcp_f32_e64 v24, v24\n"
"v_mfma_f32_16x16x32_bf16 v[128:131], v[0:3], a[16:19], 0\n"
"v_rcp_f32_e64 v28, v28\n"
"v_mfma_f32_16x16x32_bf16 v[132:135], v[0:3], a[20:23], 0\n"
"s_xor_b32 s92, s9, 0\n"
"s_nop 0\n"
"buffer_store_dwordx4 v[192:195], v242, s[48:51], s92 offen sc1\n"
"v_cvt_pk_bf16_f32 v196, v81, v85\n"
"v_cvt_pk_bf16_f32 v197, v89, v93\n"
"v_mfma_f32_16x16x32_bf16 v[136:139], v[0:3], a[24:27], 0\n"
"v_rcp_f32_e64 v17, v17\n"
"v_mfma_f32_16x16x32_bf16 v[140:143], v[0:3], a[28:31], 0\n"
"v_rcp_f32_e64 v21, v21\n"
"v_mfma_f32_16x16x32_bf16 v[112:115], v[4:7], a[32:35], v[112:115]\n"
"v_cvt_pk_bf16_f32 v198, v97, v101\n"
"v_cvt_pk_bf16_f32 v199, v105, v109\n"
"s_xor_b32 s93, s9, 0x1000\n"
"buffer_store_dwordx4 v[196:199], v242, s[48:51], s93 offen sc1\n"
"v_mfma_f32_16x16x32_bf16 v[116:119], v[4:7], a[36:39], v[116:119]\n"
"v_rcp_f32_e64 v25, v25\n"
"v_mfma_f32_16x16x32_bf16 v[120:123], v[4:7], a[40:43], v[120:123]\n"
"v_rcp_f32_e64 v29, v29\n"
"v_mfma_f32_16x16x32_bf16 v[124:127], v[4:7], a[44:47], v[124:127]\n"
"v_cvt_pk_bf16_f32 v200, v82, v86\n"
"v_add_f32 v16, 1.0, v16\n"
"v_add_f32 v20, 1.0, v20\n"
"v_add_f32_e64 v24, 1.0, v24\n"
"v_mfma_f32_16x16x32_bf16 v[128:131], v[4:7], a[48:51], v[128:131]\n"
"v_rcp_f32_e64 v18, v18\n"
"v_mfma_f32_16x16x32_bf16 v[132:135], v[4:7], a[52:55], v[132:135]\n"
"v_rcp_f32_e64 v22, v22\n"
"v_mfma_f32_16x16x32_bf16 v[136:139], v[4:7], a[56:59], v[136:139]\n"
"v_add_f32_e64 v28, 1.0, v28\n"
"v_cvt_pk_bf16_f32 v176, v16, v20\n"
"v_cvt_pk_bf16_f32 v177, v24, v28\n"
"ds_write_b64 v244, v[176:177] offset:16384\n"
"v_mfma_f32_16x16x32_bf16 v[140:143], v[4:7], a[60:63], v[140:143]\n"
"v_rcp_f32_e64 v26, v26\n"
"v_mfma_f32_16x16x32_bf16 v[112:115], v[8:11], a[64:67], v[112:115]\n"
"v_rcp_f32_e64 v30, v30\n"
"v_mfma_f32_16x16x32_bf16 v[116:119], v[8:11], a[68:71], v[116:119]\n"
"v_add_f32 v17, 1.0, v17\n"
"v_add_f32 v21, 1.0, v21\n"
"v_add_f32 v25, 1.0, v25\n"
"v_add_f32 v29, 1.0, v29\n"
"v_mfma_f32_16x16x32_bf16 v[120:123], v[8:11], a[72:75], v[120:123]\n"
"v_rcp_f32_e64 v19, v19\n"
"v_mfma_f32_16x16x32_bf16 v[124:127], v[8:11], a[76:79], v[124:127]\n"
"v_rcp_f32_e64 v23, v23\n"
"v_mfma_f32_16x16x32_bf16 v[128:131], v[8:11], a[80:83], v[128:131]\n"
"v_cvt_pk_bf16_f32 v178, v17, v21\n"
"v_cvt_pk_bf16_f32 v179, v25, v29\n"
"ds_write_b64 v245, v[178:179] offset:18432\n"
"v_add_f32_e64 v18, 1.0, v18\n"
"v_mfma_f32_16x16x32_bf16 v[132:135], v[8:11], a[84:87], v[132:135]\n"
"v_rcp_f32_e64 v27, v27\n"
"v_mfma_f32_16x16x32_bf16 v[136:139], v[8:11], a[88:91], v[136:139]\n"
"v_rcp_f32_e64 v31, v31\n"
"v_mfma_f32_16x16x32_bf16 v[140:143], v[8:11], a[92:95], v[140:143]\n"
"v_add_f32 v22, 1.0, v22\n"
"v_add_f32 v26, 1.0, v26\n"
"v_add_f32_e64 v30, 1.0, v30\n"
"v_cvt_pk_bf16_f32 v180, v18, v22\n"
"v_mfma_f32_16x16x32_bf16 v[112:115], v[12:15], a[96:99], v[112:115]\n"
"v_rcp_f32_e64 v32, v32\n"
"v_mfma_f32_16x16x32_bf16 v[116:119], v[12:15], a[100:103], v[116:119]\n"
"v_rcp_f32_e64 v36, v36\n"
"v_mfma_f32_16x16x32_bf16 v[120:123], v[12:15], a[104:107], v[120:123]\n"
"v_cvt_pk_bf16_f32 v181, v26, v30\n"
"ds_write_b64 v246, v[180:181] offset:20480\n"
"v_add_f32 v19, 1.0, v19\n"
"v_add_f32 v23, 1.0, v23\n"
"v_mfma_f32_16x16x32_bf16 v[124:127], v[12:15], a[108:111], v[124:127]\n"
"v_rcp_f32_e64 v40, v40\n"
"v_mfma_f32_16x16x32_bf16 v[128:131], v[12:15], a[112:115], v[128:131]\n"
"v_rcp_f32_e64 v44, v44\n"
"v_mfma_f32_16x16x32_bf16 v[132:135], v[12:15], a[116:119], v[132:135]\n"
"v_add_f32 v27, 1.0, v27\n"
"v_add_f32 v31, 1.0, v31\n"
"v_cvt_pk_bf16_f32 v182, v19, v23\n"
"v_mfma_f32_16x16x32_bf16 v[136:139], v[12:15], a[120:123], v[136:139]\n"
"v_rcp_f32_e64 v33, v33\n"
"v_mfma_f32_16x16x32_bf16 v[140:143], v[12:15], a[124:127], v[140:143]\n"
"v_rcp_f32_e64 v37, v37\n"
"v_mfma_f32_16x16x32_bf16 v[112:115], v[48:51], a[128:131], v[112:115]\n"
"v_cvt_pk_bf16_f32 v183, v27, v31\n"
"ds_write_b64 v247, v[182:183] offset:22528\n"
"v_add_f32_e64 v32, 1.0, v32\n"
"v_mfma_f32_16x16x32_bf16 v[116:119], v[48:51], a[132:135], v[116:119]\n"
"v_rcp_f32_e64 v41, v41\n"
"v_mfma_f32_16x16x32_bf16 v[120:123], v[48:51], a[136:139], v[120:123]\n"
"v_rcp_f32_e64 v45, v45\n"
"v_mfma_f32_16x16x32_bf16 v[124:127], v[48:51], a[140:143], v[124:127]\n"
"v_add_f32 v36, 1.0, v36\n"
"v_add_f32 v40, 1.0, v40\n"
"v_add_f32_e64 v44, 1.0, v44\n"
"v_mfma_f32_16x16x32_bf16 v[128:131], v[48:51], a[144:147], v[128:131]\n"
"v_rcp_f32_e64 v34, v34\n"
"v_mfma_f32_16x16x32_bf16 v[132:135], v[48:51], a[148:151], v[132:135]\n"
"v_rcp_f32_e64 v38, v38\n"
"v_mfma_f32_16x16x32_bf16 v[136:139], v[48:51], a[152:155], v[136:139]\n"
"v_cvt_pk_bf16_f32 v184, v32, v36\n"
"v_cvt_pk_bf16_f32 v185, v40, v44\n"
"ds_write_b64 v244, v[184:185] offset:24576\n"
"v_mfma_f32_16x16x32_bf16 v[140:143], v[48:51], a[156:159], v[140:143]\n"
"v_rcp_f32_e64 v42, v42\n"
"v_mfma_f32_16x16x32_bf16 v[112:115], v[52:55], a[160:163], v[112:115]\n"
"v_rcp_f32_e64 v46, v46\n"
"v_mfma_f32_16x16x32_bf16 v[116:119], v[52:55], a[164:167], v[116:119]\n"
"v_add_f32 v33, 1.0, v33\n"
"v_add_f32 v37, 1.0, v37\n"
"v_add_f32_e64 v41, 1.0, v41\n"
"v_mfma_f32_16x16x32_bf16 v[120:123], v[52:55], a[168:171], v[120:123]\n"
"v_rcp_f32_e64 v35, v35\n"
"v_mfma_f32_16x16x32_bf16 v[124:127], v[52:55], a[172:175], v[124:127]\n"
"v_rcp_f32_e64 v39, v39\n"
"v_mfma_f32_16x16x32_bf16 v[128:131], v[52:55], a[176:179], v[128:131]\n"
"v_add_f32_e64 v45, 1.0, v45\n"
"v_cvt_pk_bf16_f32 v186, v33, v37\n"
"v_cvt_pk_bf16_f32 v187, v41, v45\n"
"v_mfma_f32_16x16x32_bf16 v[132:135], v[52:55], a[180:183], v[132:135]\n"
"v_rcp_f32_e64 v43, v43\n"
"v_mfma_f32_16x16x32_bf16 v[136:139], v[52:55], a[184:187], v[136:139]\n"
"v_rcp_f32_e64 v47, v47\n"
"v_mfma_f32_16x16x32_bf16 v[140:143], v[52:55], a[188:191], v[140:143]\n"
"ds_write_b64 v245, v[186:187] offset:26624\n"
"v_add_f32 v34, 1.0, v34\n"
"v_add_f32 v38, 1.0, v38\n"
"v_mfma_f32_16x16x32_bf16 v[112:115], v[56:59], a[192:195], v[112:115]\n"
"v_add_f32 v42, 1.0, v42\n"
"v_add_f32 v46, 1.0, v46\n"
"v_cvt_pk_bf16_f32 v188, v34, v38\n"
"v_mfma_f32_16x16x32_bf16 v[116:119], v[56:59], a[196:199], v[116:119]\n"
"v_cvt_pk_bf16_f32 v189, v42, v46\n"
"ds_write_b64 v246, v[188:189] offset:28672\n"
"v_add_f32_e64 v35, 1.0, v35\n"
"v_mfma_f32_16x16x32_bf16 v[120:123], v[56:59], a[200:203], v[120:123]\n"
"v_add_f32 v39, 1.0, v39\n"
"v_add_f32 v43, 1.0, v43\n"
"v_add_f32_e64 v47, 1.0, v47\n"
"v_mfma_f32_16x16x32_bf16 v[124:127], v[56:59], a[204:207], v[124:127]\n"
"v_cvt_pk_bf16_f32 v190, v35, v39\n"
"v_cvt_pk_bf16_f32 v191, v43, v47\n"
"ds_write_b64 v247, v[190:191] offset:30720\n"
"v_mfma_f32_16x16x32_bf16 v[128:131], v[56:59], a[208:211], v[128:131]\n"
"v_mfma_f32_16x16x32_bf16 v[132:135], v[56:59], a[212:215], v[132:135]\n"
"s_waitcnt lgkmcnt(0)\n"
"s_barrier\n"
"v_mfma_f32_16x16x32_bf16 v[136:139], v[56:59], a[216:219], v[136:139]\n"
"ds_read_b128 v[64:67], v248\n"
"ds_read_b128 v[68:71], v249\n"
"v_mfma_f32_16x16x32_bf16 v[140:143], v[56:59], a[220:223], v[140:143]\n"
"ds_read_b128 v[72:75], v250\n"
"ds_read_b128 v[76:79], v251\n"
"v_mfma_f32_16x16x32_bf16 v[112:115], v[60:63], a[224:227], v[112:115]\n"
"ds_read_b128 v[0:3], v248 offset:256\n"
"ds_read_b128 v[4:7], v249 offset:256\n"
"v_mfma_f32_16x16x32_bf16 v[116:119], v[60:63], a[228:231], v[116:119]\n"
"ds_read_b128 v[8:11], v250 offset:256\n"
"ds_read_b128 v[12:15], v251 offset:256\n"
"v_mfma_f32_16x16x32_bf16 v[120:123], v[60:63], a[232:235], v[120:123]\n"
"v_cvt_pk_bf16_f32 v201, v90, v94\n"
"v_cvt_pk_bf16_f32 v202, v98, v102\n"
"v_mfma_f32_16x16x32_bf16 v[124:127], v[60:63], a[236:239], v[124:127]\n"
"v_cvt_pk_bf16_f32 v203, v106, v110\n"
"s_xor_b32 s92, s9, 0x9000\n"
"v_mfma_f32_16x16x32_bf16 v[128:131], v[60:63], a[240:243], v[128:131]\n"
"buffer_store_dwordx4 v[200:203], v242, s[48:51], s92 offen sc1\n"
"v_cvt_pk_bf16_f32 v204, v83, v87\n"
"v_mfma_f32_16x16x32_bf16 v[132:135], v[60:63], a[244:247], v[132:135]\n"
"v_cvt_pk_bf16_f32 v205, v91, v95\n"
"v_cvt_pk_bf16_f32 v206, v99, v103\n"
"v_mfma_f32_16x16x32_bf16 v[136:139], v[60:63], a[248:251], v[136:139]\n"
"v_cvt_pk_bf16_f32 v207, v107, v111\n"
"s_xor_b32 s93, s9, 0x8000\n"
"v_mfma_f32_16x16x32_bf16 v[140:143], v[60:63], a[252:255], v[140:143]\n"
"buffer_store_dwordx4 v[204:207], v242, s[48:51], s93 offen sc1\n"
"v_pk_fma_f32 v[16:17], v[148:149], v[208:209], -0.5 op_sel:[0,0,0] op_sel_hi:[1,0,0] neg_lo:[0,1,0] neg_hi:[0,1,0]\n"
"v_pk_fma_f32 v[18:19], v[150:151], v[208:209], -0.5 op_sel:[0,0,0] op_sel_hi:[1,0,0] neg_lo:[0,1,0] neg_hi:[0,1,0]\n"
"v_pk_fma_f32 v[20:21], v[156:157], v[212:213], -0.5 op_sel:[0,0,0] op_sel_hi:[1,0,0] neg_lo:[0,1,0] neg_hi:[0,1,0]\n"
"v_pk_fma_f32 v[22:23], v[158:159], v[212:213], -0.5 op_sel:[0,0,0] op_sel_hi:[1,0,0] neg_lo:[0,1,0] neg_hi:[0,1,0]\n"
"v_pk_fma_f32 v[24:25], v[164:165], v[216:217], -0.5 op_sel:[0,0,0] op_sel_hi:[1,0,0] neg_lo:[0,1,0] neg_hi:[0,1,0]\n"
"v_pk_fma_f32 v[26:27], v[166:167], v[216:217], -0.5 op_sel:[0,0,0] op_sel_hi:[1,0,0] neg_lo:[0,1,0] neg_hi:[0,1,0]\n"
"v_pk_fma_f32 v[28:29], v[172:173], v[220:221], -0.5 op_sel:[0,0,0] op_sel_hi:[1,0,0] neg_lo:[0,1,0] neg_hi:[0,1,0]\n"
"v_pk_fma_f32 v[30:31], v[174:175], v[220:221], -0.5 op_sel:[0,0,0] op_sel_hi:[1,0,0] neg_lo:[0,1,0] neg_hi:[0,1,0]\n"
"v_pk_fma_f32 v[32:33], v[144:145], v[224:225], -0.5 op_sel:[0,0,0] op_sel_hi:[1,0,0] neg_lo:[0,1,0] neg_hi:[0,1,0]\n"
"v_pk_fma_f32 v[34:35], v[146:147], v[224:225], -0.5 op_sel:[0,0,0] op_sel_hi:[1,0,0] neg_lo:[0,1,0] neg_hi:[0,1,0]\n"
"v_pk_fma_f32 v[36:37], v[152:153], v[228:229], -0.5 op_sel:[0,0,0] op_sel_hi:[1,0,0] neg_lo:[0,1,0] neg_hi:[0,1,0]\n"
"v_pk_fma_f32 v[38:39], v[154:155], v[228:229], -0.5 op_sel:[0,0,0] op_sel_hi:[1,0,0] neg_lo:[0,1,0] neg_hi:[0,1,0]\n"
"v_pk_fma_f32 v[40:41], v[160:161], v[232:233], -0.5 op_sel:[0,0,0] op_sel_hi:[1,0,0] neg_lo:[0,1,0] neg_hi:[0,1,0]\n"
"v_pk_fma_f32 v[42:43], v[162:163], v[232:233], -0.5 op_sel:[0,0,0] op_sel_hi:[1,0,0] neg_lo:[0,1,0] neg_hi:[0,1,0]\n"
"v_pk_fma_f32 v[44:45], v[168:169], v[236:237], -0.5 op_sel:[0,0,0] op_sel_hi:[1,0,0] neg_lo:[0,1,0] neg_hi:[0,1,0]\n"
"v_pk_fma_f32 v[46:47], v[170:171], v[236:237], -0.5 op_sel:[0,0,0] op_sel_hi:[1,0,0] neg_lo:[0,1,0] neg_hi:[0,1,0]\n"
"s_waitcnt lgkmcnt(0)\n"
"v_mfma_f32_16x16x32_bf16 v[80:83], v[64:67], a[0:3], 0\n"
"v_rcp_f32 v16, v16\n"
"v_mfma_f32_16x16x32_bf16 v[84:87], v[64:67], a[4:7], 0\n"
"v_cvt_pk_bf16_f32 v192, v112, v116\n"
"v_cvt_pk_bf16_f32 v193, v120, v124\n"
"v_cvt_pk_bf16_f32 v194, v128, v132\n"
"v_mfma_f32_16x16x32_bf16 v[88:91], v[64:67], a[8:11], 0\n"
"v_rcp_f32_e64 v20, v20\n"
"v_mfma_f32_16x16x32_bf16 v[92:95], v[64:67], a[12:15], 0\n"
"v_cvt_pk_bf16_f32 v195, v136, v140\n"
"s_xor_b32 s92, s9, 0x2000\n"
"buffer_store_dwordx4 v[192:195], v242, s[48:51], s92 offen sc1\n"
"v_mfma_f32_16x16x32_bf16 v[96:99], v[64:67], a[16:19], 0\n"
"v_rcp_f32_e64 v24, v24\n"
"v_mfma_f32_16x16x32_bf16 v[100:103], v[64:67], a[20:23], 0\n"
"v_rcp_f32_e64 v28, v28\n"
"v_mfma_f32_16x16x32_bf16 v[104:107], v[64:67], a[24:27], 0\n"
"v_cvt_pk_bf16_f32 v196, v113, v117\n"
"v_cvt_pk_bf16_f32 v197, v121, v125\n"
"v_cvt_pk_bf16_f32 v198, v129, v133\n"
"v_mfma_f32_16x16x32_bf16 v[108:111], v[64:67], a[28:31], 0\n"
"v_rcp_f32_e64 v17, v17\n"
"v_mfma_f32_16x16x32_bf16 v[80:83], v[68:71], a[32:35], v[80:83]\n"
"v_cvt_pk_bf16_f32 v199, v137, v141\n"
"s_xor_b32 s93, s9, 0x3000\n"
"buffer_store_dwordx4 v[196:199], v242, s[48:51], s93 offen sc1\n"
"v_mfma_f32_16x16x32_bf16 v[84:87], v[68:71], a[36:39], v[84:87]\n"
"v_rcp_f32_e64 v21, v21\n"
"v_mfma_f32_16x16x32_bf16 v[88:91], v[68:71], a[40:43], v[88:91]\n"
"v_rcp_f32_e64 v25, v25\n"
"v_mfma_f32_16x16x32_bf16 v[92:95], v[68:71], a[44:47], v[92:95]\n"
"v_cvt_pk_bf16_f32 v200, v114, v118\n"
"v_cvt_pk_bf16_f32 v201, v122, v126\n"
"v_cvt_pk_bf16_f32 v202, v130, v134\n"
"v_mfma_f32_16x16x32_bf16 v[96:99], v[68:71], a[48:51], v[96:99]\n"
"v_rcp_f32_e64 v29, v29\n"
"v_mfma_f32_16x16x32_bf16 v[100:103], v[68:71], a[52:55], v[100:103]\n"
"v_cvt_pk_bf16_f32 v203, v138, v142\n"
"s_xor_b32 s92, s9, 0xb000\n"
"buffer_store_dwordx4 v[200:203], v242, s[48:51], s92 offen sc1\n"
"v_mfma_f32_16x16x32_bf16 v[104:107], v[68:71], a[56:59], v[104:107]\n"
"v_rcp_f32_e64 v18, v18\n"
"v_mfma_f32_16x16x32_bf16 v[108:111], v[68:71], a[60:63], v[108:111]\n"
"v_rcp_f32_e64 v22, v22\n"
"v_mfma_f32_16x16x32_bf16 v[80:83], v[72:75], a[64:67], v[80:83]\n"
"v_cvt_pk_bf16_f32 v204, v115, v119\n"
"v_cvt_pk_bf16_f32 v205, v123, v127\n"
"v_cvt_pk_bf16_f32 v206, v131, v135\n"
"v_mfma_f32_16x16x32_bf16 v[84:87], v[72:75], a[68:71], v[84:87]\n"
"v_rcp_f32_e64 v26, v26\n"
"v_mfma_f32_16x16x32_bf16 v[88:91], v[72:75], a[72:75], v[88:91]\n"
"v_cvt_pk_bf16_f32 v207, v139, v143\n"
"s_xor_b32 s93, s9, 0xa000\n"
"buffer_store_dwordx4 v[204:207], v242, s[48:51], s93 offen sc1\n"
"v_mfma_f32_16x16x32_bf16 v[92:95], v[72:75], a[76:79], v[92:95]\n"
"v_rcp_f32_e64 v30, v30\n"
"v_mfma_f32_16x16x32_bf16 v[96:99], v[72:75], a[80:83], v[96:99]\n"
"v_rcp_f32_e64 v19, v19\n"
"v_mfma_f32_16x16x32_bf16 v[100:103], v[72:75], a[84:87], v[100:103]\n"
"v_add_f32 v16, 1.0, v16\n"
"v_add_f32 v20, 1.0, v20\n"
"v_add_f32_e64 v24, 1.0, v24\n"
"v_mfma_f32_16x16x32_bf16 v[104:107], v[72:75], a[88:91], v[104:107]\n"
"v_rcp_f32_e64 v23, v23\n"
"v_mfma_f32_16x16x32_bf16 v[108:111], v[72:75], a[92:95], v[108:111]\n"
"v_add_f32_e64 v28, 1.0, v28\n"
"v_cvt_pk_bf16_f32 v176, v16, v20\n"
"v_cvt_pk_bf16_f32 v177, v24, v28\n"
"v_mfma_f32_16x16x32_bf16 v[80:83], v[76:79], a[96:99], v[80:83]\n"
"v_rcp_f32_e64 v27, v27\n"
"v_mfma_f32_16x16x32_bf16 v[84:87], v[76:79], a[100:103], v[84:87]\n"
"v_rcp_f32_e64 v31, v31\n"
"v_mfma_f32_16x16x32_bf16 v[88:91], v[76:79], a[104:107], v[88:91]\n"
"ds_write_b64 v244, v[176:177] offset:32768\n"
"v_add_f32 v17, 1.0, v17\n"
"v_add_f32 v21, 1.0, v21\n"
"v_mfma_f32_16x16x32_bf16 v[92:95], v[76:79], a[108:111], v[92:95]\n"
"v_rcp_f32_e64 v32, v32\n"
"v_mfma_f32_16x16x32_bf16 v[96:99], v[76:79], a[112:115], v[96:99]\n"
"v_add_f32 v25, 1.0, v25\n"
"v_add_f32 v29, 1.0, v29\n"
"v_cvt_pk_bf16_f32 v178, v17, v21\n"
"v_mfma_f32_16x16x32_bf16 v[100:103], v[76:79], a[116:119], v[100:103]\n"
"v_rcp_f32_e64 v36, v36\n"
"v_mfma_f32_16x16x32_bf16 v[104:107], v[76:79], a[120:123], v[104:107]\n"
"v_rcp_f32_e64 v40, v40\n"
"v_mfma_f32_16x16x32_bf16 v[108:111], v[76:79], a[124:127], v[108:111]\n"
"v_cvt_pk_bf16_f32 v179, v25, v29\n"
"ds_write_b64 v245, v[178:179] offset:34816\n"
"v_add_f32_e64 v18, 1.0, v18\n"
"v_mfma_f32_16x16x32_bf16 v[80:83], v[0:3], a[128:131], v[80:83]\n"
"v_rcp_f32_e64 v44, v44\n"
"v_mfma_f32_16x16x32_bf16 v[84:87], v[0:3], a[132:135], v[84:87]\n"
"v_add_f32 v22, 1.0, v22\n"
"v_add_f32 v26, 1.0, v26\n"
"v_add_f32_e64 v30, 1.0, v30\n"
"v_mfma_f32_16x16x32_bf16 v[88:91], v[0:3], a[136:139], v[88:91]\n"
"v_rcp_f32_e64 v33, v33\n"
"v_mfma_f32_16x16x32_bf16 v[92:95], v[0:3], a[140:143], v[92:95]\n"
"v_rcp_f32_e64 v37, v37\n"
"v_mfma_f32_16x16x32_bf16 v[96:99], v[0:3], a[144:147], v[96:99]\n"
"v_cvt_pk_bf16_f32 v180, v18, v22\n"
"v_cvt_pk_bf16_f32 v181, v26, v30\n"
"ds_write_b64 v246, v[180:181] offset:36864\n"
"v_mfma_f32_16x16x32_bf16 v[100:103], v[0:3], a[148:151], v[100:103]\n"
"v_rcp_f32_e64 v41, v41\n"
"v_mfma_f32_16x16x32_bf16 v[104:107], v[0:3], a[152:155], v[104:107]\n"
"v_add_f32 v19, 1.0, v19\n"
"v_add_f32 v23, 1.0, v23\n"
"v_add_f32_e64 v27, 1.0, v27\n"
"v_mfma_f32_16x16x32_bf16 v[108:111], v[0:3], a[156:159], v[108:111]\n"
"v_rcp_f32_e64 v45, v45\n"
"v_mfma_f32_16x16x32_bf16 v[80:83], v[4:7], a[160:163], v[80:83]\n"
"v_rcp_f32_e64 v34, v34\n"
"v_mfma_f32_16x16x32_bf16 v[84:87], v[4:7], a[164:167], v[84:87]\n"
"v_add_f32_e64 v31, 1.0, v31\n"
"v_cvt_pk_bf16_f32 v182, v19, v23\n"
"v_cvt_pk_bf16_f32 v183, v27, v31\n"
"v_mfma_f32_16x16x32_bf16 v[88:91], v[4:7], a[168:171], v[88:91]\n"
"v_rcp_f32_e64 v38, v38\n"
"v_mfma_f32_16x16x32_bf16 v[92:95], v[4:7], a[172:175], v[92:95]\n"
"ds_write_b64 v247, v[182:183] offset:38912\n"
"v_add_f32 v32, 1.0, v32\n"
"v_add_f32 v36, 1.0, v36\n"
"v_mfma_f32_16x16x32_bf16 v[96:99], v[4:7], a[176:179], v[96:99]\n"
"v_rcp_f32_e64 v42, v42\n"
"v_mfma_f32_16x16x32_bf16 v[100:103], v[4:7], a[180:183], v[100:103]\n"
"v_rcp_f32_e64 v46, v46\n"
"v_mfma_f32_16x16x32_bf16 v[104:107], v[4:7], a[184:187], v[104:107]\n"
"v_add_f32 v40, 1.0, v40\n"
"v_add_f32 v44, 1.0, v44\n"
"v_cvt_pk_bf16_f32 v184, v32, v36\n"
"v_mfma_f32_16x16x32_bf16 v[108:111], v[4:7], a[188:191], v[108:111]\n"
"v_rcp_f32_e64 v35, v35\n"
"v_mfma_f32_16x16x32_bf16 v[80:83], v[8:11], a[192:195], v[80:83]\n"
"v_cvt_pk_bf16_f32 v185, v40, v44\n"
"ds_write_b64 v244, v[184:185] offset:40960\n"
"ds_read_b128 v[48:51], v248 offset:16384\n"
"v_mfma_f32_16x16x32_bf16 v[84:87], v[8:11], a[196:199], v[84:87]\n"
"v_rcp_f32_e64 v39, v39\n"
"v_mfma_f32_16x16x32_bf16 v[88:91], v[8:11], a[200:203], v[88:91]\n"
"v_rcp_f32_e64 v43, v43\n"
"v_mfma_f32_16x16x32_bf16 v[92:95], v[8:11], a[204:207], v[92:95]\n"
"ds_read_b128 v[52:55], v249 offset:16384\n"
"v_add_f32 v33, 1.0, v33\n"
"v_add_f32 v37, 1.0, v37\n"
"v_mfma_f32_16x16x32_bf16 v[96:99], v[8:11], a[208:211], v[96:99]\n"
"v_rcp_f32_e64 v47, v47\n"
"v_mfma_f32_16x16x32_bf16 v[100:103], v[8:11], a[212:215], v[100:103]\n"
"v_add_f32 v41, 1.0, v41\n"
"v_add_f32 v45, 1.0, v45\n"
"v_cvt_pk_bf16_f32 v186, v33, v37\n"
"v_mfma_f32_16x16x32_bf16 v[104:107], v[8:11], a[216:219], v[104:107]\n"
"v_cvt_pk_bf16_f32 v187, v41, v45\n"
"ds_write_b64 v245, v[186:187] offset:43008\n"
"ds_read_b128 v[56:59], v250 offset:16384\n"
"v_mfma_f32_16x16x32_bf16 v[108:111], v[8:11], a[220:223], v[108:111]\n"
"ds_read_b128 v[60:63], v251 offset:16384\n"
"ds_read_b128 v[64:67], v248 offset:16640\n"
"v_add_f32_e64 v34, 1.0, v34\n"
"v_mfma_f32_16x16x32_bf16 v[80:83], v[12:15], a[224:227], v[80:83]\n"
"v_add_f32 v38, 1.0, v38\n"
"v_add_f32 v42, 1.0, v42\n"
"v_mfma_f32_16x16x32_bf16 v[84:87], v[12:15], a[228:231], v[84:87]\n"
"v_add_f32_e64 v46, 1.0, v46\n"
"v_cvt_pk_bf16_f32 v188, v34, v38\n"
"v_mfma_f32_16x16x32_bf16 v[88:91], v[12:15], a[232:235], v[88:91]\n"
"v_cvt_pk_bf16_f32 v189, v42, v46\n"
"ds_write_b64 v246, v[188:189] offset:45056\n"
"v_mfma_f32_16x16x32_bf16 v[92:95], v[12:15], a[236:239], v[92:95]\n"
"ds_read_b128 v[68:71], v249 offset:16640\n"
"ds_read_b128 v[72:75], v250 offset:16640\n"
"v_mfma_f32_16x16x32_bf16 v[96:99], v[12:15], a[240:243], v[96:99]\n"
"ds_read_b128 v[76:79], v251 offset:16640\n"
"v_add_f32_e64 v35, 1.0, v35\n"
"v_mfma_f32_16x16x32_bf16 v[100:103], v[12:15], a[244:247], v[100:103]\n"
"v_add_f32 v39, 1.0, v39\n"
"v_add_f32 v43, 1.0, v43\n"
"v_mfma_f32_16x16x32_bf16 v[104:107], v[12:15], a[248:251], v[104:107]\n"
"v_add_f32_e64 v47, 1.0, v47\n"
"v_cvt_pk_bf16_f32 v190, v35, v39\n"
"v_mfma_f32_16x16x32_bf16 v[108:111], v[12:15], a[252:255], v[108:111]\n"
"v_cvt_pk_bf16_f32 v191, v43, v47\n"
"ds_write_b64 v247, v[190:191] offset:47104\n"
"v_pk_fma_f32 v[16:17], v[148:149], v[208:209], -0.5 op_sel:[0,1,0] op_sel_hi:[1,1,0] neg_lo:[0,1,0] neg_hi:[0,1,0]\n"
"v_pk_fma_f32 v[18:19], v[150:151], v[208:209], -0.5 op_sel:[0,1,0] op_sel_hi:[1,1,0] neg_lo:[0,1,0] neg_hi:[0,1,0]\n"
"v_pk_fma_f32 v[20:21], v[156:157], v[212:213], -0.5 op_sel:[0,1,0] op_sel_hi:[1,1,0] neg_lo:[0,1,0] neg_hi:[0,1,0]\n"
"v_pk_fma_f32 v[22:23], v[158:159], v[212:213], -0.5 op_sel:[0,1,0] op_sel_hi:[1,1,0] neg_lo:[0,1,0] neg_hi:[0,1,0]\n"
"v_pk_fma_f32 v[24:25], v[164:165], v[216:217], -0.5 op_sel:[0,1,0] op_sel_hi:[1,1,0] neg_lo:[0,1,0] neg_hi:[0,1,0]\n"
"v_pk_fma_f32 v[26:27], v[166:167], v[216:217], -0.5 op_sel:[0,1,0] op_sel_hi:[1,1,0] neg_lo:[0,1,0] neg_hi:[0,1,0]\n"
"v_pk_fma_f32 v[28:29], v[172:173], v[220:221], -0.5 op_sel:[0,1,0] op_sel_hi:[1,1,0] neg_lo:[0,1,0] neg_hi:[0,1,0]\n"
"v_pk_fma_f32 v[30:31], v[174:175], v[220:221], -0.5 op_sel:[0,1,0] op_sel_hi:[1,1,0] neg_lo:[0,1,0] neg_hi:[0,1,0]\n"
"v_pk_fma_f32 v[32:33], v[144:145], v[224:225], -0.5 op_sel:[0,1,0] op_sel_hi:[1,1,0] neg_lo:[0,1,0] neg_hi:[0,1,0]\n"
"v_pk_fma_f32 v[34:35], v[146:147], v[224:225], -0.5 op_sel:[0,1,0] op_sel_hi:[1,1,0] neg_lo:[0,1,0] neg_hi:[0,1,0]\n"
"v_pk_fma_f32 v[36:37], v[152:153], v[228:229], -0.5 op_sel:[0,1,0] op_sel_hi:[1,1,0] neg_lo:[0,1,0] neg_hi:[0,1,0]\n"
"v_pk_fma_f32 v[38:39], v[154:155], v[228:229], -0.5 op_sel:[0,1,0] op_sel_hi:[1,1,0] neg_lo:[0,1,0] neg_hi:[0,1,0]\n"
"v_pk_fma_f32 v[40:41], v[160:161], v[232:233], -0.5 op_sel:[0,1,0] op_sel_hi:[1,1,0] neg_lo:[0,1,0] neg_hi:[0,1,0]\n"
"v_pk_fma_f32 v[42:43], v[162:163], v[232:233], -0.5 op_sel:[0,1,0] op_sel_hi:[1,1,0] neg_lo:[0,1,0] neg_hi:[0,1,0]\n"
"v_pk_fma_f32 v[44:45], v[168:169], v[236:237], -0.5 op_sel:[0,1,0] op_sel_hi:[1,1,0] neg_lo:[0,1,0] neg_hi:[0,1,0]\n"
"v_pk_fma_f32 v[46:47], v[170:171], v[236:237], -0.5 op_sel:[0,1,0] op_sel_hi:[1,1,0] neg_lo:[0,1,0] neg_hi:[0,1,0]\n"
"s_waitcnt lgkmcnt(1)\n"
"v_mfma_f32_16x16x32_bf16 v[112:115], v[48:51], a[0:3], 0\n"
"v_rcp_f32 v16, v16\n"
"v_mfma_f32_16x16x32_bf16 v[116:119], v[48:51], a[4:7], 0\n"
"v_rcp_f32_e64 v20, v20\n"
"v_mfma_f32_16x16x32_bf16 v[120:123], v[48:51], a[8:11], 0\n"
"v_cvt_pk_bf16_f32 v192, v80, v84\n"
"v_cvt_pk_bf16_f32 v193, v88, v92\n"
"v_cvt_pk_bf16_f32 v194, v96, v100\n"
"v_cvt_pk_bf16_f32 v195, v104, v108\n"
"v_mfma_f32_16x16x32_bf16 v[124:127], v[48:51], a[12:15], 0\n"
"v_rcp_f32_e64 v24, v24\n"
"v_mfma_f32_16x16x32_bf16 v[128:131], v[48:51], a[16:19], 0\n"
"v_rcp_f32_e64 v28, v28\n"
"v_mfma_f32_16x16x32_bf16 v[132:135], v[48:51], a[20:23], 0\n"
"s_xor_b32 s92, s9, 0x4000\n"
"buffer_store_dwordx4 v[192:195], v242, s[48:51], s92 offen sc1\n"
"v_cvt_pk_bf16_f32 v196, v81, v85\n"
"v_cvt_pk_bf16_f32 v197, v89, v93\n"
"v_mfma_f32_16x16x32_bf16 v[136:139], v[48:51], a[24:27], 0\n"
"v_rcp_f32_e64 v17, v17\n"
"v_mfma_f32_16x16x32_bf16 v[140:143], v[48:51], a[28:31], 0\n"
"v_rcp_f32_e64 v21, v21\n"
"v_mfma_f32_16x16x32_bf16 v[112:115], v[52:55], a[32:35], v[112:115]\n"
"v_cvt_pk_bf16_f32 v198, v97, v101\n"
"v_cvt_pk_bf16_f32 v199, v105, v109\n"
"s_xor_b32 s93, s9, 0x5000\n"
"buffer_store_dwordx4 v[196:199], v242, s[48:51], s93 offen sc1\n"
"v_mfma_f32_16x16x32_bf16 v[116:119], v[52:55], a[36:39], v[116:119]\n"
"v_rcp_f32_e64 v25, v25\n"
"v_mfma_f32_16x16x32_bf16 v[120:123], v[52:55], a[40:43], v[120:123]\n"
"v_rcp_f32_e64 v29, v29\n"
"v_mfma_f32_16x16x32_bf16 v[124:127], v[52:55], a[44:47], v[124:127]\n"
"v_cvt_pk_bf16_f32 v200, v82, v86\n"
"v_add_f32 v16, 1.0, v16\n"
"v_add_f32 v20, 1.0, v20\n"
"v_add_f32_e64 v24, 1.0, v24\n"
"v_mfma_f32_16x16x32_bf16 v[128:131], v[52:55], a[48:51], v[128:131]\n"
"v_rcp_f32_e64 v18, v18\n"
"v_mfma_f32_16x16x32_bf16 v[132:135], v[52:55], a[52:55], v[132:135]\n"
"v_rcp_f32_e64 v22, v22\n"
"v_mfma_f32_16x16x32_bf16 v[136:139], v[52:55], a[56:59], v[136:139]\n"
"v_add_f32_e64 v28, 1.0, v28\n"
"v_cvt_pk_bf16_f32 v176, v16, v20\n"
"v_cvt_pk_bf16_f32 v177, v24, v28\n"
"ds_write_b64 v244, v[176:177] offset:49152\n"
"v_mfma_f32_16x16x32_bf16 v[140:143], v[52:55], a[60:63], v[140:143]\n"
"v_rcp_f32_e64 v26, v26\n"
"v_mfma_f32_16x16x32_bf16 v[112:115], v[56:59], a[64:67], v[112:115]\n"
"v_rcp_f32_e64 v30, v30\n"
"v_mfma_f32_16x16x32_bf16 v[116:119], v[56:59], a[68:71], v[116:119]\n"
"v_add_f32 v17, 1.0, v17\n"
"v_add_f32 v21, 1.0, v21\n"
"v_add_f32 v25, 1.0, v25\n"
"v_add_f32 v29, 1.0, v29\n"
"v_mfma_f32_16x16x32_bf16 v[120:123], v[56:59], a[72:75], v[120:123]\n"
"v_rcp_f32_e64 v19, v19\n"
"v_mfma_f32_16x16x32_bf16 v[124:127], v[56:59], a[76:79], v[124:127]\n"
"v_rcp_f32_e64 v23, v23\n"
"v_mfma_f32_16x16x32_bf16 v[128:131], v[56:59], a[80:83], v[128:131]\n"
"v_cvt_pk_bf16_f32 v178, v17, v21\n"
"v_cvt_pk_bf16_f32 v179, v25, v29\n"
"ds_write_b64 v245, v[178:179] offset:51200\n"
"v_add_f32_e64 v18, 1.0, v18\n"
"v_mfma_f32_16x16x32_bf16 v[132:135], v[56:59], a[84:87], v[132:135]\n"
"v_rcp_f32_e64 v27, v27\n"
"v_mfma_f32_16x16x32_bf16 v[136:139], v[56:59], a[88:91], v[136:139]\n"
"v_rcp_f32_e64 v31, v31\n"
"v_mfma_f32_16x16x32_bf16 v[140:143], v[56:59], a[92:95], v[140:143]\n"
"v_add_f32 v22, 1.0, v22\n"
"v_add_f32 v26, 1.0, v26\n"
"v_add_f32_e64 v30, 1.0, v30\n"
"v_cvt_pk_bf16_f32 v180, v18, v22\n"
"v_mfma_f32_16x16x32_bf16 v[112:115], v[60:63], a[96:99], v[112:115]\n"
"v_rcp_f32_e64 v32, v32\n"
"v_mfma_f32_16x16x32_bf16 v[116:119], v[60:63], a[100:103], v[116:119]\n"
"v_rcp_f32_e64 v36, v36\n"
"v_mfma_f32_16x16x32_bf16 v[120:123], v[60:63], a[104:107], v[120:123]\n"
"v_cvt_pk_bf16_f32 v181, v26, v30\n"
"ds_write_b64 v246, v[180:181] offset:53248\n"
"v_add_f32 v19, 1.0, v19\n"
"v_add_f32 v23, 1.0, v23\n"
"v_mfma_f32_16x16x32_bf16 v[124:127], v[60:63], a[108:111], v[124:127]\n"
"v_rcp_f32_e64 v40, v40\n"
"v_mfma_f32_16x16x32_bf16 v[128:131], v[60:63], a[112:115], v[128:131]\n"
"v_rcp_f32_e64 v44, v44\n"
"v_mfma_f32_16x16x32_bf16 v[132:135], v[60:63], a[116:119], v[132:135]\n"
"v_add_f32 v27, 1.0, v27\n"
"v_add_f32 v31, 1.0, v31\n"
"v_cvt_pk_bf16_f32 v182, v19, v23\n"
"v_mfma_f32_16x16x32_bf16 v[136:139], v[60:63], a[120:123], v[136:139]\n"
"v_rcp_f32_e64 v33, v33\n"
"v_mfma_f32_16x16x32_bf16 v[140:143], v[60:63], a[124:127], v[140:143]\n"
"v_rcp_f32_e64 v37, v37\n"
"v_mfma_f32_16x16x32_bf16 v[112:115], v[64:67], a[128:131], v[112:115]\n"
"v_cvt_pk_bf16_f32 v183, v27, v31\n"
"ds_write_b64 v247, v[182:183] offset:55296\n"
"v_add_f32_e64 v32, 1.0, v32\n"
"v_mfma_f32_16x16x32_bf16 v[116:119], v[64:67], a[132:135], v[116:119]\n"
"v_rcp_f32_e64 v41, v41\n"
"v_mfma_f32_16x16x32_bf16 v[120:123], v[64:67], a[136:139], v[120:123]\n"
"v_rcp_f32_e64 v45, v45\n"
"v_mfma_f32_16x16x32_bf16 v[124:127], v[64:67], a[140:143], v[124:127]\n"
"v_add_f32 v36, 1.0, v36\n"
"v_add_f32 v40, 1.0, v40\n"
"v_add_f32_e64 v44, 1.0, v44\n"
"v_mfma_f32_16x16x32_bf16 v[128:131], v[64:67], a[144:147], v[128:131]\n"
"v_rcp_f32_e64 v34, v34\n"
"v_mfma_f32_16x16x32_bf16 v[132:135], v[64:67], a[148:151], v[132:135]\n"
"v_rcp_f32_e64 v38, v38\n"
"v_mfma_f32_16x16x32_bf16 v[136:139], v[64:67], a[152:155], v[136:139]\n"
"v_cvt_pk_bf16_f32 v184, v32, v36\n"
"v_cvt_pk_bf16_f32 v185, v40, v44\n"
"ds_write_b64 v244, v[184:185] offset:57344\n"
"v_mfma_f32_16x16x32_bf16 v[140:143], v[64:67], a[156:159], v[140:143]\n"
"v_rcp_f32_e64 v42, v42\n"
"v_mfma_f32_16x16x32_bf16 v[112:115], v[68:71], a[160:163], v[112:115]\n"
"v_rcp_f32_e64 v46, v46\n"
"v_mfma_f32_16x16x32_bf16 v[116:119], v[68:71], a[164:167], v[116:119]\n"
"v_add_f32 v33, 1.0, v33\n"
"v_add_f32 v37, 1.0, v37\n"
"v_add_f32_e64 v41, 1.0, v41\n"
"v_mfma_f32_16x16x32_bf16 v[120:123], v[68:71], a[168:171], v[120:123]\n"
"v_rcp_f32_e64 v35, v35\n"
"v_mfma_f32_16x16x32_bf16 v[124:127], v[68:71], a[172:175], v[124:127]\n"
"v_rcp_f32_e64 v39, v39\n"
"v_mfma_f32_16x16x32_bf16 v[128:131], v[68:71], a[176:179], v[128:131]\n"
"v_add_f32_e64 v45, 1.0, v45\n"
"v_cvt_pk_bf16_f32 v186, v33, v37\n"
"v_cvt_pk_bf16_f32 v187, v41, v45\n"
"v_mfma_f32_16x16x32_bf16 v[132:135], v[68:71], a[180:183], v[132:135]\n"
"v_rcp_f32_e64 v43, v43\n"
"v_mfma_f32_16x16x32_bf16 v[136:139], v[68:71], a[184:187], v[136:139]\n"
"v_rcp_f32_e64 v47, v47\n"
"v_mfma_f32_16x16x32_bf16 v[140:143], v[68:71], a[188:191], v[140:143]\n"
"ds_write_b64 v245, v[186:187] offset:59392\n"
"v_add_f32 v34, 1.0, v34\n"
"v_add_f32 v38, 1.0, v38\n"
"v_mfma_f32_16x16x32_bf16 v[112:115], v[72:75], a[192:195], v[112:115]\n"
"v_add_f32 v42, 1.0, v42\n"
"v_add_f32 v46, 1.0, v46\n"
"v_cvt_pk_bf16_f32 v188, v34, v38\n"
"v_mfma_f32_16x16x32_bf16 v[116:119], v[72:75], a[196:199], v[116:119]\n"
"v_cvt_pk_bf16_f32 v189, v42, v46\n"
"ds_write_b64 v246, v[188:189] offset:61440\n"
"v_add_f32_e64 v35, 1.0, v35\n"
"v_mfma_f32_16x16x32_bf16 v[120:123], v[72:75], a[200:203], v[120:123]\n"
"v_add_f32 v39, 1.0, v39\n"
"v_add_f32 v43, 1.0, v43\n"
"v_add_f32_e64 v47, 1.0, v47\n"
"v_mfma_f32_16x16x32_bf16 v[124:127], v[72:75], a[204:207], v[124:127]\n"
"v_cvt_pk_bf16_f32 v190, v35, v39\n"
"v_cvt_pk_bf16_f32 v191, v43, v47\n"
"ds_write_b64 v247, v[190:191] offset:63488\n"
"v_mfma_f32_16x16x32_bf16 v[128:131], v[72:75], a[208:211], v[128:131]\n"
"v_mfma_f32_16x16x32_bf16 v[132:135], v[72:75], a[212:215], v[132:135]\n"
"s_waitcnt lgkmcnt(0)\n"
"s_barrier\n"
"v_mfma_f32_16x16x32_bf16 v[136:139], v[72:75], a[216:219], v[136:139]\n"
"ds_read_b128 v[0:3], v248 offset:32768\n"
"ds_read_b128 v[4:7], v249 offset:32768\n"
"v_mfma_f32_16x16x32_bf16 v[140:143], v[72:75], a[220:223], v[140:143]\n"
"ds_read_b128 v[8:11], v250 offset:32768\n"
"ds_read_b128 v[12:15], v251 offset:32768\n"
"v_mfma_f32_16x16x32_bf16 v[112:115], v[76:79], a[224:227], v[112:115]\n"
"ds_read_b128 v[48:51], v248 offset:33024\n"
"ds_read_b128 v[52:55], v249 offset:33024\n"
"v_mfma_f32_16x16x32_bf16 v[116:119], v[76:79], a[228:231], v[116:119]\n"
"ds_read_b128 v[56:59], v250 offset:33024\n"
"ds_read_b128 v[60:63], v251 offset:33024\n"
"v_mfma_f32_16x16x32_bf16 v[120:123], v[76:79], a[232:235], v[120:123]\n"
"v_cvt_pk_bf16_f32 v201, v90, v94\n"
"v_cvt_pk_bf16_f32 v202, v98, v102\n"
"v_mfma_f32_16x16x32_bf16 v[124:127], v[76:79], a[236:239], v[124:127]\n"
"v_cvt_pk_bf16_f32 v203, v106, v110\n"
"s_xor_b32 s92, s9, 0xd000\n"
"v_mfma_f32_16x16x32_bf16 v[128:131], v[76:79], a[240:243], v[128:131]\n"
"buffer_store_dwordx4 v[200:203], v242, s[48:51], s92 offen sc1\n"
"v_cvt_pk_bf16_f32 v204, v83, v87\n"
"v_mfma_f32_16x16x32_bf16 v[132:135], v[76:79], a[244:247], v[132:135]\n"
"v_cvt_pk_bf16_f32 v205, v91, v95\n"
"v_cvt_pk_bf16_f32 v206, v99, v103\n"
"v_mfma_f32_16x16x32_bf16 v[136:139], v[76:79], a[248:251], v[136:139]\n"
"v_cvt_pk_bf16_f32 v207, v107, v111\n"
"s_xor_b32 s93, s9, 0xc000\n"
"v_mfma_f32_16x16x32_bf16 v[140:143], v[76:79], a[252:255], v[140:143]\n"
"buffer_store_dwordx4 v[204:207], v242, s[48:51], s93 offen sc1\n"
"v_pk_fma_f32 v[16:17], v[148:149], v[210:211], -0.5 op_sel:[0,0,0] op_sel_hi:[1,0,0] neg_lo:[0,1,0] neg_hi:[0,1,0]\n"
"v_pk_fma_f32 v[18:19], v[150:151], v[210:211], -0.5 op_sel:[0,0,0] op_sel_hi:[1,0,0] neg_lo:[0,1,0] neg_hi:[0,1,0]\n"
"v_pk_fma_f32 v[20:21], v[156:157], v[214:215], -0.5 op_sel:[0,0,0] op_sel_hi:[1,0,0] neg_lo:[0,1,0] neg_hi:[0,1,0]\n"
"v_pk_fma_f32 v[22:23], v[158:159], v[214:215], -0.5 op_sel:[0,0,0] op_sel_hi:[1,0,0] neg_lo:[0,1,0] neg_hi:[0,1,0]\n"
"v_pk_fma_f32 v[24:25], v[164:165], v[218:219], -0.5 op_sel:[0,0,0] op_sel_hi:[1,0,0] neg_lo:[0,1,0] neg_hi:[0,1,0]\n"
"v_pk_fma_f32 v[26:27], v[166:167], v[218:219], -0.5 op_sel:[0,0,0] op_sel_hi:[1,0,0] neg_lo:[0,1,0] neg_hi:[0,1,0]\n"
"v_pk_fma_f32 v[28:29], v[172:173], v[222:223], -0.5 op_sel:[0,0,0] op_sel_hi:[1,0,0] neg_lo:[0,1,0] neg_hi:[0,1,0]\n"
"v_pk_fma_f32 v[30:31], v[174:175], v[222:223], -0.5 op_sel:[0,0,0] op_sel_hi:[1,0,0] neg_lo:[0,1,0] neg_hi:[0,1,0]\n"
"v_pk_fma_f32 v[32:33], v[144:145], v[226:227], -0.5 op_sel:[0,0,0] op_sel_hi:[1,0,0] neg_lo:[0,1,0] neg_hi:[0,1,0]\n"
"v_pk_fma_f32 v[34:35], v[146:147], v[226:227], -0.5 op_sel:[0,0,0] op_sel_hi:[1,0,0] neg_lo:[0,1,0] neg_hi:[0,1,0]\n"
"v_pk_fma_f32 v[36:37], v[152:153], v[230:231], -0.5 op_sel:[0,0,0] op_sel_hi:[1,0,0] neg_lo:[0,1,0] neg_hi:[0,1,0]\n"
"v_pk_fma_f32 v[38:39], v[154:155], v[230:231], -0.5 op_sel:[0,0,0] op_sel_hi:[1,0,0] neg_lo:[0,1,0] neg_hi:[0,1,0]\n"
"v_pk_fma_f32 v[40:41], v[160:161], v[234:235], -0.5 op_sel:[0,0,0] op_sel_hi:[1,0,0] neg_lo:[0,1,0] neg_hi:[0,1,0]\n"
"v_pk_fma_f32 v[42:43], v[162:163], v[234:235], -0.5 op_sel:[0,0,0] op_sel_hi:[1,0,0] neg_lo:[0,1,0] neg_hi:[0,1,0]\n"
"v_pk_fma_f32 v[44:45], v[168:169], v[238:239], -0.5 op_sel:[0,0,0] op_sel_hi:[1,0,0] neg_lo:[0,1,0] neg_hi:[0,1,0]\n"
"v_pk_fma_f32 v[46:47], v[170:171], v[238:239], -0.5 op_sel:[0,0,0] op_sel_hi:[1,0,0] neg_lo:[0,1,0] neg_hi:[0,1,0]\n"
"s_waitcnt lgkmcnt(0)\n"
"v_mfma_f32_16x16x32_bf16 v[80:83], v[0:3], a[0:3], 0\n"
"v_rcp_f32 v16, v16\n"
"v_mfma_f32_16x16x32_bf16 v[84:87], v[0:3], a[4:7], 0\n"
"v_cvt_pk_bf16_f32 v192, v112, v116\n"
"v_cvt_pk_bf16_f32 v193, v120, v124\n"
"v_cvt_pk_bf16_f32 v194, v128, v132\n"
"v_mfma_f32_16x16x32_bf16 v[88:91], v[0:3], a[8:11], 0\n"
"v_rcp_f32_e64 v20, v20\n"
"v_mfma_f32_16x16x32_bf16 v[92:95], v[0:3], a[12:15], 0\n"
"v_cvt_pk_bf16_f32 v195, v136, v140\n"
"s_xor_b32 s92, s9, 0x6000\n"
"buffer_store_dwordx4 v[192:195], v242, s[48:51], s92 offen sc1\n"
"v_mfma_f32_16x16x32_bf16 v[96:99], v[0:3], a[16:19], 0\n"
"v_rcp_f32_e64 v24, v24\n"
"v_mfma_f32_16x16x32_bf16 v[100:103], v[0:3], a[20:23], 0\n"
"v_rcp_f32_e64 v28, v28\n"
"v_mfma_f32_16x16x32_bf16 v[104:107], v[0:3], a[24:27], 0\n"
"v_cvt_pk_bf16_f32 v196, v113, v117\n"
"v_cvt_pk_bf16_f32 v197, v121, v125\n"
"v_cvt_pk_bf16_f32 v198, v129, v133\n"
"v_mfma_f32_16x16x32_bf16 v[108:111], v[0:3], a[28:31], 0\n"
"v_rcp_f32_e64 v17, v17\n"
"v_mfma_f32_16x16x32_bf16 v[80:83], v[4:7], a[32:35], v[80:83]\n"
"v_cvt_pk_bf16_f32 v199, v137, v141\n"
"s_xor_b32 s93, s9, 0x7000\n"
"buffer_store_dwordx4 v[196:199], v242, s[48:51], s93 offen sc1\n"
"v_mfma_f32_16x16x32_bf16 v[84:87], v[4:7], a[36:39], v[84:87]\n"
"v_rcp_f32_e64 v21, v21\n"
"v_mfma_f32_16x16x32_bf16 v[88:91], v[4:7], a[40:43], v[88:91]\n"
"v_rcp_f32_e64 v25, v25\n"
"v_mfma_f32_16x16x32_bf16 v[92:95], v[4:7], a[44:47], v[92:95]\n"
"v_cvt_pk_bf16_f32 v200, v114, v118\n"
"v_cvt_pk_bf16_f32 v201, v122, v126\n"
"v_cvt_pk_bf16_f32 v202, v130, v134\n"
"v_mfma_f32_16x16x32_bf16 v[96:99], v[4:7], a[48:51], v[96:99]\n"
"v_rcp_f32_e64 v29, v29\n"
"v_mfma_f32_16x16x32_bf16 v[100:103], v[4:7], a[52:55], v[100:103]\n"
"v_cvt_pk_bf16_f32 v203, v138, v142\n"
"s_xor_b32 s92, s9, 0xf000\n"
"buffer_store_dwordx4 v[200:203], v242, s[48:51], s92 offen sc1\n"
"v_mfma_f32_16x16x32_bf16 v[104:107], v[4:7], a[56:59], v[104:107]\n"
"v_rcp_f32_e64 v18, v18\n"
"v_mfma_f32_16x16x32_bf16 v[108:111], v[4:7], a[60:63], v[108:111]\n"
"v_rcp_f32_e64 v22, v22\n"
"v_mfma_f32_16x16x32_bf16 v[80:83], v[8:11], a[64:67], v[80:83]\n"
"v_cvt_pk_bf16_f32 v204, v115, v119\n"
"v_cvt_pk_bf16_f32 v205, v123, v127\n"
"v_cvt_pk_bf16_f32 v206, v131, v135\n"
"v_mfma_f32_16x16x32_bf16 v[84:87], v[8:11], a[68:71], v[84:87]\n"
"v_rcp_f32_e64 v26, v26\n"
"v_mfma_f32_16x16x32_bf16 v[88:91], v[8:11], a[72:75], v[88:91]\n"
"v_cvt_pk_bf16_f32 v207, v139, v143\n"
"s_xor_b32 s93, s9, 0xe000\n"
"buffer_store_dwordx4 v[204:207], v242, s[48:51], s93 offen sc1\n"
"v_mfma_f32_16x16x32_bf16 v[92:95], v[8:11], a[76:79], v[92:95]\n"
"v_rcp_f32_e64 v30, v30\n"
"v_mfma_f32_16x16x32_bf16 v[96:99], v[8:11], a[80:83], v[96:99]\n"
"v_rcp_f32_e64 v19, v19\n"
"v_mfma_f32_16x16x32_bf16 v[100:103], v[8:11], a[84:87], v[100:103]\n"
"v_add_f32 v16, 1.0, v16\n"
"v_add_f32 v20, 1.0, v20\n"
"v_add_f32_e64 v24, 1.0, v24\n"
"v_mfma_f32_16x16x32_bf16 v[104:107], v[8:11], a[88:91], v[104:107]\n"
"v_rcp_f32_e64 v23, v23\n"
"v_mfma_f32_16x16x32_bf16 v[108:111], v[8:11], a[92:95], v[108:111]\n"
"v_add_f32_e64 v28, 1.0, v28\n"
"v_cvt_pk_bf16_f32 v176, v16, v20\n"
"v_cvt_pk_bf16_f32 v177, v24, v28\n"
"v_mfma_f32_16x16x32_bf16 v[80:83], v[12:15], a[96:99], v[80:83]\n"
"v_rcp_f32_e64 v27, v27\n"
"v_mfma_f32_16x16x32_bf16 v[84:87], v[12:15], a[100:103], v[84:87]\n"
"v_rcp_f32_e64 v31, v31\n"
"v_mfma_f32_16x16x32_bf16 v[88:91], v[12:15], a[104:107], v[88:91]\n"
"ds_write_b64 v244, v[176:177]\n"
"v_add_f32 v17, 1.0, v17\n"
"v_add_f32 v21, 1.0, v21\n"
"v_mfma_f32_16x16x32_bf16 v[92:95], v[12:15], a[108:111], v[92:95]\n"
"v_rcp_f32_e64 v32, v32\n"
"v_mfma_f32_16x16x32_bf16 v[96:99], v[12:15], a[112:115], v[96:99]\n"
"v_add_f32 v25, 1.0, v25\n"
"v_add_f32 v29, 1.0, v29\n"
"v_cvt_pk_bf16_f32 v178, v17, v21\n"
"v_mfma_f32_16x16x32_bf16 v[100:103], v[12:15], a[116:119], v[100:103]\n"
"v_rcp_f32_e64 v36, v36\n"
"v_mfma_f32_16x16x32_bf16 v[104:107], v[12:15], a[120:123], v[104:107]\n"
"v_rcp_f32_e64 v40, v40\n"
"v_mfma_f32_16x16x32_bf16 v[108:111], v[12:15], a[124:127], v[108:111]\n"
"v_cvt_pk_bf16_f32 v179, v25, v29\n"
"ds_write_b64 v245, v[178:179] offset:2048\n"
"v_add_f32_e64 v18, 1.0, v18\n"
"v_mfma_f32_16x16x32_bf16 v[80:83], v[48:51], a[128:131], v[80:83]\n"
"v_rcp_f32_e64 v44, v44\n"
"v_mfma_f32_16x16x32_bf16 v[84:87], v[48:51], a[132:135], v[84:87]\n"
"v_add_f32 v22, 1.0, v22\n"
"v_add_f32 v26, 1.0, v26\n"
"v_add_f32_e64 v30, 1.0, v30\n"
"v_mfma_f32_16x16x32_bf16 v[88:91], v[48:51], a[136:139], v[88:91]\n"
"v_rcp_f32_e64 v33, v33\n"
"v_mfma_f32_16x16x32_bf16 v[92:95], v[48:51], a[140:143], v[92:95]\n"
"v_rcp_f32_e64 v37, v37\n"
"v_mfma_f32_16x16x32_bf16 v[96:99], v[48:51], a[144:147], v[96:99]\n"
"v_cvt_pk_bf16_f32 v180, v18, v22\n"
"v_cvt_pk_bf16_f32 v181, v26, v30\n"
"ds_write_b64 v246, v[180:181] offset:4096\n"
"v_mfma_f32_16x16x32_bf16 v[100:103], v[48:51], a[148:151], v[100:103]\n"
"v_rcp_f32_e64 v41, v41\n"
"v_mfma_f32_16x16x32_bf16 v[104:107], v[48:51], a[152:155], v[104:107]\n"
"v_add_f32 v19, 1.0, v19\n"
"v_add_f32 v23, 1.0, v23\n"
"v_add_f32_e64 v27, 1.0, v27\n"
"v_mfma_f32_16x16x32_bf16 v[108:111], v[48:51], a[156:159], v[108:111]\n"
"v_rcp_f32_e64 v45, v45\n"
"v_mfma_f32_16x16x32_bf16 v[80:83], v[52:55], a[160:163], v[80:83]\n"
"v_rcp_f32_e64 v34, v34\n"
"v_mfma_f32_16x16x32_bf16 v[84:87], v[52:55], a[164:167], v[84:87]\n"
"v_add_f32_e64 v31, 1.0, v31\n"
"v_cvt_pk_bf16_f32 v182, v19, v23\n"
"v_cvt_pk_bf16_f32 v183, v27, v31\n"
"v_mfma_f32_16x16x32_bf16 v[88:91], v[52:55], a[168:171], v[88:91]\n"
"v_rcp_f32_e64 v38, v38\n"
"v_mfma_f32_16x16x32_bf16 v[92:95], v[52:55], a[172:175], v[92:95]\n"
"ds_write_b64 v247, v[182:183] offset:6144\n"
"v_add_f32 v32, 1.0, v32\n"
"v_add_f32 v36, 1.0, v36\n"
"v_mfma_f32_16x16x32_bf16 v[96:99], v[52:55], a[176:179], v[96:99]\n"
"v_rcp_f32_e64 v42, v42\n"
"v_mfma_f32_16x16x32_bf16 v[100:103], v[52:55], a[180:183], v[100:103]\n"
"v_rcp_f32_e64 v46, v46\n"
"v_mfma_f32_16x16x32_bf16 v[104:107], v[52:55], a[184:187], v[104:107]\n"
"v_add_f32 v40, 1.0, v40\n"
"v_add_f32 v44, 1.0, v44\n"
"v_cvt_pk_bf16_f32 v184, v32, v36\n"
"v_mfma_f32_16x16x32_bf16 v[108:111], v[52:55], a[188:191], v[108:111]\n"
"v_rcp_f32_e64 v35, v35\n"
"v_mfma_f32_16x16x32_bf16 v[80:83], v[56:59], a[192:195], v[80:83]\n"
"v_cvt_pk_bf16_f32 v185, v40, v44\n"
"ds_write_b64 v244, v[184:185] offset:8192\n"
"ds_read_b128 v[64:67], v248 offset:49152\n"
"v_mfma_f32_16x16x32_bf16 v[84:87], v[56:59], a[196:199], v[84:87]\n"
"v_rcp_f32_e64 v39, v39\n"
"v_mfma_f32_16x16x32_bf16 v[88:91], v[56:59], a[200:203], v[88:91]\n"
"v_rcp_f32_e64 v43, v43\n"
"v_mfma_f32_16x16x32_bf16 v[92:95], v[56:59], a[204:207], v[92:95]\n"
"ds_read_b128 v[68:71], v249 offset:49152\n"
"v_add_f32 v33, 1.0, v33\n"
"v_add_f32 v37, 1.0, v37\n"
"v_mfma_f32_16x16x32_bf16 v[96:99], v[56:59], a[208:211], v[96:99]\n"
"v_rcp_f32_e64 v47, v47\n"
"v_mfma_f32_16x16x32_bf16 v[100:103], v[56:59], a[212:215], v[100:103]\n"
"v_add_f32 v41, 1.0, v41\n"
"v_add_f32 v45, 1.0, v45\n"
"v_cvt_pk_bf16_f32 v186, v33, v37\n"
"v_mfma_f32_16x16x32_bf16 v[104:107], v[56:59], a[216:219], v[104:107]\n"
"v_cvt_pk_bf16_f32 v187, v41, v45\n"
"ds_write_b64 v245, v[186:187] offset:10240\n"
"ds_read_b128 v[72:75], v250 offset:49152\n"
"v_mfma_f32_16x16x32_bf16 v[108:111], v[56:59], a[220:223], v[108:111]\n"
"ds_read_b128 v[76:79], v251 offset:49152\n"
"ds_read_b128 v[0:3], v248 offset:49408\n"
"v_add_f32_e64 v34, 1.0, v34\n"
"v_mfma_f32_16x16x32_bf16 v[80:83], v[60:63], a[224:227], v[80:83]\n"
"v_add_f32 v38, 1.0, v38\n"
"v_add_f32 v42, 1.0, v42\n"
"v_mfma_f32_16x16x32_bf16 v[84:87], v[60:63], a[228:231], v[84:87]\n"
"v_add_f32_e64 v46, 1.0, v46\n"
"v_cvt_pk_bf16_f32 v188, v34, v38\n"
"v_mfma_f32_16x16x32_bf16 v[88:91], v[60:63], a[232:235], v[88:91]\n"
"v_cvt_pk_bf16_f32 v189, v42, v46\n"
"ds_write_b64 v246, v[188:189] offset:12288\n"
"v_mfma_f32_16x16x32_bf16 v[92:95], v[60:63], a[236:239], v[92:95]\n"
"ds_read_b128 v[4:7], v249 offset:49408\n"
"ds_read_b128 v[8:11], v250 offset:49408\n"
"v_mfma_f32_16x16x32_bf16 v[96:99], v[60:63], a[240:243], v[96:99]\n"
"ds_read_b128 v[12:15], v251 offset:49408\n"
"v_add_f32_e64 v35, 1.0, v35\n"
"v_mfma_f32_16x16x32_bf16 v[100:103], v[60:63], a[244:247], v[100:103]\n"
"v_add_f32 v39, 1.0, v39\n"
"v_add_f32 v43, 1.0, v43\n"
"v_mfma_f32_16x16x32_bf16 v[104:107], v[60:63], a[248:251], v[104:107]\n"
"v_add_f32_e64 v47, 1.0, v47\n"
"v_cvt_pk_bf16_f32 v190, v35, v39\n"
"v_mfma_f32_16x16x32_bf16 v[108:111], v[60:63], a[252:255], v[108:111]\n"
"v_cvt_pk_bf16_f32 v191, v43, v47\n"
"ds_write_b64 v247, v[190:191] offset:14336\n"
"v_pk_fma_f32 v[16:17], v[148:149], v[210:211], -0.5 op_sel:[0,1,0] op_sel_hi:[1,1,0] neg_lo:[0,1,0] neg_hi:[0,1,0]\n"
"v_pk_fma_f32 v[18:19], v[150:151], v[210:211], -0.5 op_sel:[0,1,0] op_sel_hi:[1,1,0] neg_lo:[0,1,0] neg_hi:[0,1,0]\n"
"v_pk_fma_f32 v[20:21], v[156:157], v[214:215], -0.5 op_sel:[0,1,0] op_sel_hi:[1,1,0] neg_lo:[0,1,0] neg_hi:[0,1,0]\n"
"v_pk_fma_f32 v[22:23], v[158:159], v[214:215], -0.5 op_sel:[0,1,0] op_sel_hi:[1,1,0] neg_lo:[0,1,0] neg_hi:[0,1,0]\n"
"v_pk_fma_f32 v[24:25], v[164:165], v[218:219], -0.5 op_sel:[0,1,0] op_sel_hi:[1,1,0] neg_lo:[0,1,0] neg_hi:[0,1,0]\n"
"v_pk_fma_f32 v[26:27], v[166:167], v[218:219], -0.5 op_sel:[0,1,0] op_sel_hi:[1,1,0] neg_lo:[0,1,0] neg_hi:[0,1,0]\n"
"v_pk_fma_f32 v[28:29], v[172:173], v[222:223], -0.5 op_sel:[0,1,0] op_sel_hi:[1,1,0] neg_lo:[0,1,0] neg_hi:[0,1,0]\n"
"v_pk_fma_f32 v[30:31], v[174:175], v[222:223], -0.5 op_sel:[0,1,0] op_sel_hi:[1,1,0] neg_lo:[0,1,0] neg_hi:[0,1,0]\n"
"v_pk_fma_f32 v[32:33], v[144:145], v[226:227], -0.5 op_sel:[0,1,0] op_sel_hi:[1,1,0] neg_lo:[0,1,0] neg_hi:[0,1,0]\n"
"v_pk_fma_f32 v[34:35], v[146:147], v[226:227], -0.5 op_sel:[0,1,0] op_sel_hi:[1,1,0] neg_lo:[0,1,0] neg_hi:[0,1,0]\n"
"v_pk_fma_f32 v[36:37], v[152:153], v[230:231], -0.5 op_sel:[0,1,0] op_sel_hi:[1,1,0] neg_lo:[0,1,0] neg_hi:[0,1,0]\n"
"v_pk_fma_f32 v[38:39], v[154:155], v[230:231], -0.5 op_sel:[0,1,0] op_sel_hi:[1,1,0] neg_lo:[0,1,0] neg_hi:[0,1,0]\n"
"v_pk_fma_f32 v[40:41], v[160:161], v[234:235], -0.5 op_sel:[0,1,0] op_sel_hi:[1,1,0] neg_lo:[0,1,0] neg_hi:[0,1,0]\n"
"v_pk_fma_f32 v[42:43], v[162:163], v[234:235], -0.5 op_sel:[0,1,0] op_sel_hi:[1,1,0] neg_lo:[0,1,0] neg_hi:[0,1,0]\n"
"v_pk_fma_f32 v[44:45], v[168:169], v[238:239], -0.5 op_sel:[0,1,0] op_sel_hi:[1,1,0] neg_lo:[0,1,0] neg_hi:[0,1,0]\n"
"v_pk_fma_f32 v[46:47], v[170:171], v[238:239], -0.5 op_sel:[0,1,0] op_sel_hi:[1,1,0] neg_lo:[0,1,0] neg_hi:[0,1,0]\n"
"s_waitcnt lgkmcnt(1)\n"
"v_mfma_f32_16x16x32_bf16 v[112:115], v[64:67], a[0:3], 0\n"
"v_rcp_f32 v16, v16\n"
"v_mfma_f32_16x16x32_bf16 v[116:119], v[64:67], a[4:7], 0\n"
"v_rcp_f32_e64 v20, v20\n"
"v_mfma_f32_16x16x32_bf16 v[120:123], v[64:67], a[8:11], 0\n"
"v_xor_b32 v242, 0x800, v242\n"
"v_cvt_pk_bf16_f32 v192, v80, v84\n"
"v_cvt_pk_bf16_f32 v193, v88, v92\n"
"v_cvt_pk_bf16_f32 v194, v96, v100\n"
"v_mfma_f32_16x16x32_bf16 v[124:127], v[64:67], a[12:15], 0\n"
"v_rcp_f32_e64 v24, v24\n"
"v_mfma_f32_16x16x32_bf16 v[128:131], v[64:67], a[16:19], 0\n"
"v_rcp_f32_e64 v28, v28\n"
"v_mfma_f32_16x16x32_bf16 v[132:135], v[64:67], a[20:23], 0\n"
"v_cvt_pk_bf16_f32 v195, v104, v108\n"
"s_xor_b32 s92, s9, 0\n"
"buffer_store_dwordx4 v[192:195], v242, s[48:51], s92 offen sc1\n"
"v_cvt_pk_bf16_f32 v196, v81, v85\n"
"v_mfma_f32_16x16x32_bf16 v[136:139], v[64:67], a[24:27], 0\n"
"v_rcp_f32 v17, v17\n"
"v_mfma_f32_16x16x32_bf16 v[140:143], v[64:67], a[28:31], 0\n"
"v_rcp_f32_e64 v21, v21\n"
"v_mfma_f32_16x16x32_bf16 v[112:115], v[68:71], a[32:35], v[112:115]\n"
"v_cvt_pk_bf16_f32 v197, v89, v93\n"
"v_cvt_pk_bf16_f32 v198, v97, v101\n"
"v_cvt_pk_bf16_f32 v199, v105, v109\n"
"s_xor_b32 s93, s9, 0x1000\n"
"v_mfma_f32_16x16x32_bf16 v[116:119], v[68:71], a[36:39], v[116:119]\n"
"v_rcp_f32_e64 v25, v25\n"
"v_mfma_f32_16x16x32_bf16 v[120:123], v[68:71], a[40:43], v[120:123]\n"
"v_rcp_f32_e64 v29, v29\n"
"v_mfma_f32_16x16x32_bf16 v[124:127], v[68:71], a[44:47], v[124:127]\n"
"buffer_store_dwordx4 v[196:199], v242, s[48:51], s93 offen sc1\n"
"v_add_f32 v16, 1.0, v16\n"
"v_add_f32 v20, 1.0, v20\n"
"v_add_f32_e64 v24, 1.0, v24\n"
"v_mfma_f32_16x16x32_bf16 v[128:131], v[68:71], a[48:51], v[128:131]\n"
"v_rcp_f32_e64 v18, v18\n"
"v_mfma_f32_16x16x32_bf16 v[132:135], v[68:71], a[52:55], v[132:135]\n"
"v_rcp_f32_e64 v22, v22\n"
"v_mfma_f32_16x16x32_bf16 v[136:139], v[68:71], a[56:59], v[136:139]\n"
"v_add_f32_e64 v28, 1.0, v28\n"
"v_cvt_pk_bf16_f32 v176, v16, v20\n"
"v_cvt_pk_bf16_f32 v177, v24, v28\n"
"ds_write_b64 v244, v[176:177] offset:16384\n"
"v_mfma_f32_16x16x32_bf16 v[140:143], v[68:71], a[60:63], v[140:143]\n"
"v_rcp_f32_e64 v26, v26\n"
"v_mfma_f32_16x16x32_bf16 v[112:115], v[72:75], a[64:67], v[112:115]\n"
"v_rcp_f32_e64 v30, v30\n"
"v_mfma_f32_16x16x32_bf16 v[116:119], v[72:75], a[68:71], v[116:119]\n"
"v_add_f32 v17, 1.0, v17\n"
"v_add_f32 v21, 1.0, v21\n"
"v_add_f32 v25, 1.0, v25\n"
"v_add_f32 v29, 1.0, v29\n"
"v_mfma_f32_16x16x32_bf16 v[120:123], v[72:75], a[72:75], v[120:123]\n"
"v_rcp_f32_e64 v19, v19\n"
"v_mfma_f32_16x16x32_bf16 v[124:127], v[72:75], a[76:79], v[124:127]\n"
"v_rcp_f32_e64 v23, v23\n"
"v_mfma_f32_16x16x32_bf16 v[128:131], v[72:75], a[80:83], v[128:131]\n"
"v_cvt_pk_bf16_f32 v178, v17, v21\n"
"v_cvt_pk_bf16_f32 v179, v25, v29\n"
"ds_write_b64 v245, v[178:179] offset:18432\n"
"v_add_f32_e64 v18, 1.0, v18\n"
"v_mfma_f32_16x16x32_bf16 v[132:135], v[72:75], a[84:87], v[132:135]\n"
"v_rcp_f32_e64 v27, v27\n"
"v_mfma_f32_16x16x32_bf16 v[136:139], v[72:75], a[88:91], v[136:139]\n"
"v_rcp_f32_e64 v31, v31\n"
"v_mfma_f32_16x16x32_bf16 v[140:143], v[72:75], a[92:95], v[140:143]\n"
"v_add_f32 v22, 1.0, v22\n"
"v_add_f32 v26, 1.0, v26\n"
"v_add_f32_e64 v30, 1.0, v30\n"
"v_cvt_pk_bf16_f32 v180, v18, v22\n"
"v_mfma_f32_16x16x32_bf16 v[112:115], v[76:79], a[96:99], v[112:115]\n"
"v_rcp_f32_e64 v32, v32\n"
"v_mfma_f32_16x16x32_bf16 v[116:119], v[76:79], a[100:103], v[116:119]\n"
"v_rcp_f32_e64 v36, v36\n"
"v_mfma_f32_16x16x32_bf16 v[120:123], v[76:79], a[104:107], v[120:123]\n"
"v_cvt_pk_bf16_f32 v181, v26, v30\n"
"ds_write_b64 v246, v[180:181] offset:20480\n"
"v_add_f32 v19, 1.0, v19\n"
"v_add_f32 v23, 1.0, v23\n"
"v_mfma_f32_16x16x32_bf16 v[124:127], v[76:79], a[108:111], v[124:127]\n"
"v_rcp_f32_e64 v40, v40\n"
"v_mfma_f32_16x16x32_bf16 v[128:131], v[76:79], a[112:115], v[128:131]\n"
"v_rcp_f32_e64 v44, v44\n"
"v_mfma_f32_16x16x32_bf16 v[132:135], v[76:79], a[116:119], v[132:135]\n"
"v_add_f32 v27, 1.0, v27\n"
"v_add_f32 v31, 1.0, v31\n"
"v_cvt_pk_bf16_f32 v182, v19, v23\n"
"v_mfma_f32_16x16x32_bf16 v[136:139], v[76:79], a[120:123], v[136:139]\n"
"v_rcp_f32_e64 v33, v33\n"
"v_mfma_f32_16x16x32_bf16 v[140:143], v[76:79], a[124:127], v[140:143]\n"
"v_rcp_f32_e64 v37, v37\n"
"v_mfma_f32_16x16x32_bf16 v[112:115], v[0:3], a[128:131], v[112:115]\n"
"v_cvt_pk_bf16_f32 v183, v27, v31\n"
"ds_write_b64 v247, v[182:183] offset:22528\n"
"v_add_f32_e64 v32, 1.0, v32\n"
"v_mfma_f32_16x16x32_bf16 v[116:119], v[0:3], a[132:135], v[116:119]\n"
"v_rcp_f32_e64 v41, v41\n"
"v_mfma_f32_16x16x32_bf16 v[120:123], v[0:3], a[136:139], v[120:123]\n"
"v_rcp_f32_e64 v45, v45\n"
"v_mfma_f32_16x16x32_bf16 v[124:127], v[0:3], a[140:143], v[124:127]\n"
"v_add_f32 v36, 1.0, v36\n"
"v_add_f32 v40, 1.0, v40\n"
"v_add_f32_e64 v44, 1.0, v44\n"
"v_mfma_f32_16x16x32_bf16 v[128:131], v[0:3], a[144:147], v[128:131]\n"
"v_rcp_f32_e64 v34, v34\n"
"v_mfma_f32_16x16x32_bf16 v[132:135], v[0:3], a[148:151], v[132:135]\n"
"v_rcp_f32_e64 v38, v38\n"
"v_mfma_f32_16x16x32_bf16 v[136:139], v[0:3], a[152:155], v[136:139]\n"
"v_cvt_pk_bf16_f32 v184, v32, v36\n"
"v_cvt_pk_bf16_f32 v185, v40, v44\n"
"ds_write_b64 v244, v[184:185] offset:24576\n"
"v_mfma_f32_16x16x32_bf16 v[140:143], v[0:3], a[156:159], v[140:143]\n"
"v_rcp_f32_e64 v42, v42\n"
"v_mfma_f32_16x16x32_bf16 v[112:115], v[4:7], a[160:163], v[112:115]\n"
"v_rcp_f32_e64 v46, v46\n"
"v_mfma_f32_16x16x32_bf16 v[116:119], v[4:7], a[164:167], v[116:119]\n"
"v_add_f32 v33, 1.0, v33\n"
"v_add_f32 v37, 1.0, v37\n"
"v_add_f32_e64 v41, 1.0, v41\n"
"v_mfma_f32_16x16x32_bf16 v[120:123], v[4:7], a[168:171], v[120:123]\n"
"v_rcp_f32_e64 v35, v35\n"
"v_mfma_f32_16x16x32_bf16 v[124:127], v[4:7], a[172:175], v[124:127]\n"
"v_rcp_f32_e64 v39, v39\n"
"v_mfma_f32_16x16x32_bf16 v[128:131], v[4:7], a[176:179], v[128:131]\n"
"v_add_f32_e64 v45, 1.0, v45\n"
"v_cvt_pk_bf16_f32 v186, v33, v37\n"
"v_cvt_pk_bf16_f32 v187, v41, v45\n"
"v_mfma_f32_16x16x32_bf16 v[132:135], v[4:7], a[180:183], v[132:135]\n"
"v_rcp_f32_e64 v43, v43\n"
"v_mfma_f32_16x16x32_bf16 v[136:139], v[4:7], a[184:187], v[136:139]\n"
"v_rcp_f32_e64 v47, v47\n"
"v_mfma_f32_16x16x32_bf16 v[140:143], v[4:7], a[188:191], v[140:143]\n"
"ds_write_b64 v245, v[186:187] offset:26624\n"
"v_add_f32 v34, 1.0, v34\n"
"v_add_f32 v38, 1.0, v38\n"
"v_mfma_f32_16x16x32_bf16 v[112:115], v[8:11], a[192:195], v[112:115]\n"
"v_add_f32 v42, 1.0, v42\n"
"v_add_f32 v46, 1.0, v46\n"
"v_cvt_pk_bf16_f32 v188, v34, v38\n"
"v_mfma_f32_16x16x32_bf16 v[116:119], v[8:11], a[196:199], v[116:119]\n"
"v_cvt_pk_bf16_f32 v189, v42, v46\n"
"ds_write_b64 v246, v[188:189] offset:28672\n"
"v_add_f32_e64 v35, 1.0, v35\n"
"v_mfma_f32_16x16x32_bf16 v[120:123], v[8:11], a[200:203], v[120:123]\n"
"v_add_f32 v39, 1.0, v39\n"
"v_add_f32 v43, 1.0, v43\n"
"v_add_f32_e64 v47, 1.0, v47\n"
"v_mfma_f32_16x16x32_bf16 v[124:127], v[8:11], a[204:207], v[124:127]\n"
"v_cvt_pk_bf16_f32 v190, v35, v39\n"
"v_cvt_pk_bf16_f32 v191, v43, v47\n"
"ds_write_b64 v247, v[190:191] offset:30720\n"
"v_mfma_f32_16x16x32_bf16 v[128:131], v[8:11], a[208:211], v[128:131]\n"
"v_mfma_f32_16x16x32_bf16 v[132:135], v[8:11], a[212:215], v[132:135]\n"
"s_waitcnt lgkmcnt(0)\n"
"s_barrier\n"
"v_mfma_f32_16x16x32_bf16 v[136:139], v[8:11], a[216:219], v[136:139]\n"
"ds_read_b128 v[48:51], v248\n"
"ds_read_b128 v[52:55], v249\n"
"v_mfma_f32_16x16x32_bf16 v[140:143], v[8:11], a[220:223], v[140:143]\n"
"ds_read_b128 v[56:59], v250\n"
"ds_read_b128 v[60:63], v251\n"
"v_mfma_f32_16x16x32_bf16 v[112:115], v[12:15], a[224:227], v[112:115]\n"
"ds_read_b128 v[64:67], v248 offset:256\n"
"ds_read_b128 v[68:71], v249 offset:256\n"
"v_mfma_f32_16x16x32_bf16 v[116:119], v[12:15], a[228:231], v[116:119]\n"
"ds_read_b128 v[72:75], v250 offset:256\n"
"ds_read_b128 v[76:79], v251 offset:256\n"
"v_mfma_f32_16x16x32_bf16 v[120:123], v[12:15], a[232:235], v[120:123]\n"
"v_cvt_pk_bf16_f32 v200, v82, v86\n"
"v_cvt_pk_bf16_f32 v201, v90, v94\n"
"v_mfma_f32_16x16x32_bf16 v[124:127], v[12:15], a[236:239], v[124:127]\n"
"v_cvt_pk_bf16_f32 v202, v98, v102\n"
"v_cvt_pk_bf16_f32 v203, v106, v110\n"
"v_mfma_f32_16x16x32_bf16 v[128:131], v[12:15], a[240:243], v[128:131]\n"
"s_xor_b32 s92, s9, 0x9000\n"
"buffer_store_dwordx4 v[200:203], v242, s[48:51], s92 offen sc1\n"
"v_mfma_f32_16x16x32_bf16 v[132:135], v[12:15], a[244:247], v[132:135]\n"
"v_cvt_pk_bf16_f32 v204, v83, v87\n"
"v_cvt_pk_bf16_f32 v205, v91, v95\n"
"v_mfma_f32_16x16x32_bf16 v[136:139], v[12:15], a[248:251], v[136:139]\n"
"v_cvt_pk_bf16_f32 v206, v99, v103\n"
"v_cvt_pk_bf16_f32 v207, v107, v111\n"
"v_mfma_f32_16x16x32_bf16 v[140:143], v[12:15], a[252:255], v[140:143]\n"
"s_xor_b32 s93, s9, 0x8000\n"
"buffer_store_dwordx4 v[204:207], v242, s[48:51], s93 offen sc1\n"
"s_waitcnt lgkmcnt(0)\n"
"s_nop 0\n"
"v_mfma_f32_16x16x32_bf16 v[80:83], v[48:51], a[0:3], 0\n"
"v_cvt_pk_bf16_f32 v192, v112, v116\n"
"v_cvt_pk_bf16_f32 v193, v120, v124\n"
"v_mfma_f32_16x16x32_bf16 v[84:87], v[48:51], a[4:7], 0\n"
"v_cvt_pk_bf16_f32 v194, v128, v132\n"
"v_cvt_pk_bf16_f32 v195, v136, v140\n"
"v_mfma_f32_16x16x32_bf16 v[88:91], v[48:51], a[8:11], 0\n"
"s_xor_b32 s92, s9, 0x2000\n"
"buffer_store_dwordx4 v[192:195], v242, s[48:51], s92 offen sc1\n"
"v_mfma_f32_16x16x32_bf16 v[92:95], v[48:51], a[12:15], 0\n"
"v_cvt_pk_bf16_f32 v196, v113, v117\n"
"v_cvt_pk_bf16_f32 v197, v121, v125\n"
"v_mfma_f32_16x16x32_bf16 v[96:99], v[48:51], a[16:19], 0\n"
"v_cvt_pk_bf16_f32 v198, v129, v133\n"
"v_cvt_pk_bf16_f32 v199, v137, v141\n"
"v_mfma_f32_16x16x32_bf16 v[100:103], v[48:51], a[20:23], 0\n"
"s_xor_b32 s93, s9, 0x3000\n"
"buffer_store_dwordx4 v[196:199], v242, s[48:51], s93 offen sc1\n"
"v_mfma_f32_16x16x32_bf16 v[104:107], v[48:51], a[24:27], 0\n"
"v_cvt_pk_bf16_f32 v200, v114, v118\n"
"v_cvt_pk_bf16_f32 v201, v122, v126\n"
"v_mfma_f32_16x16x32_bf16 v[108:111], v[48:51], a[28:31], 0\n"
"v_cvt_pk_bf16_f32 v202, v130, v134\n"
"v_cvt_pk_bf16_f32 v203, v138, v142\n"
"v_mfma_f32_16x16x32_bf16 v[80:83], v[52:55], a[32:35], v[80:83]\n"
"s_xor_b32 s92, s9, 0xb000\n"
"buffer_store_dwordx4 v[200:203], v242, s[48:51], s92 offen sc1\n"
"v_mfma_f32_16x16x32_bf16 v[84:87], v[52:55], a[36:39], v[84:87]\n"
"v_cvt_pk_bf16_f32 v204, v115, v119\n"
"v_cvt_pk_bf16_f32 v205, v123, v127\n"
"v_mfma_f32_16x16x32_bf16 v[88:91], v[52:55], a[40:43], v[88:91]\n"
"v_cvt_pk_bf16_f32 v206, v131, v135\n"
"v_cvt_pk_bf16_f32 v207, v139, v143\n"
"v_mfma_f32_16x16x32_bf16 v[92:95], v[52:55], a[44:47], v[92:95]\n"
"s_xor_b32 s93, s9, 0xa000\n"
"buffer_store_dwordx4 v[204:207], v242, s[48:51], s93 offen sc1\n"
"v_mfma_f32_16x16x32_bf16 v[96:99], v[52:55], a[48:51], v[96:99]\n"
"v_mfma_f32_16x16x32_bf16 v[100:103], v[52:55], a[52:55], v[100:103]\n"
"v_mfma_f32_16x16x32_bf16 v[104:107], v[52:55], a[56:59], v[104:107]\n"
"v_mfma_f32_16x16x32_bf16 v[108:111], v[52:55], a[60:63], v[108:111]\n"
"v_mfma_f32_16x16x32_bf16 v[80:83], v[56:59], a[64:67], v[80:83]\n"
"v_mfma_f32_16x16x32_bf16 v[84:87], v[56:59], a[68:71], v[84:87]\n"
"v_mfma_f32_16x16x32_bf16 v[88:91], v[56:59], a[72:75], v[88:91]\n"
"v_mfma_f32_16x16x32_bf16 v[92:95], v[56:59], a[76:79], v[92:95]\n"
"v_mfma_f32_16x16x32_bf16 v[96:99], v[56:59], a[80:83], v[96:99]\n"
"v_mfma_f32_16x16x32_bf16 v[100:103], v[56:59], a[84:87], v[100:103]\n"
"v_mfma_f32_16x16x32_bf16 v[104:107], v[56:59], a[88:91], v[104:107]\n"
"v_mfma_f32_16x16x32_bf16 v[108:111], v[56:59], a[92:95], v[108:111]\n"
"v_mfma_f32_16x16x32_bf16 v[80:83], v[60:63], a[96:99], v[80:83]\n"
"v_mfma_f32_16x16x32_bf16 v[84:87], v[60:63], a[100:103], v[84:87]\n"
"v_mfma_f32_16x16x32_bf16 v[88:91], v[60:63], a[104:107], v[88:91]\n"
"v_mfma_f32_16x16x32_bf16 v[92:95], v[60:63], a[108:111], v[92:95]\n"
"v_mfma_f32_16x16x32_bf16 v[96:99], v[60:63], a[112:115], v[96:99]\n"
"v_mfma_f32_16x16x32_bf16 v[100:103], v[60:63], a[116:119], v[100:103]\n"
"v_mfma_f32_16x16x32_bf16 v[104:107], v[60:63], a[120:123], v[104:107]\n"
"v_mfma_f32_16x16x32_bf16 v[108:111], v[60:63], a[124:127], v[108:111]\n"
"v_mfma_f32_16x16x32_bf16 v[80:83], v[64:67], a[128:131], v[80:83]\n"
"v_mfma_f32_16x16x32_bf16 v[84:87], v[64:67], a[132:135], v[84:87]\n"
"v_mfma_f32_16x16x32_bf16 v[88:91], v[64:67], a[136:139], v[88:91]\n"
"v_mfma_f32_16x16x32_bf16 v[92:95], v[64:67], a[140:143], v[92:95]\n"
"v_mfma_f32_16x16x32_bf16 v[96:99], v[64:67], a[144:147], v[96:99]\n"
"v_mfma_f32_16x16x32_bf16 v[100:103], v[64:67], a[148:151], v[100:103]\n"
"v_mfma_f32_16x16x32_bf16 v[104:107], v[64:67], a[152:155], v[104:107]\n"
"v_mfma_f32_16x16x32_bf16 v[108:111], v[64:67], a[156:159], v[108:111]\n"
"v_mfma_f32_16x16x32_bf16 v[80:83], v[68:71], a[160:163], v[80:83]\n"
"ds_read_b128 v[0:3], v248 offset:16384\n"
"v_mfma_f32_16x16x32_bf16 v[84:87], v[68:71], a[164:167], v[84:87]\n"
"v_mfma_f32_16x16x32_bf16 v[88:91], v[68:71], a[168:171], v[88:91]\n"
"ds_read_b128 v[4:7], v249 offset:16384\n"
"v_mfma_f32_16x16x32_bf16 v[92:95], v[68:71], a[172:175], v[92:95]\n"
"v_mfma_f32_16x16x32_bf16 v[96:99], v[68:71], a[176:179], v[96:99]\n"
"ds_read_b128 v[8:11], v250 offset:16384\n"
"v_mfma_f32_16x16x32_bf16 v[100:103], v[68:71], a[180:183], v[100:103]\n"
"v_mfma_f32_16x16x32_bf16 v[104:107], v[68:71], a[184:187], v[104:107]\n"
"ds_read_b128 v[12:15], v251 offset:16384\n"
"v_mfma_f32_16x16x32_bf16 v[108:111], v[68:71], a[188:191], v[108:111]\n"
"v_mfma_f32_16x16x32_bf16 v[80:83], v[72:75], a[192:195], v[80:83]\n"
"ds_read_b128 v[48:51], v248 offset:16640\n"
"v_mfma_f32_16x16x32_bf16 v[84:87], v[72:75], a[196:199], v[84:87]\n"
"v_mfma_f32_16x16x32_bf16 v[88:91], v[72:75], a[200:203], v[88:91]\n"
"ds_read_b128 v[52:55], v249 offset:16640\n"
"v_mfma_f32_16x16x32_bf16 v[92:95], v[72:75], a[204:207], v[92:95]\n"
"v_mfma_f32_16x16x32_bf16 v[96:99], v[72:75], a[208:211], v[96:99]\n"
"ds_read_b128 v[56:59], v250 offset:16640\n"
"v_mfma_f32_16x16x32_bf16 v[100:103], v[72:75], a[212:215], v[100:103]\n"
"v_mfma_f32_16x16x32_bf16 v[104:107], v[72:75], a[216:219], v[104:107]\n"
"ds_read_b128 v[60:63], v251 offset:16640\n"
"v_mfma_f32_16x16x32_bf16 v[108:111], v[72:75], a[220:223], v[108:111]\n"
"v_mfma_f32_16x16x32_bf16 v[80:83], v[76:79], a[224:227], v[80:83]\n"
"v_mfma_f32_16x16x32_bf16 v[84:87], v[76:79], a[228:231], v[84:87]\n"
"v_mfma_f32_16x16x32_bf16 v[88:91], v[76:79], a[232:235], v[88:91]\n"
"v_mfma_f32_16x16x32_bf16 v[92:95], v[76:79], a[236:239], v[92:95]\n"
"v_mfma_f32_16x16x32_bf16 v[96:99], v[76:79], a[240:243], v[96:99]\n"
"v_mfma_f32_16x16x32_bf16 v[100:103], v[76:79], a[244:247], v[100:103]\n"
"v_mfma_f32_16x16x32_bf16 v[104:107], v[76:79], a[248:251], v[104:107]\n"
"v_mfma_f32_16x16x32_bf16 v[108:111], v[76:79], a[252:255], v[108:111]\n"
"s_waitcnt lgkmcnt(0)\n"
"v_mfma_f32_16x16x32_bf16 v[112:115], v[0:3], a[0:3], 0\n"
"v_cvt_pk_bf16_f32 v192, v80, v84\n"
"s_nop 0\n"
"v_cvt_pk_bf16_f32 v193, v88, v92\n"
"v_mfma_f32_16x16x32_bf16 v[116:119], v[0:3], a[4:7], 0\n"
"v_cvt_pk_bf16_f32 v194, v96, v100\n"
"s_nop 0\n"
"s_nop 0\n"
"v_cvt_pk_bf16_f32 v195, v104, v108\n"
"v_mfma_f32_16x16x32_bf16 v[120:123], v[0:3], a[8:11], 0\n"
"s_xor_b32 s92, s9, 0x4000\n"
"buffer_store_dwordx4 v[192:195], v242, s[48:51], s92 offen sc1\n"
"v_mfma_f32_16x16x32_bf16 v[124:127], v[0:3], a[12:15], 0\n"
"v_cvt_pk_bf16_f32 v196, v81, v85\n"
"v_cvt_pk_bf16_f32 v197, v89, v93\n"
"v_mfma_f32_16x16x32_bf16 v[128:131], v[0:3], a[16:19], 0\n"
"v_cvt_pk_bf16_f32 v198, v97, v101\n"
"v_cvt_pk_bf16_f32 v199, v105, v109\n"
"v_mfma_f32_16x16x32_bf16 v[132:135], v[0:3], a[20:23], 0\n"
"s_xor_b32 s93, s9, 0x5000\n"
"buffer_store_dwordx4 v[196:199], v242, s[48:51], s93 offen sc1\n"
"v_mfma_f32_16x16x32_bf16 v[136:139], v[0:3], a[24:27], 0\n"
"v_cvt_pk_bf16_f32 v200, v82, v86\n"
"v_cvt_pk_bf16_f32 v201, v90, v94\n"
"v_mfma_f32_16x16x32_bf16 v[140:143], v[0:3], a[28:31], 0\n"
"v_cvt_pk_bf16_f32 v202, v98, v102\n"
"v_cvt_pk_bf16_f32 v203, v106, v110\n"
"v_mfma_f32_16x16x32_bf16 v[112:115], v[4:7], a[32:35], v[112:115]\n"
"s_xor_b32 s92, s9, 0xd000\n"
"buffer_store_dwordx4 v[200:203], v242, s[48:51], s92 offen sc1\n"
"v_mfma_f32_16x16x32_bf16 v[116:119], v[4:7], a[36:39], v[116:119]\n"
"v_cvt_pk_bf16_f32 v204, v83, v87\n"
"v_cvt_pk_bf16_f32 v205, v91, v95\n"
"v_mfma_f32_16x16x32_bf16 v[120:123], v[4:7], a[40:43], v[120:123]\n"
"v_cvt_pk_bf16_f32 v206, v99, v103\n"
"v_cvt_pk_bf16_f32 v207, v107, v111\n"
"v_mfma_f32_16x16x32_bf16 v[124:127], v[4:7], a[44:47], v[124:127]\n"
"s_xor_b32 s93, s9, 0xc000\n"
"buffer_store_dwordx4 v[204:207], v242, s[48:51], s93 offen sc1\n"
"v_mfma_f32_16x16x32_bf16 v[128:131], v[4:7], a[48:51], v[128:131]\n"
"v_mfma_f32_16x16x32_bf16 v[132:135], v[4:7], a[52:55], v[132:135]\n"
"v_mfma_f32_16x16x32_bf16 v[136:139], v[4:7], a[56:59], v[136:139]\n"
"v_mfma_f32_16x16x32_bf16 v[140:143], v[4:7], a[60:63], v[140:143]\n"
"v_mfma_f32_16x16x32_bf16 v[112:115], v[8:11], a[64:67], v[112:115]\n"
"v_mfma_f32_16x16x32_bf16 v[116:119], v[8:11], a[68:71], v[116:119]\n"
"v_mfma_f32_16x16x32_bf16 v[120:123], v[8:11], a[72:75], v[120:123]\n"
"v_mfma_f32_16x16x32_bf16 v[124:127], v[8:11], a[76:79], v[124:127]\n"
"v_mfma_f32_16x16x32_bf16 v[128:131], v[8:11], a[80:83], v[128:131]\n"
"v_mfma_f32_16x16x32_bf16 v[132:135], v[8:11], a[84:87], v[132:135]\n"
"v_mfma_f32_16x16x32_bf16 v[136:139], v[8:11], a[88:91], v[136:139]\n"
"v_mfma_f32_16x16x32_bf16 v[140:143], v[8:11], a[92:95], v[140:143]\n"
"v_mfma_f32_16x16x32_bf16 v[112:115], v[12:15], a[96:99], v[112:115]\n"
"v_mfma_f32_16x16x32_bf16 v[116:119], v[12:15], a[100:103], v[116:119]\n"
"v_mfma_f32_16x16x32_bf16 v[120:123], v[12:15], a[104:107], v[120:123]\n"
"v_mfma_f32_16x16x32_bf16 v[124:127], v[12:15], a[108:111], v[124:127]\n"
"v_mfma_f32_16x16x32_bf16 v[128:131], v[12:15], a[112:115], v[128:131]\n"
"v_mfma_f32_16x16x32_bf16 v[132:135], v[12:15], a[116:119], v[132:135]\n"
"v_mfma_f32_16x16x32_bf16 v[136:139], v[12:15], a[120:123], v[136:139]\n"
"v_mfma_f32_16x16x32_bf16 v[140:143], v[12:15], a[124:127], v[140:143]\n"
"v_mfma_f32_16x16x32_bf16 v[112:115], v[48:51], a[128:131], v[112:115]\n"
"v_mfma_f32_16x16x32_bf16 v[116:119], v[48:51], a[132:135], v[116:119]\n"
"v_mfma_f32_16x16x32_bf16 v[120:123], v[48:51], a[136:139], v[120:123]\n"
"v_mfma_f32_16x16x32_bf16 v[124:127], v[48:51], a[140:143], v[124:127]\n"
"v_mfma_f32_16x16x32_bf16 v[128:131], v[48:51], a[144:147], v[128:131]\n"
"v_mfma_f32_16x16x32_bf16 v[132:135], v[48:51], a[148:151], v[132:135]\n"
"v_mfma_f32_16x16x32_bf16 v[136:139], v[48:51], a[152:155], v[136:139]\n"
"v_mfma_f32_16x16x32_bf16 v[140:143], v[48:51], a[156:159], v[140:143]\n"
"v_mfma_f32_16x16x32_bf16 v[112:115], v[52:55], a[160:163], v[112:115]\n"
"v_mfma_f32_16x16x32_bf16 v[116:119], v[52:55], a[164:167], v[116:119]\n"
"v_mfma_f32_16x16x32_bf16 v[120:123], v[52:55], a[168:171], v[120:123]\n"
"v_mfma_f32_16x16x32_bf16 v[124:127], v[52:55], a[172:175], v[124:127]\n"
"v_mfma_f32_16x16x32_bf16 v[128:131], v[52:55], a[176:179], v[128:131]\n"
"v_mfma_f32_16x16x32_bf16 v[132:135], v[52:55], a[180:183], v[132:135]\n"
"v_mfma_f32_16x16x32_bf16 v[136:139], v[52:55], a[184:187], v[136:139]\n"
"v_mfma_f32_16x16x32_bf16 v[140:143], v[52:55], a[188:191], v[140:143]\n"
"v_mfma_f32_16x16x32_bf16 v[112:115], v[56:59], a[192:195], v[112:115]\n"
"v_mfma_f32_16x16x32_bf16 v[116:119], v[56:59], a[196:199], v[116:119]\n"
"v_mfma_f32_16x16x32_bf16 v[120:123], v[56:59], a[200:203], v[120:123]\n"
"v_mfma_f32_16x16x32_bf16 v[124:127], v[56:59], a[204:207], v[124:127]\n"
"v_mfma_f32_16x16x32_bf16 v[128:131], v[56:59], a[208:211], v[128:131]\n"
"v_mfma_f32_16x16x32_bf16 v[132:135], v[56:59], a[212:215], v[132:135]\n"
"v_mfma_f32_16x16x32_bf16 v[136:139], v[56:59], a[216:219], v[136:139]\n"
"v_mfma_f32_16x16x32_bf16 v[140:143], v[56:59], a[220:223], v[140:143]\n"
"v_mfma_f32_16x16x32_bf16 v[112:115], v[60:63], a[224:227], v[112:115]\n"
"v_mfma_f32_16x16x32_bf16 v[116:119], v[60:63], a[228:231], v[116:119]\n"
"v_mfma_f32_16x16x32_bf16 v[120:123], v[60:63], a[232:235], v[120:123]\n"
"v_mfma_f32_16x16x32_bf16 v[124:127], v[60:63], a[236:239], v[124:127]\n"
"v_mfma_f32_16x16x32_bf16 v[128:131], v[60:63], a[240:243], v[128:131]\n"
"v_mfma_f32_16x16x32_bf16 v[132:135], v[60:63], a[244:247], v[132:135]\n"
"v_mfma_f32_16x16x32_bf16 v[136:139], v[60:63], a[248:251], v[136:139]\n"
"v_mfma_f32_16x16x32_bf16 v[140:143], v[60:63], a[252:255], v[140:143]\n"
"s_nop 1\n"
"v_cvt_pk_bf16_f32 v192, v112, v116\n"
"s_nop 0\n"
"v_cvt_pk_bf16_f32 v193, v120, v124\n"
"s_nop 0\n"
"v_cvt_pk_bf16_f32 v194, v128, v132\n"
"s_nop 0\n"
"v_cvt_pk_bf16_f32 v195, v136, v140\n"
"s_xor_b32 s92, s9, 0x6000\n"
"buffer_store_dwordx4 v[192:195], v242, s[48:51], s92 offen sc1\n"
"v_cvt_pk_bf16_f32 v196, v113, v117\n"
"v_cvt_pk_bf16_f32 v197, v121, v125\n"
"v_cvt_pk_bf16_f32 v198, v129, v133\n"
"v_cvt_pk_bf16_f32 v199, v137, v141\n"
"s_xor_b32 s93, s9, 0x7000\n"
"buffer_store_dwordx4 v[196:199], v242, s[48:51], s93 offen sc1\n"
"v_cvt_pk_bf16_f32 v200, v114, v118\n"
"v_cvt_pk_bf16_f32 v201, v122, v126\n"
"v_cvt_pk_bf16_f32 v202, v130, v134\n"
"v_cvt_pk_bf16_f32 v203, v138, v142\n"
"s_xor_b32 s92, s9, 0xf000\n"
"buffer_store_dwordx4 v[200:203], v242, s[48:51], s92 offen sc1\n"
"v_cvt_pk_bf16_f32 v204, v115, v119\n"
"v_cvt_pk_bf16_f32 v205, v123, v127\n"
"v_cvt_pk_bf16_f32 v206, v131, v135\n"
"v_cvt_pk_bf16_f32 v207, v139, v143\n"
"s_xor_b32 s93, s9, 0xe000\n"
"buffer_store_dwordx4 v[204:207], v242, s[48:51], s93 offen sc1\n"
"s_endpgm\n"
".LheadB:\n"
"v_lshlrev_b32 v15, 7, v14\n"
"s_mul_i32 s62, s63, 0x1400\n"
"s_add_u32 s62, s62, 0x40000\n"
"v_add_u32 v241, s62, v15\n"
"v_cmp_lt_u32 vcc, 39, v14\n"
"s_nop 1\n"
"v_cndmask_b32 v241, v241, v12, vcc\n"
"v_mov_b32_e64 v12, v13\n"
"s_and_b32 s29, s29, 0xffff\n"
"s_mov_b32 s30, 0x200000\n"
"s_mov_b32 s31, 0x20000\n"
"s_mov_b32 s58, s57\n"
"s_lshl_b32 s36, s58, 3\n"
"s_add_u32 s36, s36, 0x80\n"
"s_lshl_b32 s37, s58, 2\n"
"s_add_u32 s37, s37, 0x2000\n"
"buffer_load_dword v242, v4, s[28:31], s37 offen sc1\n"
"s_lshl_b32 s58, s57, 17\n"
"s_add_u32 s48, s18, s58\n"
"s_addc_u32 s49, s19, 0\n"
"s_mov_b32 s96, 0\n"
"s_nop 0\n"
"buffer_load_dwordx4 v[48:51], v1, s[24:27], s96 offen\n"
"s_and_b32 s49, s49, 0xffff\n"
"buffer_load_dwordx4 v[52:55], v1, s[24:27], s96 offen offset:512\n"
"s_mov_b32 s50, 0x20000\n"
"buffer_load_dwordx4 v[56:59], v1, s[24:27], s96 offen offset:1024\n"
"s_mov_b32 s51, 0x20000\n"
"buffer_load_dwordx4 v[60:63], v1, s[24:27], s96 offen offset:1536\n"
"s_add_u32 s38, s38, 0x22000\n"
"s_cmp_eq_u32 s54, 0\n"
"s_cselect_b32 s4, 1, 0\n"
"s_cselect_b32 s5, 2, 0\n"
"buffer_load_dwordx4 v[64:67], v1, s[24:27], s96 offen offset:2048\n"
"s_lshl_b32 s58, s55, 1\n"
"buffer_load_dwordx4 v[68:71], v1, s[24:27], s96 offen offset:2560\n"
"s_add_u32 s58, s58, s56\n"
"buffer_load_dwordx4 v[72:75], v1, s[24:27], s96 offen offset:3072\n"
"s_lshl_b32 s95, s58, 12\n"
"buffer_load_dwordx4 v[76:79], v1, s[24:27], s96 offen offset:3584\n"
"v_lshl_add_u32 v7, v14, 4, s95\n"
"s_mov_b32 s97, 0x8000\n"
"buffer_load_dwordx4 v[80:83], v1, s[24:27], s97 offen\n"
"s_lshl_b32 s58, s54, 14\n"
"s_add_u32 s99, s58, 0x12000\n"
"v_lshl_add_u32 v6, v14, 4, s99\n"
"s_lshl_b32 s58, s56, 2\n"
"v_add_u32_e64 v14, s58, v12\n"
"s_mov_b32 s52, 0x5040100\n"
"s_mov_b32 s53, 0x7060302\n"
"s_mov_b32 s60, 0xbdd2d3e8\n"
"s_mov_b32 s61, 0xc0135761\n"
"v_lshlrev_b32 v15, 1, v2\n"
"v_subrev_u32 v13, 7, v15\n"
"v_cmp_lt_u32 vcc, 3, v2\n"
"v_subrev_u32 v11, 16, v15\n"
"s_nop 0\n"
"v_cndmask_b32 v15, v15, v13, vcc\n"
"v_cmp_lt_u32 vcc, 11, v2\n"
"s_nop 1\n"
"v_cndmask_b32 v15, v15, v11, vcc\n"
"s_lshl_b32 s58, s56, 14\n"
"s_xor_b32 s99, s58, 0x4000\n"
"v_add_u32 v11, 1, v15\n"
"v_and_b32 v11, 15, v11\n"
"v_xor_b32 v11, v11, v14\n"
"v_lshlrev_b32 v11, 4, v11\n"
"v_lshl_add_u32 v11, v15, 9, v11\n"
"v_add_u32 v11, 0x200, v11\n"
"v_add_u32 v244, s58, v11\n"
"v_add_u32 v248, s99, v11\n"
"v_xor_b32 v245, 0x80, v244\n"
"v_xor_b32 v249, 0x80, v248\n"
"v_add_u32 v11, 2, v15\n"
"v_and_b32 v11, 15, v11\n"
"v_xor_b32 v11, v11, v14\n"
"v_lshlrev_b32 v11, 4, v11\n"
"v_lshl_add_u32 v11, v15, 9, v11\n"
"v_add_u32 v11, 0x200, v11\n"
"v_add_u32 v246, s58, v11\n"
"v_add_u32 v250, s99, v11\n"
"v_xor_b32 v247, 0x80, v246\n"
"v_xor_b32 v251, 0x80, v250\n"
"v_mul_u32_u24 v13, 0x210, v15\n"
"v_lshl_add_u32 v13, v12, 4, v13\n"
"v_lshrrev_b32 v14, 1, v3\n"
"v_xor_b32 v15, v3, v14\n"
"v_and_b32_e64 v15, 1, v15\n"
"v_lshl_add_u32 v15, v14, 3, v15\n"
"s_lshl_b32 s58, s55, 8\n"
"v_lshl_add_u32 v14, v2, 4, s58\n"
"s_mov_b32 s40, s28\n"
"s_mov_b32 s41, s29\n"
"s_add_u32 s92, s93, 0x1000\n"
"s_lshl_b32 s6, s57, 10\n"
"s_add_u32 s6, s6, 0x100000\n"
"s_add_u32 s7, s6, 0x400\n"
"v_and_b32 v252, 63, v0\n"
"v_lshlrev_b32 v252, 4, v252\n"
"v_mov_b32 v44, 0\n"
"v_mov_b32 v45, 0\n"
"v_mov_b32 v46, 0\n"
"v_mov_b32 v47, 0\n"
"v_lshlrev_b32_e64 v253, 4, v0\n"
"v_mov_b32 v254, 0x7ffffff0\n"
"v_cmp_lt_u32 vcc, 63, v0\n"
"s_nop 1\n"
"v_cndmask_b32 v253, v253, v254, vcc\n"
"buffer_store_dwordx4 v[44:47], v253, s[28:31], s6 offen sc1\n"
"s_waitcnt vmcnt(12)\n"
"v_perm_b32 v16, v212, v208, s52\n"
"v_perm_b32 v17, v220, v216, s52\n"
"v_perm_b32 v18, v228, v224, s52\n"
"v_perm_b32 v19, v236, v232, s52\n"
"buffer_store_dwordx4 v[16:19], v7, s[40:43], s93 offen sc1\n"
"v_perm_b32 v20, v212, v208, s53\n"
"v_perm_b32 v21, v220, v216, s53\n"
"v_perm_b32 v22, v228, v224, s53\n"
"v_perm_b32 v23, v236, v232, s53\n"
"buffer_store_dwordx4 v[20:23], v7, s[40:43], s93 offen offset:1024 sc1\n"
"v_perm_b32 v24, v213, v209, s52\n"
"v_perm_b32 v25, v221, v217, s52\n"
"v_perm_b32 v26, v229, v225, s52\n"
"v_perm_b32 v27, v237, v233, s52\n"
"buffer_store_dwordx4 v[24:27], v7, s[40:43], s93 offen offset:2048 sc1\n"
"v_perm_b32 v28, v213, v209, s53\n"
"v_perm_b32 v29, v221, v217, s53\n"
"v_perm_b32 v30, v229, v225, s53\n"
"v_perm_b32 v31, v237, v233, s53\n"
"buffer_store_dwordx4 v[28:31], v7, s[40:43], s93 offen offset:3072 sc1\n"
"v_perm_b32 v32, v214, v210, s52\n"
"v_perm_b32 v33, v222, v218, s52\n"
"v_perm_b32 v34, v230, v226, s52\n"
"v_perm_b32 v35, v238, v234, s52\n"
"buffer_store_dwordx4 v[32:35], v7, s[40:43], s92 offen sc1\n"
"v_perm_b32 v36, v214, v210, s53\n"
"v_perm_b32 v37, v222, v218, s53\n"
"v_perm_b32 v38, v230, v226, s53\n"
"v_perm_b32 v39, v238, v234, s53\n"
"buffer_store_dwordx4 v[36:39], v7, s[40:43], s92 offen offset:1024 sc1\n"
"v_perm_b32 v40, v215, v211, s52\n"
"v_perm_b32 v41, v223, v219, s52\n"
"v_perm_b32 v42, v231, v227, s52\n"
"v_perm_b32 v43, v239, v235, s52\n"
"buffer_store_dwordx4 v[40:43], v7, s[40:43], s92 offen offset:2048 sc1\n"
"v_perm_b32 v44, v215, v211, s53\n"
"v_perm_b32 v45, v223, v219, s53\n"
"v_perm_b32 v46, v231, v227, s53\n"
"v_perm_b32 v47, v239, v235, s53\n"
"buffer_store_dwordx4 v[44:47], v7, s[40:43], s92 offen offset:3072 sc1\n"
"buffer_load_dwordx4 v[84:87], v1, s[24:27], s97 offen offset:512\n"
"buffer_load_dwordx4 v[88:91], v1, s[24:27], s97 offen offset:1024\n"
"buffer_load_dwordx4 v[92:95], v1, s[24:27], s97 offen offset:1536\n"
"buffer_load_dwordx4 v[96:99], v1, s[24:27], s97 offen offset:2048\n"
"buffer_load_dwordx4 v[100:103], v1, s[24:27], s97 offen offset:2560\n"
"buffer_load_dwordx4 v[104:107], v1, s[24:27], s97 offen offset:3072\n"
"buffer_load_dwordx4 v[108:111], v1, s[24:27], s97 offen offset:3584\n"
"buffer_load_dword v243, v4, s[28:31], 0 offen sc1\n"
"s_waitcnt vmcnt(26)\n"
"v_add_u32 v242, 1, v242\n"
"s_mov_b32 s96, 0x20000\n"
"buffer_load_dwordx4 v[112:115], v1, s[24:27], s96 offen\n"
"buffer_load_dwordx4 v[116:119], v1, s[24:27], s96 offen offset:512\n"
"buffer_load_dwordx4 v[120:123], v1, s[24:27], s96 offen offset:1024\n"
"buffer_load_dwordx4 v[124:127], v1, s[24:27], s96 offen offset:1536\n"
"buffer_load_dwordx4 v[128:131], v1, s[24:27], s96 offen offset:2048\n"
"buffer_load_dwordx4 v[132:135], v1, s[24:27], s96 offen offset:2560\n"
"buffer_load_dwordx4 v[136:139], v1, s[24:27], s96 offen offset:3072\n"
"buffer_load_dwordx4 v[140:143], v1, s[24:27], s96 offen offset:3584\n"
"s_mov_b32 s97, 0x28000\n"
"buffer_load_dwordx4 v[144:147], v1, s[24:27], s97 offen\n"
"buffer_load_dwordx4 v[148:151], v1, s[24:27], s97 offen offset:512\n"
"buffer_load_dwordx4 v[152:155], v1, s[24:27], s97 offen offset:1024\n"
"s_waitcnt vmcnt(11)\n"
"s_cmp_lg_u32 s54, 0\n"
"s_cbranch_scc1 .LBspin_nd\n"
"s_waitcnt vmcnt(0)\n"
".LBspin_nd:\n"
"buffer_load_dwordx4 v[156:159], v1, s[24:27], s97 offen offset:1536\n"
"buffer_store_dwordx2 v[242:243], v4, s[40:43], s98 offen sc1\n"
"buffer_store_dwordx2 v[242:243], v4, s[28:31], s36 offen sc1\n"
"buffer_load_dwordx4 v[160:163], v1, s[24:27], s97 offen offset:2048\n"
"buffer_load_dwordx4 v[164:167], v1, s[24:27], s97 offen offset:2560\n"
"buffer_load_dwordx4 v[168:171], v1, s[24:27], s97 offen offset:3072\n"
"buffer_load_dwordx4 v[172:175], v1, s[24:27], s97 offen offset:3584\n"
"s_mov_b32 s96, 0x40000\n"
"buffer_load_dwordx4 v[176:179], v1, s[24:27], s96 offen\n"
"buffer_load_dwordx4 v[180:183], v1, s[24:27], s96 offen offset:512\n"
"buffer_load_dwordx4 v[184:187], v1, s[24:27], s96 offen offset:1024\n"
"buffer_load_dwordx4 v[188:191], v1, s[24:27], s96 offen offset:1536\n"
"buffer_load_dwordx4 v[192:195], v1, s[24:27], s96 offen offset:2048\n"
"buffer_load_dwordx4 v[196:199], v1, s[24:27], s96 offen offset:2560\n"
"buffer_load_dwordx4 v[200:203], v1, s[24:27], s96 offen offset:3072\n"
"buffer_load_dwordx4 v[204:207], v1, s[24:27], s96 offen offset:3584\n"
"s_mov_b32 s97, 0x48000\n"
"buffer_load_dwordx4 v[208:211], v1, s[24:27], s97 offen\n"
"buffer_load_dwordx4 v[212:215], v1, s[24:27], s97 offen offset:512\n"
"buffer_load_dwordx4 v[216:219], v1, s[24:27], s97 offen offset:1024\n"
"buffer_load_dwordx4 v[220:223], v1, s[24:27], s97 offen offset:1536\n"
"buffer_load_dwordx4 v[224:227], v1, s[24:27], s97 offen offset:2048\n"
"buffer_load_dwordx4 v[228:231], v1, s[24:27], s97 offen offset:2560\n"
"buffer_load_dwordx4 v[232:235], v1, s[24:27], s97 offen offset:3072\n"
"buffer_load_dwordx4 v[236:239], v1, s[24:27], s97 offen offset:3584\n"
"s_add_u32 m0, s91, 0\n"
"s_mov_b32 s90, 0\n"
"buffer_load_dwordx4 v10, s[20:23], s90 offen nt lds\n"
"s_add_u32 m0, s91, 0x1000\n"
"s_mov_b32 s90, 0x2000\n"
"buffer_load_dwordx4 v9, s[20:23], s90 offen nt lds\n"
"s_add_u32 m0, s91, 0x2000\n"
"s_mov_b32 s90, 0x4000\n"
"buffer_load_dwordx4 v8, s[20:23], s90 offen nt lds\n"
"s_add_u32 m0, s91, 0x3000\n"
"s_mov_b32 s90, 0x6000\n"
"buffer_load_dwordx4 v9, s[20:23], s90 offen nt lds\n"
"s_add_u32 m0, s91, 0x4000\n"
"s_mov_b32 s90, 0x8000\n"
"buffer_load_dwordx4 v8, s[20:23], s90 offen nt lds\n"
"s_add_u32 m0, s91, 0x5000\n"
"s_mov_b32 s90, 0xa000\n"
"buffer_load_dwordx4 v9, s[20:23], s90 offen nt lds\n"
"s_add_u32 m0, s91, 0x6000\n"
"s_mov_b32 s90, 0xc000\n"
"buffer_load_dwordx4 v8, s[20:23], s90 offen nt lds\n"
"s_add_u32 m0, s91, 0x7000\n"
"s_mov_b32 s90, 0xe000\n"
"buffer_load_dwordx4 v9, s[20:23], s90 offen nt lds\n"
"s_add_u32 m0, s91, 0x8000\n"
"s_mov_b32 s90, 0x10000\n"
"buffer_load_dwordx4 v8, s[20:23], s90 offen nt lds\n"
"buffer_load_dword v5, v4, s[28:31], 0 offen sc1\n"
"s_add_u32 m0, s91, 0x9000\n"
"s_mov_b32 s90, 0x100\n"
"buffer_load_dwordx4 v10, s[20:23], s90 offen nt lds\n"
"s_add_u32 m0, s91, 0xa000\n"
"s_mov_b32 s90, 0x2100\n"
"buffer_load_dwordx4 v9, s[20:23], s90 offen nt lds\n"
"s_add_u32 m0, s91, 0xb000\n"
"s_mov_b32 s90, 0x4100\n"
"buffer_load_dwordx4 v8, s[20:23], s90 offen nt lds\n"
"s_add_u32 m0, s91, 0xc000\n"
"s_mov_b32 s90, 0x6100\n"
"buffer_load_dwordx4 v9, s[20:23], s90 offen nt lds\n"
"s_add_u32 m0, s91, 0xd000\n"
"s_mov_b32 s90, 0x8100\n"
"buffer_load_dwordx4 v8, s[20:23], s90 offen nt lds\n"
"s_add_u32 m0, s91, 0xe000\n"
"s_mov_b32 s90, 0xa100\n"
"buffer_load_dwordx4 v9, s[20:23], s90 offen nt lds\n"
"s_add_u32 m0, s91, 0xf000\n"
"s_mov_b32 s90, 0xc100\n"
"buffer_load_dwordx4 v8, s[20:23], s90 offen nt lds\n"
"s_add_u32 m0, s91, 0x10000\n"
"s_mov_b32 s90, 0xe100\n"
"buffer_load_dwordx4 v9, s[20:23], s90 offen nt lds\n"
"s_add_u32 m0, s91, 0x11000\n"
"s_mov_b32 s90, 0x10100\n"
"buffer_load_dwordx4 v8, s[20:23], s90 offen nt lds\n"
"s_waitcnt vmcnt(10)\n"
"s_barrier\n"
"ds_read_b128 a[168:171], v248\n"
"ds_read_b128 a[172:175], v248 offset:8192\n"
"ds_read_b128 a[160:163], v244\n"
"ds_read_b128 a[164:167], v244 offset:8192\n"
"v_perm_b32 v16, v52, v48, s52\n"
"v_perm_b32 v17, v60, v56, s52\n"
"v_perm_b32 v18, v68, v64, s52\n"
"v_perm_b32 v19, v76, v72, s52\n"
"v_perm_b32 v20, v52, v48, s53\n"
"v_perm_b32 v21, v60, v56, s53\n"
"v_perm_b32 v22, v68, v64, s53\n"
"v_perm_b32 v23, v76, v72, s53\n"
"v_perm_b32 v24, v53, v49, s52\n"
"v_perm_b32 v25, v61, v57, s52\n"
"v_perm_b32 v26, v69, v65, s52\n"
"v_perm_b32 v27, v77, v73, s52\n"
"v_perm_b32 v28, v53, v49, s53\n"
"v_perm_b32 v29, v61, v57, s53\n"
"v_perm_b32 v30, v69, v65, s53\n"
"v_perm_b32 v31, v77, v73, s53\n"
"v_perm_b32 v32, v54, v50, s52\n"
"v_perm_b32 v33, v62, v58, s52\n"
"v_perm_b32 v34, v70, v66, s52\n"
"v_perm_b32 v35, v78, v74, s52\n"
"v_perm_b32 v36, v54, v50, s53\n"
"v_perm_b32 v37, v62, v58, s53\n"
"v_perm_b32 v38, v70, v66, s53\n"
"v_perm_b32 v39, v78, v74, s53\n"
"v_perm_b32 v40, v55, v51, s52\n"
"v_perm_b32 v41, v63, v59, s52\n"
"v_perm_b32 v42, v71, v67, s52\n"
"v_perm_b32 v43, v79, v75, s52\n"
"s_waitcnt lgkmcnt(0)\n"
"s_nop 0\n"
"v_mfma_f32_16x16x32_bf16 a[64:67], a[168:171], v[16:19], 0\n"
"v_perm_b32 v44, v55, v51, s53\n"
"v_mfma_f32_16x16x32_bf16 a[96:99], a[172:175], v[16:19], 0\n"
"v_perm_b32 v45, v63, v59, s53\n"
"v_mfma_f32_16x16x32_bf16 a[0:3], a[160:163], v[16:19], 0\n"
"v_perm_b32 v46, v71, v67, s53\n"
"v_mfma_f32_16x16x32_bf16 a[32:35], a[164:167], v[16:19], 0\n"
"v_perm_b32 v47, v79, v75, s53\n"
"s_mov_b32 s96, 0x10000\n"
"buffer_load_dwordx4 v[48:51], v1, s[24:27], s96 offen\n"
"v_mfma_f32_16x16x32_bf16 a[68:71], a[168:171], v[20:23], 0\n"
"buffer_load_dwordx4 v[52:55], v1, s[24:27], s96 offen offset:512\n"
"ds_read_b128 a[188:191], v249\n"
"buffer_load_dwordx4 v[56:59], v1, s[24:27], s96 offen offset:1024\n"
"v_mfma_f32_16x16x32_bf16 a[100:103], a[172:175], v[20:23], 0\n"
"buffer_load_dwordx4 v[60:63], v1, s[24:27], s96 offen offset:1536\n"
"v_mfma_f32_16x16x32_bf16 a[4:7], a[160:163], v[20:23], 0\n"
"ds_read_b128 a[192:195], v249 offset:8192\n"
"v_mfma_f32_16x16x32_bf16 a[36:39], a[164:167], v[20:23], 0\n"
"v_mfma_f32_16x16x32_bf16 a[72:75], a[168:171], v[24:27], 0\n"
"ds_read_b128 a[180:183], v245\n"
"v_mfma_f32_16x16x32_bf16 a[104:107], a[172:175], v[24:27], 0\n"
"v_mfma_f32_16x16x32_bf16 a[8:11], a[160:163], v[24:27], 0\n"
"ds_read_b128 a[184:187], v245 offset:8192\n"
"v_mfma_f32_16x16x32_bf16 a[40:43], a[164:167], v[24:27], 0\n"
"v_mfma_f32_16x16x32_bf16 a[76:79], a[168:171], v[28:31], 0\n"
"v_mfma_f32_16x16x32_bf16 a[108:111], a[172:175], v[28:31], 0\n"
"v_mfma_f32_16x16x32_bf16 a[12:15], a[160:163], v[28:31], 0\n"
"buffer_load_dwordx4 v[64:67], v1, s[24:27], s96 offen offset:2048\n"
"v_mfma_f32_16x16x32_bf16 a[44:47], a[164:167], v[28:31], 0\n"
"v_mfma_f32_16x16x32_bf16 a[80:83], a[168:171], v[32:35], 0\n"
"v_mfma_f32_16x16x32_bf16 a[112:115], a[172:175], v[32:35], 0\n"
"v_mfma_f32_16x16x32_bf16 a[16:19], a[160:163], v[32:35], 0\n"
"v_mfma_f32_16x16x32_bf16 a[48:51], a[164:167], v[32:35], 0\n"
"v_mfma_f32_16x16x32_bf16 a[84:87], a[168:171], v[36:39], 0\n"
"v_mfma_f32_16x16x32_bf16 a[116:119], a[172:175], v[36:39], 0\n"
"v_mfma_f32_16x16x32_bf16 a[20:23], a[160:163], v[36:39], 0\n"
"v_mfma_f32_16x16x32_bf16 a[52:55], a[164:167], v[36:39], 0\n"
"v_mfma_f32_16x16x32_bf16 a[88:91], a[168:171], v[40:43], 0\n"
"v_mfma_f32_16x16x32_bf16 a[120:123], a[172:175], v[40:43], 0\n"
"v_mfma_f32_16x16x32_bf16 a[24:27], a[160:163], v[40:43], 0\n"
"buffer_load_dwordx4 v[68:71], v1, s[24:27], s96 offen offset:2560\n"
"v_mfma_f32_16x16x32_bf16 a[56:59], a[164:167], v[40:43], 0\n"
"v_mfma_f32_16x16x32_bf16 a[92:95], a[168:171], v[44:47], 0\n"
"v_mfma_f32_16x16x32_bf16 a[124:127], a[172:175], v[44:47], 0\n"
"v_mfma_f32_16x16x32_bf16 a[28:31], a[160:163], v[44:47], 0\n"
"v_mfma_f32_16x16x32_bf16 a[60:63], a[164:167], v[44:47], 0\n"
"v_perm_b32 v16, v84, v80, s52\n"
"v_perm_b32 v17, v92, v88, s52\n"
"v_perm_b32 v18, v100, v96, s52\n"
"v_perm_b32 v19, v108, v104, s52\n"
"v_perm_b32 v20, v84, v80, s53\n"
"v_perm_b32 v21, v92, v88, s53\n"
"v_perm_b32 v22, v100, v96, s53\n"
"v_perm_b32 v23, v108, v104, s53\n"
"v_perm_b32 v24, v85, v81, s52\n"
"v_perm_b32 v25, v93, v89, s52\n"
"v_perm_b32 v26, v101, v97, s52\n"
"v_perm_b32 v27, v109, v105, s52\n"
"v_perm_b32 v28, v85, v81, s53\n"
"v_perm_b32 v29, v93, v89, s53\n"
"v_perm_b32 v30, v101, v97, s53\n"
"v_perm_b32 v31, v109, v105, s53\n"
"v_perm_b32 v32, v86, v82, s52\n"
"v_perm_b32 v33, v94, v90, s52\n"
"v_perm_b32 v34, v102, v98, s52\n"
"v_perm_b32 v35, v110, v106, s52\n"
"v_perm_b32 v36, v86, v82, s53\n"
"v_perm_b32 v37, v94, v90, s53\n"
"buffer_load_dwordx4 v[72:75], v1, s[24:27], s96 offen offset:3072\n"
"v_perm_b32 v38, v102, v98, s53\n"
"v_perm_b32 v39, v110, v106, s53\n"
"v_perm_b32 v40, v87, v83, s52\n"
"v_perm_b32 v41, v95, v91, s52\n"
"v_perm_b32 v42, v103, v99, s52\n"
"v_perm_b32 v43, v111, v107, s52\n"
"s_waitcnt lgkmcnt(0)\n"
"s_nop 0\n"
"v_mfma_f32_16x16x32_bf16 a[64:67], a[188:191], v[16:19], a[64:67]\n"
"v_perm_b32 v44, v87, v83, s53\n"
"v_perm_b32 v45, v95, v91, s53\n"
"v_mfma_f32_16x16x32_bf16 a[96:99], a[192:195], v[16:19], a[96:99]\n"
"v_perm_b32 v46, v103, v99, s53\n"
"v_mfma_f32_16x16x32_bf16 a[0:3], a[180:183], v[16:19], a[0:3]\n"
"v_perm_b32 v47, v111, v107, s53\n"
"s_waitcnt vmcnt(16)\n"
"v_mov_b32 v243, v5\n"
"v_mfma_f32_16x16x32_bf16 a[32:35], a[184:187], v[16:19], a[32:35]\n"
"buffer_store_dwordx2 v[242:243], v4, s[40:43], s98 offen sc1\n"
"buffer_store_dwordx2 v[242:243], v4, s[28:31], s36 offen sc1\n"
"v_mfma_f32_16x16x32_bf16 a[68:71], a[188:191], v[20:23], a[68:71]\n"
"ds_read_b128 a[168:171], v248 offset:256\n"
"v_mfma_f32_16x16x32_bf16 a[100:103], a[192:195], v[20:23], a[100:103]\n"
"v_mfma_f32_16x16x32_bf16 a[4:7], a[180:183], v[20:23], a[4:7]\n"
"ds_read_b128 a[172:175], v248 offset:8448\n"
"v_mfma_f32_16x16x32_bf16 a[36:39], a[184:187], v[20:23], a[36:39]\n"
"v_mfma_f32_16x16x32_bf16 a[72:75], a[188:191], v[24:27], a[72:75]\n"
"ds_read_b128 a[160:163], v244 offset:256\n"
"buffer_load_dwordx4 v[76:79], v1, s[24:27], s96 offen offset:3584\n"
"v_mfma_f32_16x16x32_bf16 a[104:107], a[192:195], v[24:27], a[104:107]\n"
"v_mfma_f32_16x16x32_bf16 a[8:11], a[180:183], v[24:27], a[8:11]\n"
"ds_read_b128 a[164:167], v244 offset:8448\n"
"v_mfma_f32_16x16x32_bf16 a[40:43], a[184:187], v[24:27], a[40:43]\n"
"v_mfma_f32_16x16x32_bf16 a[76:79], a[188:191], v[28:31], a[76:79]\n"
"v_perm_b32 v16, v116, v112, s52\n"
"v_mfma_f32_16x16x32_bf16 a[108:111], a[192:195], v[28:31], a[108:111]\n"
"v_perm_b32 v17, v124, v120, s52\n"
"v_mfma_f32_16x16x32_bf16 a[12:15], a[180:183], v[28:31], a[12:15]\n"
"v_perm_b32 v18, v132, v128, s52\n"
"v_mfma_f32_16x16x32_bf16 a[44:47], a[184:187], v[28:31], a[44:47]\n"
"v_perm_b32 v19, v140, v136, s52\n"
"v_mfma_f32_16x16x32_bf16 a[80:83], a[188:191], v[32:35], a[80:83]\n"
"v_perm_b32 v20, v116, v112, s53\n"
"v_perm_b32 v21, v124, v120, s53\n"
"v_mfma_f32_16x16x32_bf16 a[112:115], a[192:195], v[32:35], a[112:115]\n"
"v_perm_b32 v22, v132, v128, s53\n"
"v_perm_b32 v23, v140, v136, s53\n"
"v_mfma_f32_16x16x32_bf16 a[16:19], a[180:183], v[32:35], a[16:19]\n"
"v_perm_b32 v24, v117, v113, s52\n"
"v_perm_b32 v25, v125, v121, s52\n"
"v_mfma_f32_16x16x32_bf16 a[48:51], a[184:187], v[32:35], a[48:51]\n"
"v_perm_b32 v26, v133, v129, s52\n"
"v_perm_b32 v27, v141, v137, s52\n"
"v_mfma_f32_16x16x32_bf16 a[84:87], a[188:191], v[36:39], a[84:87]\n"
"s_mov_b32 s97, 0x18000\n"
"buffer_load_dwordx4 v[80:83], v1, s[24:27], s97 offen\n"
"v_perm_b32 v28, v117, v113, s53\n"
"v_mfma_f32_16x16x32_bf16 a[116:119], a[192:195], v[36:39], a[116:119]\n"
"v_perm_b32 v29, v125, v121, s53\n"
"v_mfma_f32_16x16x32_bf16 a[20:23], a[180:183], v[36:39], a[20:23]\n"
"v_perm_b32 v30, v133, v129, s53\n"
"v_mfma_f32_16x16x32_bf16 a[52:55], a[184:187], v[36:39], a[52:55]\n"
"v_perm_b32 v31, v141, v137, s53\n"
"v_mfma_f32_16x16x32_bf16 a[88:91], a[188:191], v[40:43], a[88:91]\n"
"v_perm_b32 v32, v118, v114, s52\n"
"v_perm_b32 v33, v126, v122, s52\n"
"v_mfma_f32_16x16x32_bf16 a[120:123], a[192:195], v[40:43], a[120:123]\n"
"v_perm_b32 v34, v134, v130, s52\n"
"v_perm_b32 v35, v142, v138, s52\n"
"v_mfma_f32_16x16x32_bf16 a[24:27], a[180:183], v[40:43], a[24:27]\n"
"v_perm_b32 v36, v118, v114, s53\n"
"v_perm_b32 v37, v126, v122, s53\n"
"v_mfma_f32_16x16x32_bf16 a[56:59], a[184:187], v[40:43], a[56:59]\n"
"v_perm_b32 v38, v134, v130, s53\n"
"v_perm_b32 v39, v142, v138, s53\n"
"v_mfma_f32_16x16x32_bf16 a[92:95], a[188:191], v[44:47], a[92:95]\n"
"v_perm_b32 v40, v119, v115, s52\n"
"v_mfma_f32_16x16x32_bf16 a[124:127], a[192:195], v[44:47], a[124:127]\n"
"v_perm_b32 v41, v127, v123, s52\n"
"v_mfma_f32_16x16x32_bf16 a[28:31], a[180:183], v[44:47], a[28:31]\n"
"v_perm_b32 v42, v135, v131, s52\n"
"v_mfma_f32_16x16x32_bf16 a[60:63], a[184:187], v[44:47], a[60:63]\n"
"buffer_load_dwordx4 v[84:87], v1, s[24:27], s97 offen offset:512\n"
"v_perm_b32 v43, v143, v139, s52\n"
"s_waitcnt lgkmcnt(0)\n"
"s_nop 0\n"
"v_mfma_f32_16x16x32_bf16 a[64:67], a[168:171], v[16:19], a[64:67]\n"
"v_perm_b32 v44, v119, v115, s53\n"
"v_mfma_f32_16x16x32_bf16 a[96:99], a[172:175], v[16:19], a[96:99]\n"
"v_perm_b32 v45, v127, v123, s53\n"
"v_mfma_f32_16x16x32_bf16 a[0:3], a[160:163], v[16:19], a[0:3]\n"
"v_perm_b32 v46, v135, v131, s53\n"
"v_mfma_f32_16x16x32_bf16 a[32:35], a[164:167], v[16:19], a[32:35]\n"
"v_perm_b32 v47, v143, v139, s53\n"
"v_mfma_f32_16x16x32_bf16 a[68:71], a[168:171], v[20:23], a[68:71]\n"
"ds_read_b128 a[188:191], v249 offset:256\n"
"v_mfma_f32_16x16x32_bf16 a[100:103], a[172:175], v[20:23], a[100:103]\n"
"v_mfma_f32_16x16x32_bf16 a[4:7], a[160:163], v[20:23], a[4:7]\n"
"ds_read_b128 a[192:195], v249 offset:8448\n"
"v_mfma_f32_16x16x32_bf16 a[36:39], a[164:167], v[20:23], a[36:39]\n"
"v_mfma_f32_16x16x32_bf16 a[72:75], a[168:171], v[24:27], a[72:75]\n"
"ds_read_b128 a[180:183], v245 offset:256\n"
"v_mfma_f32_16x16x32_bf16 a[104:107], a[172:175], v[24:27], a[104:107]\n"
"v_mfma_f32_16x16x32_bf16 a[8:11], a[160:163], v[24:27], a[8:11]\n"
"ds_read_b128 a[184:187], v245 offset:8448\n"
"v_mfma_f32_16x16x32_bf16 a[40:43], a[164:167], v[24:27], a[40:43]\n"
"buffer_load_dwordx4 v[88:91], v1, s[24:27], s97 offen offset:1024\n"
"v_mfma_f32_16x16x32_bf16 a[76:79], a[168:171], v[28:31], a[76:79]\n"
"v_perm_b32 v16, v148, v144, s52\n"
"v_mfma_f32_16x16x32_bf16 a[108:111], a[172:175], v[28:31], a[108:111]\n"
"v_perm_b32 v17, v156, v152, s52\n"
"v_mfma_f32_16x16x32_bf16 a[12:15], a[160:163], v[28:31], a[12:15]\n"
"v_perm_b32 v18, v164, v160, s52\n"
"v_mfma_f32_16x16x32_bf16 a[44:47], a[164:167], v[28:31], a[44:47]\n"
"v_perm_b32 v19, v172, v168, s52\n"
"v_mfma_f32_16x16x32_bf16 a[80:83], a[168:171], v[32:35], a[80:83]\n"
"v_perm_b32 v20, v148, v144, s53\n"
"v_perm_b32 v21, v156, v152, s53\n"
"v_mfma_f32_16x16x32_bf16 a[112:115], a[172:175], v[32:35], a[112:115]\n"
"v_perm_b32 v22, v164, v160, s53\n"
"v_perm_b32 v23, v172, v168, s53\n"
"v_mfma_f32_16x16x32_bf16 a[16:19], a[160:163], v[32:35], a[16:19]\n"
"v_perm_b32 v24, v149, v145, s52\n"
"v_perm_b32 v25, v157, v153, s52\n"
"v_mfma_f32_16x16x32_bf16 a[48:51], a[164:167], v[32:35], a[48:51]\n"
"v_perm_b32 v26, v165, v161, s52\n"
"v_perm_b32 v27, v173, v169, s52\n"
"v_mfma_f32_16x16x32_bf16 a[84:87], a[168:171], v[36:39], a[84:87]\n"
"v_perm_b32 v28, v149, v145, s53\n"
"v_mfma_f32_16x16x32_bf16 a[116:119], a[172:175], v[36:39], a[116:119]\n"
"v_perm_b32 v29, v157, v153, s53\n"
"v_mfma_f32_16x16x32_bf16 a[20:23], a[160:163], v[36:39], a[20:23]\n"
"v_perm_b32 v30, v165, v161, s53\n"
"buffer_load_dwordx4 v[92:95], v1, s[24:27], s97 offen offset:1536\n"
"v_mfma_f32_16x16x32_bf16 a[52:55], a[164:167], v[36:39], a[52:55]\n"
"v_perm_b32 v31, v173, v169, s53\n"
"v_mfma_f32_16x16x32_bf16 a[88:91], a[168:171], v[40:43], a[88:91]\n"
"v_perm_b32 v32, v150, v146, s52\n"
"v_perm_b32 v33, v158, v154, s52\n"
"v_mfma_f32_16x16x32_bf16 a[120:123], a[172:175], v[40:43], a[120:123]\n"
"v_perm_b32 v34, v166, v162, s52\n"
"v_perm_b32 v35, v174, v170, s52\n"
"v_mfma_f32_16x16x32_bf16 a[24:27], a[160:163], v[40:43], a[24:27]\n"
"v_perm_b32 v36, v150, v146, s53\n"
"v_perm_b32 v37, v158, v154, s53\n"
"buffer_load_dwordx4 v[96:99], v1, s[24:27], s97 offen offset:2048\n"
"v_mfma_f32_16x16x32_bf16 a[56:59], a[164:167], v[40:43], a[56:59]\n"
"v_perm_b32 v38, v166, v162, s53\n"
"v_perm_b32 v39, v174, v170, s53\n"
"v_mfma_f32_16x16x32_bf16 a[92:95], a[168:171], v[44:47], a[92:95]\n"
"v_perm_b32 v40, v151, v147, s52\n"
"v_mfma_f32_16x16x32_bf16 a[124:127], a[172:175], v[44:47], a[124:127]\n"
"v_perm_b32 v41, v159, v155, s52\n"
"v_mfma_f32_16x16x32_bf16 a[28:31], a[160:163], v[44:47], a[28:31]\n"
"v_perm_b32 v42, v167, v163, s52\n"
"v_mfma_f32_16x16x32_bf16 a[60:63], a[164:167], v[44:47], a[60:63]\n"
"buffer_load_dwordx4 v[100:103], v1, s[24:27], s97 offen offset:2560\n"
"v_perm_b32 v43, v175, v171, s52\n"
"s_waitcnt lgkmcnt(0)\n"
"s_nop 0\n"
"v_mfma_f32_16x16x32_bf16 a[64:67], a[188:191], v[16:19], a[64:67]\n"
"v_perm_b32 v44, v151, v147, s53\n"
"v_mfma_f32_16x16x32_bf16 a[96:99], a[192:195], v[16:19], a[96:99]\n"
"v_perm_b32 v45, v159, v155, s53\n"
"v_mfma_f32_16x16x32_bf16 a[0:3], a[180:183], v[16:19], a[0:3]\n"
"v_perm_b32 v46, v167, v163, s53\n"
"v_mfma_f32_16x16x32_bf16 a[32:35], a[184:187], v[16:19], a[32:35]\n"
"buffer_load_dwordx4 v[104:107], v1, s[24:27], s97 offen offset:3072\n"
"v_perm_b32 v47, v175, v171, s53\n"
"v_mfma_f32_16x16x32_bf16 a[68:71], a[188:191], v[20:23], a[68:71]\n"
"ds_read_b128 a[168:171], v250 offset:512\n"
"v_mfma_f32_16x16x32_bf16 a[100:103], a[192:195], v[20:23], a[100:103]\n"
"v_mfma_f32_16x16x32_bf16 a[4:7], a[180:183], v[20:23], a[4:7]\n"
"ds_read_b128 a[172:175], v250 offset:8704\n"
"v_mfma_f32_16x16x32_bf16 a[36:39], a[184:187], v[20:23], a[36:39]\n"
"v_mfma_f32_16x16x32_bf16 a[72:75], a[188:191], v[24:27], a[72:75]\n"
"buffer_load_dwordx4 v[108:111], v1, s[24:27], s97 offen offset:3584\n"
"ds_read_b128 a[160:163], v246 offset:512\n"
"v_mfma_f32_16x16x32_bf16 a[104:107], a[192:195], v[24:27], a[104:107]\n"
"v_mfma_f32_16x16x32_bf16 a[8:11], a[180:183], v[24:27], a[8:11]\n"
"ds_read_b128 a[164:167], v246 offset:8704\n"
"v_mfma_f32_16x16x32_bf16 a[40:43], a[184:187], v[24:27], a[40:43]\n"
"v_mfma_f32_16x16x32_bf16 a[76:79], a[188:191], v[28:31], a[76:79]\n"
"s_mov_b32 s96, 0x30000\n"
"buffer_load_dwordx4 v[112:115], v1, s[24:27], s96 offen\n"
"v_perm_b32 v16, v180, v176, s52\n"
"v_mfma_f32_16x16x32_bf16 a[108:111], a[192:195], v[28:31], a[108:111]\n"
"v_perm_b32 v17, v188, v184, s52\n"
"v_mfma_f32_16x16x32_bf16 a[12:15], a[180:183], v[28:31], a[12:15]\n"
"v_perm_b32 v18, v196, v192, s52\n"
"v_mfma_f32_16x16x32_bf16 a[44:47], a[184:187], v[28:31], a[44:47]\n"
"v_perm_b32 v19, v204, v200, s52\n"
"v_mfma_f32_16x16x32_bf16 a[80:83], a[188:191], v[32:35], a[80:83]\n"
"buffer_load_dwordx4 v[116:119], v1, s[24:27], s96 offen offset:512\n"
"v_perm_b32 v20, v180, v176, s53\n"
"v_perm_b32 v21, v188, v184, s53\n"
"v_mfma_f32_16x16x32_bf16 a[112:115], a[192:195], v[32:35], a[112:115]\n"
"v_perm_b32 v22, v196, v192, s53\n"
"v_perm_b32 v23, v204, v200, s53\n"
"v_mfma_f32_16x16x32_bf16 a[16:19], a[180:183], v[32:35], a[16:19]\n"
"v_perm_b32 v24, v181, v177, s52\n"
"v_perm_b32 v25, v189, v185, s52\n"
"v_mfma_f32_16x16x32_bf16 a[48:51], a[184:187], v[32:35], a[48:51]\n"
"v_perm_b32 v26, v197, v193, s52\n"
"v_perm_b32 v27, v205, v201, s52\n"
"v_mfma_f32_16x16x32_bf16 a[84:87], a[188:191], v[36:39], a[84:87]\n"
"buffer_load_dwordx4 v[120:123], v1, s[24:27], s96 offen offset:1024\n"
"v_perm_b32 v28, v181, v177, s53\n"
"v_mfma_f32_16x16x32_bf16 a[116:119], a[192:195], v[36:39], a[116:119]\n"
"v_perm_b32 v29, v189, v185, s53\n"
"v_mfma_f32_16x16x32_bf16 a[20:23], a[180:183], v[36:39], a[20:23]\n"
"v_perm_b32 v30, v197, v193, s53\n"
"v_mfma_f32_16x16x32_bf16 a[52:55], a[184:187], v[36:39], a[52:55]\n"
"v_perm_b32 v31, v205, v201, s53\n"
"v_mfma_f32_16x16x32_bf16 a[88:91], a[188:191], v[40:43], a[88:91]\n"
"v_perm_b32 v32, v182, v178, s52\n"
"buffer_load_dwordx4 v[124:127], v1, s[24:27], s96 offen offset:1536\n"
"v_perm_b32 v33, v190, v186, s52\n"
"v_mfma_f32_16x16x32_bf16 a[120:123], a[192:195], v[40:43], a[120:123]\n"
"v_perm_b32 v34, v198, v194, s52\n"
"v_perm_b32 v35, v206, v202, s52\n"
"v_mfma_f32_16x16x32_bf16 a[24:27], a[180:183], v[40:43], a[24:27]\n"
"v_perm_b32 v36, v182, v178, s53\n"
"v_perm_b32 v37, v190, v186, s53\n"
"v_mfma_f32_16x16x32_bf16 a[56:59], a[184:187], v[40:43], a[56:59]\n"
"v_perm_b32 v38, v198, v194, s53\n"
"v_perm_b32 v39, v206, v202, s53\n"
"v_mfma_f32_16x16x32_bf16 a[92:95], a[188:191], v[44:47], a[92:95]\n"
"v_perm_b32 v40, v183, v179, s52\n"
"buffer_load_dwordx4 v[128:131], v1, s[24:27], s96 offen offset:2048\n"
"v_mfma_f32_16x16x32_bf16 a[124:127], a[192:195], v[44:47], a[124:127]\n"
"v_perm_b32 v41, v191, v187, s52\n"
"v_mfma_f32_16x16x32_bf16 a[28:31], a[180:183], v[44:47], a[28:31]\n"
"v_perm_b32 v42, v199, v195, s52\n"
"v_mfma_f32_16x16x32_bf16 a[60:63], a[184:187], v[44:47], a[60:63]\n"
"v_perm_b32 v43, v207, v203, s52\n"
"s_waitcnt lgkmcnt(0)\n"
"s_nop 0\n"
"v_mfma_f32_16x16x32_bf16 a[64:67], a[168:171], v[16:19], a[64:67]\n"
"v_perm_b32 v44, v183, v179, s53\n"
"v_mfma_f32_16x16x32_bf16 a[96:99], a[172:175], v[16:19], a[96:99]\n"
"buffer_load_dwordx4 v[132:135], v1, s[24:27], s96 offen offset:2560\n"
"v_perm_b32 v45, v191, v187, s53\n"
"v_mfma_f32_16x16x32_bf16 a[0:3], a[160:163], v[16:19], a[0:3]\n"
"v_perm_b32 v46, v199, v195, s53\n"
"v_mfma_f32_16x16x32_bf16 a[32:35], a[164:167], v[16:19], a[32:35]\n"
"v_perm_b32 v47, v207, v203, s53\n"
"v_mfma_f32_16x16x32_bf16 a[68:71], a[168:171], v[20:23], a[68:71]\n"
"ds_read_b128 a[188:191], v251 offset:512\n"
"v_mfma_f32_16x16x32_bf16 a[100:103], a[172:175], v[20:23], a[100:103]\n"
"buffer_load_dwordx4 v[136:139], v1, s[24:27], s96 offen offset:3072\n"
"v_mfma_f32_16x16x32_bf16 a[4:7], a[160:163], v[20:23], a[4:7]\n"
"ds_read_b128 a[192:195], v251 offset:8704\n"
"v_mfma_f32_16x16x32_bf16 a[36:39], a[164:167], v[20:23], a[36:39]\n"
"v_mfma_f32_16x16x32_bf16 a[72:75], a[168:171], v[24:27], a[72:75]\n"
"ds_read_b128 a[180:183], v247 offset:512\n"
"v_mfma_f32_16x16x32_bf16 a[104:107], a[172:175], v[24:27], a[104:107]\n"
"v_mfma_f32_16x16x32_bf16 a[8:11], a[160:163], v[24:27], a[8:11]\n"
"buffer_load_dwordx4 v[140:143], v1, s[24:27], s96 offen offset:3584\n"
"ds_read_b128 a[184:187], v247 offset:8704\n"
"v_mfma_f32_16x16x32_bf16 a[40:43], a[164:167], v[24:27], a[40:43]\n"
"v_mfma_f32_16x16x32_bf16 a[76:79], a[168:171], v[28:31], a[76:79]\n"
"v_perm_b32 v16, v212, v208, s52\n"
"v_mfma_f32_16x16x32_bf16 a[108:111], a[172:175], v[28:31], a[108:111]\n"
"v_perm_b32 v17, v220, v216, s52\n"
"v_mfma_f32_16x16x32_bf16 a[12:15], a[160:163], v[28:31], a[12:15]\n"
"s_mov_b32 s97, 0x38000\n"
"buffer_load_dwordx4 v[144:147], v1, s[24:27], s97 offen\n"
"v_perm_b32 v18, v228, v224, s52\n"
"v_mfma_f32_16x16x32_bf16 a[44:47], a[164:167], v[28:31], a[44:47]\n"
"v_perm_b32 v19, v236, v232, s52\n"
"v_mfma_f32_16x16x32_bf16 a[80:83], a[168:171], v[32:35], a[80:83]\n"
"v_perm_b32 v20, v212, v208, s53\n"
"v_perm_b32 v21, v220, v216, s53\n"
"v_mfma_f32_16x16x32_bf16 a[112:115], a[172:175], v[32:35], a[112:115]\n"
"v_perm_b32 v22, v228, v224, s53\n"
"v_perm_b32 v23, v236, v232, s53\n"
"v_mfma_f32_16x16x32_bf16 a[16:19], a[160:163], v[32:35], a[16:19]\n"
"buffer_load_dwordx4 v[148:151], v1, s[24:27], s97 offen offset:512\n"
"v_perm_b32 v24, v213, v209, s52\n"
"v_perm_b32 v25, v221, v217, s52\n"
"v_mfma_f32_16x16x32_bf16 a[48:51], a[164:167], v[32:35], a[48:51]\n"
"v_perm_b32 v26, v229, v225, s52\n"
"v_perm_b32 v27, v237, v233, s52\n"
"v_mfma_f32_16x16x32_bf16 a[84:87], a[168:171], v[36:39], a[84:87]\n"
"v_perm_b32 v28, v213, v209, s53\n"
"v_mfma_f32_16x16x32_bf16 a[116:119], a[172:175], v[36:39], a[116:119]\n"
"v_perm_b32 v29, v221, v217, s53\n"
"v_mfma_f32_16x16x32_bf16 a[20:23], a[160:163], v[36:39], a[20:23]\n"
"buffer_load_dwordx4 v[152:155], v1, s[24:27], s97 offen offset:1024\n"
"v_perm_b32 v30, v229, v225, s53\n"
"v_mfma_f32_16x16x32_bf16 a[52:55], a[164:167], v[36:39], a[52:55]\n"
"v_perm_b32 v31, v237, v233, s53\n"
"v_mfma_f32_16x16x32_bf16 a[88:91], a[168:171], v[40:43], a[88:91]\n"
"v_perm_b32 v32, v214, v210, s52\n"
"v_perm_b32 v33, v222, v218, s52\n"
"v_mfma_f32_16x16x32_bf16 a[120:123], a[172:175], v[40:43], a[120:123]\n"
"v_perm_b32 v34, v230, v226, s52\n"
"v_perm_b32 v35, v238, v234, s52\n"
"v_mfma_f32_16x16x32_bf16 a[24:27], a[160:163], v[40:43], a[24:27]\n"
"v_perm_b32 v36, v214, v210, s53\n"
"buffer_load_dwordx4 v[156:159], v1, s[24:27], s97 offen offset:1536\n"
"v_perm_b32 v37, v222, v218, s53\n"
"v_mfma_f32_16x16x32_bf16 a[56:59], a[164:167], v[40:43], a[56:59]\n"
"v_perm_b32 v38, v230, v226, s53\n"
"v_perm_b32 v39, v238, v234, s53\n"
"v_mfma_f32_16x16x32_bf16 a[92:95], a[168:171], v[44:47], a[92:95]\n"
"v_perm_b32 v40, v215, v211, s52\n"
"v_mfma_f32_16x16x32_bf16 a[124:127], a[172:175], v[44:47], a[124:127]\n"
"v_perm_b32 v41, v223, v219, s52\n"
"v_mfma_f32_16x16x32_bf16 a[28:31], a[160:163], v[44:47], a[28:31]\n"
"v_perm_b32 v42, v231, v227, s52\n"
"buffer_load_dwordx4 v[160:163], v1, s[24:27], s97 offen offset:2048\n"
"v_mfma_f32_16x16x32_bf16 a[60:63], a[164:167], v[44:47], a[60:63]\n"
"v_perm_b32 v43, v239, v235, s52\n"
"s_waitcnt lgkmcnt(0)\n"
"s_nop 0\n"
"v_mfma_f32_16x16x32_bf16 a[64:67], a[188:191], v[16:19], a[64:67]\n"
"v_perm_b32 v44, v215, v211, s53\n"
"v_perm_b32 v45, v223, v219, s53\n"
"v_mfma_f32_16x16x32_bf16 a[96:99], a[192:195], v[16:19], a[96:99]\n"
"v_perm_b32 v46, v231, v227, s53\n"
"v_mfma_f32_16x16x32_bf16 a[0:3], a[180:183], v[16:19], a[0:3]\n"
"v_perm_b32 v47, v239, v235, s53\n"
"v_mfma_f32_16x16x32_bf16 a[32:35], a[184:187], v[16:19], a[32:35]\n"
"s_waitcnt vmcnt(31)\n"
"s_barrier\n"
"buffer_load_dwordx4 v[164:167], v1, s[24:27], s97 offen offset:2560\n"
"v_mfma_f32_16x16x32_bf16 a[68:71], a[188:191], v[20:23], a[68:71]\n"
"ds_read_b128 a[168:171], v248 offset:36864\n"
"v_mfma_f32_16x16x32_bf16 a[100:103], a[192:195], v[20:23], a[100:103]\n"
"buffer_load_dwordx4 v[168:171], v1, s[24:27], s97 offen offset:3072\n"
"v_mfma_f32_16x16x32_bf16 a[4:7], a[180:183], v[20:23], a[4:7]\n"
"ds_read_b128 a[172:175], v248 offset:45056\n"
"v_mfma_f32_16x16x32_bf16 a[36:39], a[184:187], v[20:23], a[36:39]\n"
"v_mfma_f32_16x16x32_bf16 a[72:75], a[188:191], v[24:27], a[72:75]\n"
"ds_read_b128 a[160:163], v244 offset:36864\n"
"v_mfma_f32_16x16x32_bf16 a[104:107], a[192:195], v[24:27], a[104:107]\n"
"v_mfma_f32_16x16x32_bf16 a[8:11], a[180:183], v[24:27], a[8:11]\n"
"buffer_load_dwordx4 v[172:175], v1, s[24:27], s97 offen offset:3584\n"
"ds_read_b128 a[164:167], v244 offset:45056\n"
"v_mfma_f32_16x16x32_bf16 a[40:43], a[184:187], v[24:27], a[40:43]\n"
"v_mfma_f32_16x16x32_bf16 a[76:79], a[188:191], v[28:31], a[76:79]\n"
"s_waitcnt vmcnt(24)\n"
"s_nop 0\n"
"v_perm_b32 v16, v52, v48, s52\n"
"v_mfma_f32_16x16x32_bf16 a[108:111], a[192:195], v[28:31], a[108:111]\n"
"v_perm_b32 v17, v60, v56, s52\n"
"v_mfma_f32_16x16x32_bf16 a[12:15], a[180:183], v[28:31], a[12:15]\n"
"v_perm_b32 v18, v68, v64, s52\n"
"v_mfma_f32_16x16x32_bf16 a[44:47], a[184:187], v[28:31], a[44:47]\n"
"v_perm_b32 v19, v76, v72, s52\n"
"v_mfma_f32_16x16x32_bf16 a[80:83], a[188:191], v[32:35], a[80:83]\n"
"v_perm_b32 v20, v52, v48, s53\n"
"v_perm_b32 v21, v60, v56, s53\n"
"v_mfma_f32_16x16x32_bf16 a[112:115], a[192:195], v[32:35], a[112:115]\n"
"v_perm_b32 v22, v68, v64, s53\n"
"v_perm_b32 v23, v76, v72, s53\n"
"v_mfma_f32_16x16x32_bf16 a[16:19], a[180:183], v[32:35], a[16:19]\n"
"v_perm_b32 v24, v53, v49, s52\n"
"v_perm_b32 v25, v61, v57, s52\n"
"v_mfma_f32_16x16x32_bf16 a[48:51], a[184:187], v[32:35], a[48:51]\n"
"v_perm_b32 v26, v69, v65, s52\n"
"v_perm_b32 v27, v77, v73, s52\n"
"v_mfma_f32_16x16x32_bf16 a[84:87], a[188:191], v[36:39], a[84:87]\n"
"v_perm_b32 v28, v53, v49, s53\n"
"v_mfma_f32_16x16x32_bf16 a[116:119], a[192:195], v[36:39], a[116:119]\n"
"v_perm_b32 v29, v61, v57, s53\n"
"v_mfma_f32_16x16x32_bf16 a[20:23], a[180:183], v[36:39], a[20:23]\n"
"v_perm_b32 v30, v69, v65, s53\n"
"v_mfma_f32_16x16x32_bf16 a[52:55], a[184:187], v[36:39], a[52:55]\n"
"v_perm_b32 v31, v77, v73, s53\n"
"v_mfma_f32_16x16x32_bf16 a[88:91], a[188:191], v[40:43], a[88:91]\n"
"v_perm_b32 v32, v54, v50, s52\n"
"v_perm_b32 v33, v62, v58, s52\n"
"v_perm_b32 v34, v70, v66, s52\n"
"v_mfma_f32_16x16x32_bf16 a[120:123], a[192:195], v[40:43], a[120:123]\n"
"v_perm_b32 v35, v78, v74, s52\n"
"v_perm_b32 v36, v54, v50, s53\n"
"v_mfma_f32_16x16x32_bf16 a[24:27], a[180:183], v[40:43], a[24:27]\n"
"v_perm_b32 v37, v62, v58, s53\n"
"v_perm_b32 v38, v70, v66, s53\n"
"v_mfma_f32_16x16x32_bf16 a[56:59], a[184:187], v[40:43], a[56:59]\n"
"v_perm_b32 v39, v78, v74, s53\n"
"buffer_load_dword v5, v4, s[28:31], s37 offen sc1\n"
"v_mfma_f32_16x16x32_bf16 a[92:95], a[188:191], v[44:47], a[92:95]\n"
"buffer_load_dword v11, v4, s[28:31], 0 offen offset:4 sc1\n"
"v_perm_b32 v40, v55, v51, s52\n"
"s_mov_b32 s96, 0x50000\n"
"buffer_load_dwordx4 v[176:179], v1, s[24:27], s96 offen\n"
"v_mfma_f32_16x16x32_bf16 a[124:127], a[192:195], v[44:47], a[124:127]\n"
"buffer_load_dwordx4 v[180:183], v1, s[24:27], s96 offen offset:512\n"
"v_perm_b32 v41, v63, v59, s52\n"
"v_mfma_f32_16x16x32_bf16 a[28:31], a[180:183], v[44:47], a[28:31]\n"
"buffer_load_dwordx4 v[184:187], v1, s[24:27], s96 offen offset:1024\n"
"v_perm_b32 v42, v71, v67, s52\n"
"v_mfma_f32_16x16x32_bf16 a[60:63], a[184:187], v[44:47], a[60:63]\n"
"v_perm_b32 v43, v79, v75, s52\n"
"s_waitcnt lgkmcnt(0)\n"
"s_nop 0\n"
"v_mfma_f32_16x16x32_bf16 a[64:67], a[168:171], v[16:19], a[64:67]\n"
"v_perm_b32 v44, v55, v51, s53\n"
"v_mfma_f32_16x16x32_bf16 a[96:99], a[172:175], v[16:19], a[96:99]\n"
"v_perm_b32 v45, v63, v59, s53\n"
"v_mfma_f32_16x16x32_bf16 a[0:3], a[160:163], v[16:19], a[0:3]\n"
"v_perm_b32 v46, v71, v67, s53\n"
"buffer_load_dwordx4 v[188:191], v1, s[24:27], s96 offen offset:1536\n"
"v_mfma_f32_16x16x32_bf16 a[32:35], a[164:167], v[16:19], a[32:35]\n"
"v_perm_b32 v47, v79, v75, s53\n"
"v_mfma_f32_16x16x32_bf16 a[68:71], a[168:171], v[20:23], a[68:71]\n"
"ds_read_b128 a[188:191], v249 offset:36864\n"
"v_mfma_f32_16x16x32_bf16 a[100:103], a[172:175], v[20:23], a[100:103]\n"
"v_mfma_f32_16x16x32_bf16 a[4:7], a[160:163], v[20:23], a[4:7]\n"
"ds_read_b128 a[192:195], v249 offset:45056\n"
"v_mfma_f32_16x16x32_bf16 a[36:39], a[164:167], v[20:23], a[36:39]\n"
"buffer_load_dwordx4 v[192:195], v1, s[24:27], s96 offen offset:2048\n"
"v_mfma_f32_16x16x32_bf16 a[72:75], a[168:171], v[24:27], a[72:75]\n"
"ds_read_b128 a[180:183], v245 offset:36864\n"
"v_mfma_f32_16x16x32_bf16 a[104:107], a[172:175], v[24:27], a[104:107]\n"
"v_mfma_f32_16x16x32_bf16 a[8:11], a[160:163], v[24:27], a[8:11]\n"
"ds_read_b128 a[184:187], v245 offset:45056\n"
"v_mfma_f32_16x16x32_bf16 a[40:43], a[164:167], v[24:27], a[40:43]\n"
"v_mfma_f32_16x16x32_bf16 a[76:79], a[168:171], v[28:31], a[76:79]\n"
"buffer_load_dwordx4 v[196:199], v1, s[24:27], s96 offen offset:2560\n"
"s_waitcnt vmcnt(24)\n"
"s_nop 0\n"
"v_perm_b32 v16, v84, v80, s52\n"
"v_mfma_f32_16x16x32_bf16 a[108:111], a[172:175], v[28:31], a[108:111]\n"
"v_perm_b32 v17, v92, v88, s52\n"
"v_mfma_f32_16x16x32_bf16 a[12:15], a[160:163], v[28:31], a[12:15]\n"
"v_perm_b32 v18, v100, v96, s52\n"
"v_mfma_f32_16x16x32_bf16 a[44:47], a[164:167], v[28:31], a[44:47]\n"
"v_perm_b32 v19, v108, v104, s52\n"
"v_mfma_f32_16x16x32_bf16 a[80:83], a[168:171], v[32:35], a[80:83]\n"
"buffer_load_dwordx4 v[200:203], v1, s[24:27], s96 offen offset:3072\n"
"v_perm_b32 v20, v84, v80, s53\n"
"v_perm_b32 v21, v92, v88, s53\n"
"v_mfma_f32_16x16x32_bf16 a[112:115], a[172:175], v[32:35], a[112:115]\n"
"v_perm_b32 v22, v100, v96, s53\n"
"v_perm_b32 v23, v108, v104, s53\n"
"v_mfma_f32_16x16x32_bf16 a[16:19], a[160:163], v[32:35], a[16:19]\n"
"v_perm_b32 v24, v85, v81, s52\n"
"v_perm_b32 v25, v93, v89, s52\n"
"v_mfma_f32_16x16x32_bf16 a[48:51], a[164:167], v[32:35], a[48:51]\n"
"v_perm_b32 v26, v101, v97, s52\n"
"v_perm_b32 v27, v109, v105, s52\n"
"buffer_load_dwordx4 v[204:207], v1, s[24:27], s96 offen offset:3584\n"
"v_mfma_f32_16x16x32_bf16 a[84:87], a[168:171], v[36:39], a[84:87]\n"
"v_perm_b32 v28, v85, v81, s53\n"
"v_mfma_f32_16x16x32_bf16 a[116:119], a[172:175], v[36:39], a[116:119]\n"
"v_perm_b32 v29, v93, v89, s53\n"
"v_mfma_f32_16x16x32_bf16 a[20:23], a[160:163], v[36:39], a[20:23]\n"
"v_perm_b32 v30, v101, v97, s53\n"
"v_mfma_f32_16x16x32_bf16 a[52:55], a[164:167], v[36:39], a[52:55]\n"
"v_perm_b32 v31, v109, v105, s53\n"
"v_mfma_f32_16x16x32_bf16 a[88:91], a[168:171], v[40:43], a[88:91]\n"
"s_mov_b32 s97, 0x58000\n"
"buffer_load_dwordx4 v[208:211], v1, s[24:27], s97 offen\n"
"v_perm_b32 v32, v86, v82, s52\n"
"v_perm_b32 v33, v94, v90, s52\n"
"v_mfma_f32_16x16x32_bf16 a[120:123], a[172:175], v[40:43], a[120:123]\n"
"v_perm_b32 v34, v102, v98, s52\n"
"v_perm_b32 v35, v110, v106, s52\n"
"v_mfma_f32_16x16x32_bf16 a[24:27], a[160:163], v[40:43], a[24:27]\n"
"v_perm_b32 v36, v86, v82, s53\n"
"v_perm_b32 v37, v94, v90, s53\n"
"v_mfma_f32_16x16x32_bf16 a[56:59], a[164:167], v[40:43], a[56:59]\n"
"v_perm_b32 v38, v102, v98, s53\n"
"v_perm_b32 v39, v110, v106, s53\n"
"buffer_load_dwordx4 v[212:215], v1, s[24:27], s97 offen offset:512\n"
"v_mfma_f32_16x16x32_bf16 a[92:95], a[168:171], v[44:47], a[92:95]\n"
"v_perm_b32 v40, v87, v83, s52\n"
"v_mfma_f32_16x16x32_bf16 a[124:127], a[172:175], v[44:47], a[124:127]\n"
"v_perm_b32 v41, v95, v91, s52\n"
"v_mfma_f32_16x16x32_bf16 a[28:31], a[160:163], v[44:47], a[28:31]\n"
"v_perm_b32 v42, v103, v99, s52\n"
"v_mfma_f32_16x16x32_bf16 a[60:63], a[164:167], v[44:47], a[60:63]\n"
"v_perm_b32 v43, v111, v107, s52\n"
"s_waitcnt lgkmcnt(0)\n"
"s_nop 0\n"
"v_mfma_f32_16x16x32_bf16 a[64:67], a[188:191], v[16:19], a[64:67]\n"
"v_perm_b32 v44, v87, v83, s53\n"
"v_mfma_f32_16x16x32_bf16 a[96:99], a[192:195], v[16:19], a[96:99]\n"
"v_perm_b32 v45, v95, v91, s53\n"
"v_mfma_f32_16x16x32_bf16 a[0:3], a[180:183], v[16:19], a[0:3]\n"
"v_perm_b32 v46, v103, v99, s53\n"
"v_mfma_f32_16x16x32_bf16 a[32:35], a[184:187], v[16:19], a[32:35]\n"
"v_perm_b32 v47, v111, v107, s53\n"
"v_mfma_f32_16x16x32_bf16 a[68:71], a[188:191], v[20:23], a[68:71]\n"
"ds_read_b128 a[168:171], v248 offset:37120\n"
"v_mfma_f32_16x16x32_bf16 a[100:103], a[192:195], v[20:23], a[100:103]\n"
"v_mfma_f32_16x16x32_bf16 a[4:7], a[180:183], v[20:23], a[4:7]\n"
"ds_read_b128 a[172:175], v248 offset:45312\n"
"v_mfma_f32_16x16x32_bf16 a[36:39], a[184:187], v[20:23], a[36:39]\n"
"v_mfma_f32_16x16x32_bf16 a[72:75], a[188:191], v[24:27], a[72:75]\n"
"ds_read_b128 a[160:163], v244 offset:37120\n"
"v_mfma_f32_16x16x32_bf16 a[104:107], a[192:195], v[24:27], a[104:107]\n"
"v_mfma_f32_16x16x32_bf16 a[8:11], a[180:183], v[24:27], a[8:11]\n"
"ds_read_b128 a[164:167], v244 offset:45312\n"
"v_mfma_f32_16x16x32_bf16 a[40:43], a[184:187], v[24:27], a[40:43]\n"
"v_mfma_f32_16x16x32_bf16 a[76:79], a[188:191], v[28:31], a[76:79]\n"
"s_waitcnt vmcnt(20)\n"
"s_nop 0\n"
"v_perm_b32 v16, v116, v112, s52\n"
"v_mfma_f32_16x16x32_bf16 a[108:111], a[192:195], v[28:31], a[108:111]\n"
"v_perm_b32 v17, v124, v120, s52\n"
"v_mfma_f32_16x16x32_bf16 a[12:15], a[180:183], v[28:31], a[12:15]\n"
"v_perm_b32 v18, v132, v128, s52\n"
"v_mfma_f32_16x16x32_bf16 a[44:47], a[184:187], v[28:31], a[44:47]\n"
"v_perm_b32 v19, v140, v136, s52\n"
"v_mfma_f32_16x16x32_bf16 a[80:83], a[188:191], v[32:35], a[80:83]\n"
"v_perm_b32 v20, v116, v112, s53\n"
"v_perm_b32 v21, v124, v120, s53\n"
"v_mfma_f32_16x16x32_bf16 a[112:115], a[192:195], v[32:35], a[112:115]\n"
"v_perm_b32 v22, v132, v128, s53\n"
"v_perm_b32 v23, v140, v136, s53\n"
"v_mfma_f32_16x16x32_bf16 a[16:19], a[180:183], v[32:35], a[16:19]\n"
"v_perm_b32 v24, v117, v113, s52\n"
"v_perm_b32 v25, v125, v121, s52\n"
"v_mfma_f32_16x16x32_bf16 a[48:51], a[184:187], v[32:35], a[48:51]\n"
"v_perm_b32 v26, v133, v129, s52\n"
"v_perm_b32 v27, v141, v137, s52\n"
"v_mfma_f32_16x16x32_bf16 a[84:87], a[188:191], v[36:39], a[84:87]\n"
"v_perm_b32 v28, v117, v113, s53\n"
"v_mfma_f32_16x16x32_bf16 a[116:119], a[192:195], v[36:39], a[116:119]\n"
"v_perm_b32 v29, v125, v121, s53\n"
"v_mfma_f32_16x16x32_bf16 a[20:23], a[180:183], v[36:39], a[20:23]\n"
"v_perm_b32 v30, v133, v129, s53\n"
"v_mfma_f32_16x16x32_bf16 a[52:55], a[184:187], v[36:39], a[52:55]\n"
"v_perm_b32 v31, v141, v137, s53\n"
"v_mfma_f32_16x16x32_bf16 a[88:91], a[188:191], v[40:43], a[88:91]\n"
"v_perm_b32 v32, v118, v114, s52\n"
"v_perm_b32 v33, v126, v122, s52\n"
"v_mfma_f32_16x16x32_bf16 a[120:123], a[192:195], v[40:43], a[120:123]\n"
"v_perm_b32 v34, v134, v130, s52\n"
"v_perm_b32 v35, v142, v138, s52\n"
"v_mfma_f32_16x16x32_bf16 a[24:27], a[180:183], v[40:43], a[24:27]\n"
"v_perm_b32 v36, v118, v114, s53\n"
"v_perm_b32 v37, v126, v122, s53\n"
"v_mfma_f32_16x16x32_bf16 a[56:59], a[184:187], v[40:43], a[56:59]\n"
"v_perm_b32 v38, v134, v130, s53\n"
"v_perm_b32 v39, v142, v138, s53\n"
"v_mfma_f32_16x16x32_bf16 a[92:95], a[188:191], v[44:47], a[92:95]\n"
"v_perm_b32 v40, v119, v115, s52\n"
"v_perm_b32 v41, v127, v123, s52\n"
"v_mfma_f32_16x16x32_bf16 a[124:127], a[192:195], v[44:47], a[124:127]\n"
"v_perm_b32 v42, v135, v131, s52\n"
"v_mfma_f32_16x16x32_bf16 a[28:31], a[180:183], v[44:47], a[28:31]\n"
"v_perm_b32 v43, v143, v139, s52\n"
"v_mfma_f32_16x16x32_bf16 a[60:63], a[184:187], v[44:47], a[60:63]\n"
"s_waitcnt vmcnt(11)\n"
"v_xor_b32 v5, v5, v242\n"
"s_waitcnt vmcnt(10)\n"
"v_xor_b32 v11, v11, v243\n"
"v_or_b32 v5, v5, v11\n"
"v_cmp_ne_u32 vcc, 0, v5\n"
"s_nop 0\n"
"s_and_b32 s58, vcc_lo, s4\n"
"s_cbranch_scc1 .LBspin\n"
".LBacked:\n"
"s_barrier\n"
"buffer_load_dwordx4 v[216:219], v1, s[24:27], s97 offen offset:1024\n"
"s_lshr_b32 s58, s95, 1\n"
"buffer_load_dwordx4 v[220:223], v1, s[24:27], s97 offen offset:1536\n"
"v_lshl_add_u32 v5, v2, 4, s58\n"
"v_and_b32 v11, 7, v2\n"
"v_lshlrev_b32_e64 v11, 2, v11\n"
"v_add_u32 v11, 0x28000, v11\n"
"s_waitcnt lgkmcnt(0)\n"
"s_nop 0\n"
"v_mfma_f32_16x16x32_bf16 a[64:67], a[168:171], v[16:19], a[64:67]\n"
"buffer_load_dwordx4 v[224:227], v1, s[24:27], s97 offen offset:2048\n"
"v_perm_b32 v44, v119, v115, s53\n"
"v_mfma_f32_16x16x32_bf16 a[96:99], a[172:175], v[16:19], a[96:99]\n"
"v_perm_b32 v45, v127, v123, s53\n"
"v_mfma_f32_16x16x32_bf16 a[0:3], a[160:163], v[16:19], a[0:3]\n"
"v_perm_b32 v46, v135, v131, s53\n"
"v_mfma_f32_16x16x32_bf16 a[32:35], a[164:167], v[16:19], a[32:35]\n"
"v_perm_b32 v47, v143, v139, s53\n"
"v_mfma_f32_16x16x32_bf16 a[68:71], a[168:171], v[20:23], a[68:71]\n"
"ds_read_b128 a[188:191], v249 offset:37120\n"
"buffer_load_dwordx4 v[228:231], v1, s[24:27], s97 offen offset:2560\n"
"v_mfma_f32_16x16x32_bf16 a[100:103], a[172:175], v[20:23], a[100:103]\n"
"v_mfma_f32_16x16x32_bf16 a[4:7], a[160:163], v[20:23], a[4:7]\n"
"ds_read_b128 a[192:195], v249 offset:45312\n"
"v_mfma_f32_16x16x32_bf16 a[36:39], a[164:167], v[20:23], a[36:39]\n"
"v_mfma_f32_16x16x32_bf16 a[72:75], a[168:171], v[24:27], a[72:75]\n"
"ds_read_b128 a[180:183], v245 offset:37120\n"
"v_mfma_f32_16x16x32_bf16 a[104:107], a[172:175], v[24:27], a[104:107]\n"
"buffer_load_dwordx4 v[232:235], v1, s[24:27], s97 offen offset:3072\n"
"v_mfma_f32_16x16x32_bf16 a[8:11], a[160:163], v[24:27], a[8:11]\n"
"ds_read_b128 a[184:187], v245 offset:45312\n"
"v_mfma_f32_16x16x32_bf16 a[40:43], a[164:167], v[24:27], a[40:43]\n"
"v_mfma_f32_16x16x32_bf16 a[76:79], a[168:171], v[28:31], a[76:79]\n"
"v_perm_b32 v16, v148, v144, s52\n"
"v_mfma_f32_16x16x32_bf16 a[108:111], a[172:175], v[28:31], a[108:111]\n"
"v_perm_b32 v17, v156, v152, s52\n"
"buffer_load_dwordx4 v[236:239], v1, s[24:27], s97 offen offset:3584\n"
"v_mfma_f32_16x16x32_bf16 a[12:15], a[160:163], v[28:31], a[12:15]\n"
"v_perm_b32 v18, v164, v160, s52\n"
"v_mfma_f32_16x16x32_bf16 a[44:47], a[164:167], v[28:31], a[44:47]\n"
"v_perm_b32 v19, v172, v168, s52\n"
"v_mfma_f32_16x16x32_bf16 a[80:83], a[168:171], v[32:35], a[80:83]\n"
"v_perm_b32 v20, v148, v144, s53\n"
"v_perm_b32 v21, v156, v152, s53\n"
"v_mfma_f32_16x16x32_bf16 a[112:115], a[172:175], v[32:35], a[112:115]\n"
"v_perm_b32 v22, v164, v160, s53\n"
"v_perm_b32 v23, v172, v168, s53\n"
"buffer_load_dword a252, v241, s[28:31], 0 offen sc1\n"
"v_mfma_f32_16x16x32_bf16 a[16:19], a[160:163], v[32:35], a[16:19]\n"
"v_perm_b32 v24, v149, v145, s52\n"
"v_perm_b32 v25, v157, v153, s52\n"
"v_mfma_f32_16x16x32_bf16 a[48:51], a[164:167], v[32:35], a[48:51]\n"
"v_perm_b32 v26, v165, v161, s52\n"
"v_perm_b32 v27, v173, v169, s52\n"
"v_mfma_f32_16x16x32_bf16 a[84:87], a[168:171], v[36:39], a[84:87]\n"
"v_perm_b32 v28, v149, v145, s53\n"
"v_mfma_f32_16x16x32_bf16 a[116:119], a[172:175], v[36:39], a[116:119]\n"
"v_perm_b32 v29, v157, v153, s53\n"
"v_mfma_f32_16x16x32_bf16 a[20:23], a[160:163], v[36:39], a[20:23]\n"
"buffer_load_dwordx4 a[216:219], v5, s[28:31], s38 offen sc1\n"
"v_perm_b32 v30, v165, v161, s53\n"
"v_mfma_f32_16x16x32_bf16 a[52:55], a[164:167], v[36:39], a[52:55]\n"
"v_perm_b32 v31, v173, v169, s53\n"
"v_mfma_f32_16x16x32_bf16 a[88:91], a[168:171], v[40:43], a[88:91]\n"
"v_perm_b32 v32, v150, v146, s52\n"
"v_perm_b32 v33, v158, v154, s52\n"
"v_mfma_f32_16x16x32_bf16 a[120:123], a[172:175], v[40:43], a[120:123]\n"
"v_perm_b32 v34, v166, v162, s52\n"
"v_perm_b32 v35, v174, v170, s52\n"
"v_mfma_f32_16x16x32_bf16 a[24:27], a[160:163], v[40:43], a[24:27]\n"
"buffer_load_dwordx4 a[220:223], v5, s[28:31], s38 offen offset:256 sc1\n"
"v_perm_b32 v36, v150, v146, s53\n"
"v_perm_b32 v37, v158, v154, s53\n"
"v_mfma_f32_16x16x32_bf16 a[56:59], a[164:167], v[40:43], a[56:59]\n"
"v_perm_b32 v38, v166, v162, s53\n"
"v_perm_b32 v39, v174, v170, s53\n"
"v_mfma_f32_16x16x32_bf16 a[92:95], a[168:171], v[44:47], a[92:95]\n"
"v_perm_b32 v40, v151, v147, s52\n"
"v_mfma_f32_16x16x32_bf16 a[124:127], a[172:175], v[44:47], a[124:127]\n"
"v_perm_b32 v41, v159, v155, s52\n"
"v_mfma_f32_16x16x32_bf16 a[28:31], a[160:163], v[44:47], a[28:31]\n"
"buffer_load_dwordx4 a[224:227], v5, s[28:31], s38 offen offset:512 sc1\n"
"v_perm_b32 v42, v167, v163, s52\n"
"v_mfma_f32_16x16x32_bf16 a[60:63], a[164:167], v[44:47], a[60:63]\n"
"v_perm_b32 v43, v175, v171, s52\n"
"s_waitcnt lgkmcnt(0)\n"
"s_nop 0\n"
"v_mfma_f32_16x16x32_bf16 a[64:67], a[188:191], v[16:19], a[64:67]\n"
"v_perm_b32 v44, v151, v147, s53\n"
"v_mfma_f32_16x16x32_bf16 a[96:99], a[192:195], v[16:19], a[96:99]\n"
"v_perm_b32 v45, v159, v155, s53\n"
"v_mfma_f32_16x16x32_bf16 a[0:3], a[180:183], v[16:19], a[0:3]\n"
"v_perm_b32 v46, v167, v163, s53\n"
"buffer_load_dwordx4 a[228:231], v5, s[28:31], s38 offen offset:768 sc1\n"
"v_mfma_f32_16x16x32_bf16 a[32:35], a[184:187], v[16:19], a[32:35]\n"
"v_perm_b32 v47, v175, v171, s53\n"
"v_mfma_f32_16x16x32_bf16 a[68:71], a[188:191], v[20:23], a[68:71]\n"
"ds_read_b128 a[168:171], v250 offset:37376\n"
"v_mfma_f32_16x16x32_bf16 a[100:103], a[192:195], v[20:23], a[100:103]\n"
"v_mfma_f32_16x16x32_bf16 a[4:7], a[180:183], v[20:23], a[4:7]\n"
"ds_read_b128 a[172:175], v250 offset:45568\n"
"v_mfma_f32_16x16x32_bf16 a[36:39], a[184:187], v[20:23], a[36:39]\n"
"buffer_load_dwordx4 a[232:235], v5, s[28:31], s38 offen offset:1024 sc1\n"
"v_mfma_f32_16x16x32_bf16 a[72:75], a[188:191], v[24:27], a[72:75]\n"
"ds_read_b128 a[160:163], v246 offset:37376\n"
"v_mfma_f32_16x16x32_bf16 a[104:107], a[192:195], v[24:27], a[104:107]\n"
"v_mfma_f32_16x16x32_bf16 a[8:11], a[180:183], v[24:27], a[8:11]\n"
"ds_read_b128 a[164:167], v246 offset:45568\n"
"v_mfma_f32_16x16x32_bf16 a[40:43], a[184:187], v[24:27], a[40:43]\n"
"v_mfma_f32_16x16x32_bf16 a[76:79], a[188:191], v[28:31], a[76:79]\n"
"buffer_load_dwordx4 a[236:239], v5, s[28:31], s38 offen offset:1280 sc1\n"
"s_waitcnt vmcnt(15)\n"
"s_nop 0\n"
"v_perm_b32 v16, v180, v176, s52\n"
"v_mfma_f32_16x16x32_bf16 a[108:111], a[192:195], v[28:31], a[108:111]\n"
"v_perm_b32 v17, v188, v184, s52\n"
"v_mfma_f32_16x16x32_bf16 a[12:15], a[180:183], v[28:31], a[12:15]\n"
"v_perm_b32 v18, v196, v192, s52\n"
"v_mfma_f32_16x16x32_bf16 a[44:47], a[184:187], v[28:31], a[44:47]\n"
"v_perm_b32 v19, v204, v200, s52\n"
"v_mfma_f32_16x16x32_bf16 a[80:83], a[188:191], v[32:35], a[80:83]\n"
"buffer_load_dwordx4 a[240:243], v5, s[28:31], s38 offen offset:1536 sc1\n"
"v_perm_b32 v20, v180, v176, s53\n"
"v_perm_b32 v21, v188, v184, s53\n"
"v_mfma_f32_16x16x32_bf16 a[112:115], a[192:195], v[32:35], a[112:115]\n"
"v_perm_b32 v22, v196, v192, s53\n"
"v_perm_b32 v23, v204, v200, s53\n"
"v_mfma_f32_16x16x32_bf16 a[16:19], a[180:183], v[32:35], a[16:19]\n"
"v_perm_b32 v24, v181, v177, s52\n"
"v_perm_b32 v25, v189, v185, s52\n"
"v_mfma_f32_16x16x32_bf16 a[48:51], a[184:187], v[32:35], a[48:51]\n"
"v_perm_b32 v26, v197, v193, s52\n"
"v_perm_b32 v27, v205, v201, s52\n"
"buffer_load_dwordx4 a[244:247], v5, s[28:31], s38 offen offset:1792 sc1\n"
"v_mfma_f32_16x16x32_bf16 a[84:87], a[188:191], v[36:39], a[84:87]\n"
"v_perm_b32 v28, v181, v177, s53\n"
"v_mfma_f32_16x16x32_bf16 a[116:119], a[192:195], v[36:39], a[116:119]\n"
"v_perm_b32 v29, v189, v185, s53\n"
"v_mfma_f32_16x16x32_bf16 a[20:23], a[180:183], v[36:39], a[20:23]\n"
"v_perm_b32 v30, v197, v193, s53\n"
"v_mfma_f32_16x16x32_bf16 a[52:55], a[184:187], v[36:39], a[52:55]\n"
"v_perm_b32 v31, v205, v201, s53\n"
"v_mfma_f32_16x16x32_bf16 a[88:91], a[188:191], v[40:43], a[88:91]\n"
"buffer_load_dword v253, v4, s[28:31], s36 offen offset:12 sc1\n"
"v_perm_b32 v32, v182, v178, s52\n"
"v_perm_b32 v33, v190, v186, s52\n"
"v_mfma_f32_16x16x32_bf16 a[120:123], a[192:195], v[40:43], a[120:123]\n"
"v_perm_b32 v34, v198, v194, s52\n"
"v_perm_b32 v35, v206, v202, s52\n"
"v_mfma_f32_16x16x32_bf16 a[24:27], a[180:183], v[40:43], a[24:27]\n"
"v_perm_b32 v36, v182, v178, s53\n"
"v_perm_b32 v37, v190, v186, s53\n"
"v_mfma_f32_16x16x32_bf16 a[56:59], a[184:187], v[40:43], a[56:59]\n"
"v_perm_b32 v38, v198, v194, s53\n"
"v_perm_b32 v39, v206, v202, s53\n"
"buffer_load_dword v254, v11, s[28:31], 0 offen sc1\n"
"v_mfma_f32_16x16x32_bf16 a[92:95], a[188:191], v[44:47], a[92:95]\n"
"v_perm_b32 v40, v183, v179, s52\n"
"v_mfma_f32_16x16x32_bf16 a[124:127], a[192:195], v[44:47], a[124:127]\n"
"v_perm_b32 v41, v191, v187, s52\n"
"v_mfma_f32_16x16x32_bf16 a[28:31], a[180:183], v[44:47], a[28:31]\n"
"v_perm_b32 v42, v199, v195, s52\n"
"v_mfma_f32_16x16x32_bf16 a[60:63], a[184:187], v[44:47], a[60:63]\n"
"v_perm_b32 v43, v207, v203, s52\n"
"s_waitcnt lgkmcnt(0)\n"
"s_nop 0\n"
"v_mfma_f32_16x16x32_bf16 a[64:67], a[168:171], v[16:19], a[64:67]\n"
"s_mov_b32 s96, 0x60000\n"
"buffer_load_dwordx4 a[200:203], v7, s[28:31], s96 offen sc1\n"
"v_perm_b32 v44, v183, v179, s53\n"
"v_mfma_f32_16x16x32_bf16 a[96:99], a[172:175], v[16:19], a[96:99]\n"
"v_perm_b32 v45, v191, v187, s53\n"
"v_mfma_f32_16x16x32_bf16 a[0:3], a[160:163], v[16:19], a[0:3]\n"
"v_perm_b32 v46, v199, v195, s53\n"
"v_mfma_f32_16x16x32_bf16 a[32:35], a[164:167], v[16:19], a[32:35]\n"
"v_perm_b32 v47, v207, v203, s53\n"
"v_mfma_f32_16x16x32_bf16 a[68:71], a[168:171], v[20:23], a[68:71]\n"
"ds_read_b128 a[188:191], v251 offset:37376\n"
"v_mfma_f32_16x16x32_bf16 a[100:103], a[172:175], v[20:23], a[100:103]\n"
"v_mfma_f32_16x16x32_bf16 a[4:7], a[160:163], v[20:23], a[4:7]\n"
"ds_read_b128 a[192:195], v251 offset:45568\n"
"v_mfma_f32_16x16x32_bf16 a[36:39], a[164:167], v[20:23], a[36:39]\n"
"v_mfma_f32_16x16x32_bf16 a[72:75], a[168:171], v[24:27], a[72:75]\n"
"ds_read_b128 a[180:183], v247 offset:37376\n"
"v_mfma_f32_16x16x32_bf16 a[104:107], a[172:175], v[24:27], a[104:107]\n"
"v_mfma_f32_16x16x32_bf16 a[8:11], a[160:163], v[24:27], a[8:11]\n"
"ds_read_b128 a[184:187], v247 offset:45568\n"
"v_mfma_f32_16x16x32_bf16 a[40:43], a[164:167], v[24:27], a[40:43]\n"
"v_mfma_f32_16x16x32_bf16 a[76:79], a[168:171], v[28:31], a[76:79]\n"
"s_waitcnt vmcnt(12)\n"
"s_nop 0\n"
"v_perm_b32 v16, v212, v208, s52\n"
"v_mfma_f32_16x16x32_bf16 a[108:111], a[172:175], v[28:31], a[108:111]\n"
"v_perm_b32 v17, v220, v216, s52\n"
"v_mfma_f32_16x16x32_bf16 a[12:15], a[160:163], v[28:31], a[12:15]\n"
"v_perm_b32 v18, v228, v224, s52\n"
"v_mfma_f32_16x16x32_bf16 a[44:47], a[164:167], v[28:31], a[44:47]\n"
"v_perm_b32 v19, v236, v232, s52\n"
"v_mfma_f32_16x16x32_bf16 a[80:83], a[168:171], v[32:35], a[80:83]\n"
"v_perm_b32 v20, v212, v208, s53\n"
"v_perm_b32 v21, v220, v216, s53\n"
"v_mfma_f32_16x16x32_bf16 a[112:115], a[172:175], v[32:35], a[112:115]\n"
"v_perm_b32 v22, v228, v224, s53\n"
"v_perm_b32 v23, v236, v232, s53\n"
"v_mfma_f32_16x16x32_bf16 a[16:19], a[160:163], v[32:35], a[16:19]\n"
"v_perm_b32 v24, v213, v209, s52\n"
"v_perm_b32 v25, v221, v217, s52\n"
"v_mfma_f32_16x16x32_bf16 a[48:51], a[164:167], v[32:35], a[48:51]\n"
"v_perm_b32 v26, v229, v225, s52\n"
"v_perm_b32 v27, v237, v233, s52\n"
"v_mfma_f32_16x16x32_bf16 a[84:87], a[168:171], v[36:39], a[84:87]\n"
"v_perm_b32 v28, v213, v209, s53\n"
"v_mfma_f32_16x16x32_bf16 a[116:119], a[172:175], v[36:39], a[116:119]\n"
"v_perm_b32 v29, v221, v217, s53\n"
"v_mfma_f32_16x16x32_bf16 a[20:23], a[160:163], v[36:39], a[20:23]\n"
"v_perm_b32 v30, v229, v225, s53\n"
"v_mfma_f32_16x16x32_bf16 a[52:55], a[164:167], v[36:39], a[52:55]\n"
"v_perm_b32 v31, v237, v233, s53\n"
"v_mfma_f32_16x16x32_bf16 a[88:91], a[168:171], v[40:43], a[88:91]\n"
"v_perm_b32 v32, v214, v210, s52\n"
"v_perm_b32 v33, v222, v218, s52\n"
"v_mfma_f32_16x16x32_bf16 a[120:123], a[172:175], v[40:43], a[120:123]\n"
"v_perm_b32 v34, v230, v226, s52\n"
"v_perm_b32 v35, v238, v234, s52\n"
"v_mfma_f32_16x16x32_bf16 a[24:27], a[160:163], v[40:43], a[24:27]\n"
"v_perm_b32 v36, v214, v210, s53\n"
"v_perm_b32 v37, v222, v218, s53\n"
"v_mfma_f32_16x16x32_bf16 a[56:59], a[164:167], v[40:43], a[56:59]\n"
"v_perm_b32 v38, v230, v226, s53\n"
"v_perm_b32 v39, v238, v234, s53\n"
"v_mfma_f32_16x16x32_bf16 a[92:95], a[168:171], v[44:47], a[92:95]\n"
"v_perm_b32 v40, v215, v211, s52\n"
"v_mfma_f32_16x16x32_bf16 a[124:127], a[172:175], v[44:47], a[124:127]\n"
"v_perm_b32 v41, v223, v219, s52\n"
"v_mfma_f32_16x16x32_bf16 a[28:31], a[160:163], v[44:47], a[28:31]\n"
"v_perm_b32 v42, v231, v227, s52\n"
"v_mfma_f32_16x16x32_bf16 a[60:63], a[164:167], v[44:47], a[60:63]\n"
"v_perm_b32 v43, v239, v235, s52\n"
"s_waitcnt lgkmcnt(0)\n"
"s_nop 0\n"
"v_mfma_f32_16x16x32_bf16 a[64:67], a[188:191], v[16:19], a[64:67]\n"
"v_perm_b32 v44, v215, v211, s53\n"
"v_mfma_f32_16x16x32_bf16 a[96:99], a[192:195], v[16:19], a[96:99]\n"
"v_perm_b32 v45, v223, v219, s53\n"
"v_mfma_f32_16x16x32_bf16 a[0:3], a[180:183], v[16:19], a[0:3]\n"
"v_perm_b32 v46, v231, v227, s53\n"
"v_mfma_f32_16x16x32_bf16 a[32:35], a[184:187], v[16:19], a[32:35]\n"
"v_perm_b32 v47, v239, v235, s53\n"
"v_mfma_f32_16x16x32_bf16 a[68:71], a[188:191], v[20:23], a[68:71]\n"
"v_mfma_f32_16x16x32_bf16 a[100:103], a[192:195], v[20:23], a[100:103]\n"
"v_mfma_f32_16x16x32_bf16 a[4:7], a[180:183], v[20:23], a[4:7]\n"
"v_mfma_f32_16x16x32_bf16 a[36:39], a[184:187], v[20:23], a[36:39]\n"
"v_mfma_f32_16x16x32_bf16 a[72:75], a[188:191], v[24:27], a[72:75]\n"
"ds_write_b128 v6, a[64:67]\n"
"v_mfma_f32_16x16x32_bf16 a[104:107], a[192:195], v[24:27], a[104:107]\n"
"v_mfma_f32_16x16x32_bf16 a[8:11], a[180:183], v[24:27], a[8:11]\n"
"ds_write_b128 v6, a[96:99] offset:8192\n"
"buffer_load_dwordx4 a[204:207], v7, s[28:31], s96 offen offset:1024 sc1\n"
"v_mfma_f32_16x16x32_bf16 a[40:43], a[184:187], v[24:27], a[40:43]\n"
"v_mfma_f32_16x16x32_bf16 a[76:79], a[188:191], v[28:31], a[76:79]\n"
"ds_write_b128 v6, a[68:71] offset:1024\n"
"v_mfma_f32_16x16x32_bf16 a[108:111], a[192:195], v[28:31], a[108:111]\n"
"v_mfma_f32_16x16x32_bf16 a[12:15], a[180:183], v[28:31], a[12:15]\n"
"buffer_load_dwordx4 a[208:211], v7, s[28:31], s96 offen offset:2048 sc1\n"
"ds_write_b128 v6, a[100:103] offset:9216\n"
"v_mfma_f32_16x16x32_bf16 a[44:47], a[184:187], v[28:31], a[44:47]\n"
"v_mfma_f32_16x16x32_bf16 a[80:83], a[188:191], v[32:35], a[80:83]\n"
"ds_write_b128 v6, a[72:75] offset:2048\n"
"buffer_load_dwordx4 a[212:215], v7, s[28:31], s96 offen offset:3072 sc1\n"
"v_mfma_f32_16x16x32_bf16 a[112:115], a[192:195], v[32:35], a[112:115]\n"
"v_mfma_f32_16x16x32_bf16 a[16:19], a[180:183], v[32:35], a[16:19]\n"
"ds_write_b128 v6, a[104:107] offset:10240\n"
"v_mfma_f32_16x16x32_bf16 a[48:51], a[184:187], v[32:35], a[48:51]\n"
"v_mfma_f32_16x16x32_bf16 a[84:87], a[188:191], v[36:39], a[84:87]\n"
"s_mov_b32 s97, 0x64000\n"
"buffer_load_dwordx4 v[48:51], v7, s[28:31], s97 offen sc1\n"
"ds_write_b128 v6, a[76:79] offset:3072\n"
"v_mfma_f32_16x16x32_bf16 a[116:119], a[192:195], v[36:39], a[116:119]\n"
"v_mfma_f32_16x16x32_bf16 a[20:23], a[180:183], v[36:39], a[20:23]\n"
"ds_write_b128 v6, a[108:111] offset:11264\n"
"buffer_load_dwordx4 v[52:55], v7, s[28:31], s97 offen offset:1024 sc1\n"
"v_mfma_f32_16x16x32_bf16 a[52:55], a[184:187], v[36:39], a[52:55]\n"
"v_mfma_f32_16x16x32_bf16 a[88:91], a[188:191], v[40:43], a[88:91]\n"
"ds_write_b128 v6, a[80:83] offset:4096\n"
"v_mfma_f32_16x16x32_bf16 a[120:123], a[192:195], v[40:43], a[120:123]\n"
"v_mfma_f32_16x16x32_bf16 a[24:27], a[180:183], v[40:43], a[24:27]\n"
"buffer_load_dwordx4 v[56:59], v7, s[28:31], s97 offen offset:2048 sc1\n"
"ds_write_b128 v6, a[112:115] offset:12288\n"
"v_mfma_f32_16x16x32_bf16 a[56:59], a[184:187], v[40:43], a[56:59]\n"
"v_mfma_f32_16x16x32_bf16 a[92:95], a[188:191], v[44:47], a[92:95]\n"
"ds_write_b128 v6, a[84:87] offset:5120\n"
"v_mfma_f32_16x16x32_bf16 a[124:127], a[192:195], v[44:47], a[124:127]\n"
"buffer_load_dwordx4 v[60:63], v7, s[28:31], s97 offen offset:3072 sc1\n"
"v_mfma_f32_16x16x32_bf16 a[28:31], a[180:183], v[44:47], a[28:31]\n"
"ds_write_b128 v6, a[116:119] offset:13312\n"
"v_mfma_f32_16x16x32_bf16 a[60:63], a[184:187], v[44:47], a[60:63]\n"
"ds_write_b128 v6, a[88:91] offset:6144\n"
"s_mov_b32 s96, 0x68000\n"
"buffer_load_dwordx4 v[64:67], v7, s[28:31], s96 offen sc1\n"
"ds_write_b128 v6, a[120:123] offset:14336\n"
"ds_write_b128 v6, a[92:95] offset:7168\n"
"ds_write_b128 v6, a[124:127] offset:15360\n"
"buffer_load_dwordx4 v[68:71], v7, s[28:31], s96 offen offset:1024 sc1\n"
"v_subrev_u32 v1, 0x12000, v6\n"
"v_xor_b32 v1, 0x8000, v1\n"
"v_add_u32 v1, 0x12000, v1\n"
"buffer_load_dwordx4 v[72:75], v7, s[28:31], s96 offen offset:2048 sc1\n"
"s_waitcnt vmcnt(11)\n"
"v_cmp_ne_u32 vcc, 0, v254\n"
"buffer_load_dwordx4 v[76:79], v7, s[28:31], s96 offen offset:3072 sc1\n"
"s_nop 0\n"
"s_mov_b32 s98, vcc_lo\n"
"v_cmp_ne_u32 vcc, v253, v243\n"
"s_nop 0\n"
"s_and_b32 s58, vcc_lo, s4\n"
"s_cbranch_scc1 .LBspinn\n"
".LBgotn:\n"
"v_mov_b32_e64 v6, v252\n"
"s_waitcnt lgkmcnt(0)\n"
"s_barrier\n"
"s_nop 3\n"
"s_branch .Ljoin\n"
".Lspin:\n"
"buffer_load_dword v243, v4, s[28:31], 0 offen sc1\n"
"s_waitcnt vmcnt(0)\n"
"buffer_store_dwordx2 v[242:243], v4, s[40:43], s98 offen sc1\n"
"buffer_store_dwordx2 v[242:243], v4, s[28:31], s36 offen sc1\n"
"s_sleep 8\n"
"buffer_load_dword v5, v4, s[28:31], s37 offen sc1\n"
"s_waitcnt vmcnt(0)\n"
"buffer_load_dword v11, v4, s[28:31], 0 offen offset:4 sc1\n"
"s_waitcnt vmcnt(0)\n"
"s_add_u32 s39, s39, 1\n"
"s_cmp_gt_u32 s39, 16384\n"
"s_cbranch_scc1 .Lacked\n"
"v_xor_b32 v5, v5, v242\n"
"v_xor_b32 v11, v11, v243\n"
"v_or_b32_e64 v5, v5, v11\n"
"v_cmp_ne_u32 vcc, 0, v5\n"
"s_nop 1\n"
"s_and_b32 s58, vcc_lo, 1\n"
"s_cbranch_scc1 .Lspin\n"
"s_branch .Lacked\n"
".Lspinn:\n"
"s_sleep 4\n"
"buffer_load_dword v253, v4, s[28:31], s36 offen offset:12 sc1\n"
"s_waitcnt vmcnt(0)\n"
"s_add_u32 s39, s39, 1\n"
"s_cmp_gt_u32 s39, 16384\n"
"s_cbranch_scc1 .Lgotn\n"
"v_cmp_ne_u32 vcc, v253, v243\n"
"s_nop 1\n"
"s_and_b32 s58, vcc_lo, 1\n"
"s_cbranch_scc1 .Lspinn\n"
"s_branch .Lgotn\n"
".LBspin:\n"
"buffer_load_dword v243, v4, s[28:31], 0 offen sc1\n"
"s_waitcnt vmcnt(0)\n"
"buffer_store_dwordx2 v[242:243], v4, s[40:43], s98 offen sc1\n"
"buffer_store_dwordx2 v[242:243], v4, s[28:31], s36 offen sc1\n"
"s_sleep 8\n"
"buffer_load_dword v5, v4, s[28:31], s37 offen sc1\n"
"s_waitcnt vmcnt(0)\n"
"buffer_load_dword v11, v4, s[28:31], 0 offen offset:4 sc1\n"
"s_waitcnt vmcnt(0)\n"
"s_add_u32 s39, s39, 1\n"
"s_cmp_gt_u32 s39, 16384\n"
"s_cbranch_scc1 .LBacked\n"
"v_xor_b32 v5, v5, v242\n"
"v_xor_b32 v11, v11, v243\n"
"v_or_b32_e64 v5, v5, v11\n"
"v_cmp_ne_u32 vcc, 0, v5\n"
"s_nop 1\n"
"s_and_b32 s58, vcc_lo, 1\n"
"s_cbranch_scc1 .LBspin\n"
"s_branch .LBacked\n"
".LBspinn:\n"
"s_sleep 4\n"
"buffer_load_dword v253, v4, s[28:31], s36 offen offset:12 sc1\n"
"s_waitcnt vmcnt(0)\n"
"s_add_u32 s39, s39, 1\n"
"s_cmp_gt_u32 s39, 16384\n"
"s_cbranch_scc1 .LBgotn\n"
"v_cmp_ne_u32 vcc, v253, v243\n"
"s_nop 1\n"
"s_and_b32 s58, vcc_lo, 1\n"
"s_cbranch_scc1 .LBspinn\n"
"s_branch .LBgotn\n"
".Lspinh:\n"
"s_sleep 4\n"
"buffer_load_dwordx4 v[252:255], v6, s[28:31], s6 offen sc1\n"
"s_waitcnt vmcnt(0)\n"
"s_add_u32 s39, s39, 1\n"
"s_cmp_gt_u32 s39, 16384\n"
"s_cbranch_scc1 .Lgoth\n"
"v_xor_b32 v253, 0x5a5a5a5a, v253\n"
"v_xor_b32 v255, 0x5a5a5a5a, v255\n"
"v_or_b32 v253, v253, v255\n"
"v_cmp_ne_u32 vcc, 0, v253\n"
"s_nop 1\n"
"s_cmp_lg_u64 vcc, 0\n"
"s_cbranch_scc1 .Lspinh\n"
"s_branch .Lgoth\n"
".Lslow_path:\n"
"buffer_load_dwordx4 v7, s[28:31], s90 offen offset:3072 sc1 lds\n"
"s_add_u32 m0, s95, 0\n"
"s_nop 0\n"
"s_mov_b32 s90, 0xc0000\n"
"buffer_load_dwordx4 v7, s[28:31], s90 offen sc1 lds\n"
"buffer_load_dwordx4 v7, s[28:31], s90 offen offset:1024 sc1 lds\n"
"buffer_load_dwordx4 v7, s[28:31], s90 offen offset:2048 sc1 lds\n"
"buffer_load_dwordx4 v7, s[28:31], s90 offen offset:3072 sc1 lds\n"
"s_add_u32 m0, s95, 0x4000\n"
"s_mov_b32 s90, 0xc4000\n"
"buffer_load_dwordx4 v7, s[28:31], s90 offen sc1 lds\n"
"buffer_load_dwordx4 v7, s[28:31], s90 offen offset:1024 sc1 lds\n"
"buffer_load_dwordx4 v7, s[28:31], s90 offen offset:2048 sc1 lds\n"
"buffer_load_dwordx4 v7, s[28:31], s90 offen offset:3072 sc1 lds\n"
"s_add_u32 m0, s95, 0x8000\n"
"s_mov_b32 s90, 0xc8000\n"
"buffer_load_dwordx4 v7, s[28:31], s90 offen sc1 lds\n"
"buffer_load_dwordx4 v7, s[28:31], s90 offen offset:1024 sc1 lds\n"
"buffer_load_dwordx4 v7, s[28:31], s90 offen offset:2048 sc1 lds\n"
"buffer_load_dwordx4 v7, s[28:31], s90 offen offset:3072 sc1 lds\n"
"s_sub_u32 s38, s38, 0x2000\n"
"buffer_load_dwordx4 v[144:147], v243, s[28:31], s38 offen sc1\n"
"buffer_load_dwordx4 v[148:151], v243, s[28:31], s38 offen offset:256 sc1\n"
"buffer_load_dwordx4 v[152:155], v243, s[28:31], s38 offen offset:512 sc1\n"
"buffer_load_dwordx4 v[156:159], v243, s[28:31], s38 offen offset:768 sc1\n"
"buffer_load_dwordx4 v[160:163], v243, s[28:31], s38 offen offset:1024 sc1\n"
"buffer_load_dwordx4 v[164:167], v243, s[28:31], s38 offen offset:1280 sc1\n"
"buffer_load_dwordx4 v[168:171], v243, s[28:31], s38 offen offset:1536 sc1\n"
"buffer_load_dwordx4 v[172:175], v243, s[28:31], s38 offen offset:1792 sc1\n"
"s_waitcnt vmcnt(7)\n"
"v_pk_add_f32 v[16:17], v[144:145], v[208:209] op_sel:[0,0] op_sel_hi:[1,0]\n"
"v_pk_add_f32 v[18:19], v[146:147], v[208:209] op_sel:[0,0] op_sel_hi:[1,0]\n"
"s_waitcnt vmcnt(5)\n"
"v_pk_add_f32 v[20:21], v[152:153], v[212:213] op_sel:[0,0] op_sel_hi:[1,0]\n"
"v_pk_add_f32 v[22:23], v[154:155], v[212:213] op_sel:[0,0] op_sel_hi:[1,0]\n"
"s_waitcnt vmcnt(3)\n"
"v_pk_add_f32 v[24:25], v[160:161], v[216:217] op_sel:[0,0] op_sel_hi:[1,0]\n"
"v_pk_add_f32 v[26:27], v[162:163], v[216:217] op_sel:[0,0] op_sel_hi:[1,0]\n"
"s_waitcnt vmcnt(1)\n"
"v_pk_add_f32 v[28:29], v[168:169], v[220:221] op_sel:[0,0] op_sel_hi:[1,0]\n"
"v_pk_add_f32 v[30:31], v[170:171], v[220:221] op_sel:[0,0] op_sel_hi:[1,0]\n"
"v_pk_add_f32 v[32:33], v[148:149], v[224:225] op_sel:[0,0] op_sel_hi:[1,0]\n"
"v_pk_add_f32 v[34:35], v[150:151], v[224:225] op_sel:[0,0] op_sel_hi:[1,0]\n"
"v_pk_add_f32 v[36:37], v[156:157], v[228:229] op_sel:[0,0] op_sel_hi:[1,0]\n"
"v_pk_add_f32 v[38:39], v[158:159], v[228:229] op_sel:[0,0] op_sel_hi:[1,0]\n"
"v_pk_add_f32 v[40:41], v[164:165], v[232:233] op_sel:[0,0] op_sel_hi:[1,0]\n"
"v_pk_add_f32 v[42:43], v[166:167], v[232:233] op_sel:[0,0] op_sel_hi:[1,0]\n"
"s_waitcnt vmcnt(0)\n"
"v_pk_add_f32 v[44:45], v[172:173], v[236:237] op_sel:[0,0] op_sel_hi:[1,0]\n"
"v_pk_add_f32 v[46:47], v[174:175], v[236:237] op_sel:[0,0] op_sel_hi:[1,0]\n"
"v_exp_f32 v16, v16\n"
"v_exp_f32 v17, v17\n"
"v_exp_f32 v18, v18\n"
"v_exp_f32 v19, v19\n"
"v_exp_f32 v20, v20\n"
"v_exp_f32 v21, v21\n"
"v_exp_f32 v22, v22\n"
"v_exp_f32 v23, v23\n"
"v_exp_f32 v24, v24\n"
"v_exp_f32 v25, v25\n"
"v_exp_f32 v26, v26\n"
"v_exp_f32 v27, v27\n"
"v_exp_f32 v28, v28\n"
"v_exp_f32 v29, v29\n"
"v_exp_f32 v30, v30\n"
"v_exp_f32 v31, v31\n"
"v_exp_f32 v32, v32\n"
"v_exp_f32 v33, v33\n"
"v_exp_f32 v34, v34\n"
"v_exp_f32 v35, v35\n"
"v_exp_f32 v36, v36\n"
"v_exp_f32 v37, v37\n"
"v_exp_f32 v38, v38\n"
"v_exp_f32 v39, v39\n"
"v_exp_f32 v40, v40\n"
"v_exp_f32 v41, v41\n"
"v_exp_f32 v42, v42\n"
"v_exp_f32 v43, v43\n"
"v_exp_f32 v44, v44\n"
"v_exp_f32 v45, v45\n"
"v_exp_f32 v46, v46\n"
"v_exp_f32 v47, v47\n"
"v_sub_f32 v16, -0.5, v16\n"
"v_sub_f32 v17, -0.5, v17\n"
"v_sub_f32 v18, -0.5, v18\n"
"v_sub_f32 v19, -0.5, v19\n"
"v_sub_f32 v20, -0.5, v20\n"
"v_sub_f32 v21, -0.5, v21\n"
"v_sub_f32 v22, -0.5, v22\n"
"v_sub_f32 v23, -0.5, v23\n"
"v_sub_f32 v24, -0.5, v24\n"
"v_sub_f32 v25, -0.5, v25\n"
"v_sub_f32 v26, -0.5, v26\n"
"v_sub_f32 v27, -0.5, v27\n"
"v_sub_f32 v28, -0.5, v28\n"
"v_sub_f32 v29, -0.5, v29\n"
"v_sub_f32 v30, -0.5, v30\n"
"v_sub_f32 v31, -0.5, v31\n"
"v_sub_f32 v32, -0.5, v32\n"
"v_sub_f32 v33, -0.5, v33\n"
"v_sub_f32 v34, -0.5, v34\n"
"v_sub_f32 v35, -0.5, v35\n"
"v_sub_f32 v36, -0.5, v36\n"
"v_sub_f32 v37, -0.5, v37\n"
"v_sub_f32 v38, -0.5, v38\n"
"v_sub_f32 v39, -0.5, v39\n"
"v_sub_f32 v40, -0.5, v40\n"
"v_sub_f32 v41, -0.5, v41\n"
"v_sub_f32 v42, -0.5, v42\n"
"v_sub_f32 v43, -0.5, v43\n"
"v_sub_f32 v44, -0.5, v44\n"
"v_sub_f32 v45, -0.5, v45\n"
"v_sub_f32 v46, -0.5, v46\n"
"v_sub_f32 v47, -0.5, v47\n"
"v_rcp_f32 v16, v16\n"
"v_rcp_f32 v20, v20\n"
"v_rcp_f32 v24, v24\n"
"v_rcp_f32 v28, v28\n"
"v_rcp_f32 v17, v17\n"
"v_rcp_f32 v21, v21\n"
"v_rcp_f32 v25, v25\n"
"v_rcp_f32 v29, v29\n"
"v_rcp_f32 v18, v18\n"
"v_rcp_f32 v22, v22\n"
"v_rcp_f32 v26, v26\n"
"v_rcp_f32 v30, v30\n"
"v_rcp_f32 v19, v19\n"
"v_rcp_f32 v23, v23\n"
"v_rcp_f32 v27, v27\n"
"v_rcp_f32 v31, v31\n"
"v_rcp_f32 v32, v32\n"
"v_rcp_f32 v36, v36\n"
"v_rcp_f32 v40, v40\n"
"v_rcp_f32 v44, v44\n"
"v_rcp_f32 v33, v33\n"
"v_rcp_f32 v37, v37\n"
"v_rcp_f32 v41, v41\n"
"v_rcp_f32 v45, v45\n"
"v_rcp_f32 v34, v34\n"
"v_rcp_f32 v38, v38\n"
"v_rcp_f32 v42, v42\n"
"v_rcp_f32 v46, v46\n"
"v_rcp_f32 v35, v35\n"
"v_rcp_f32 v39, v39\n"
"v_rcp_f32 v43, v43\n"
"v_rcp_f32_e64 v47, v47\n"
"v_pk_add_f32 v[16:17], v[16:17], 1.0 op_sel_hi:[1,0]\n"
"v_pk_add_f32 v[18:19], v[18:19], 1.0 op_sel_hi:[1,0]\n"
"v_pk_add_f32 v[20:21], v[20:21], 1.0 op_sel_hi:[1,0]\n"
"v_pk_add_f32 v[22:23], v[22:23], 1.0 op_sel_hi:[1,0]\n"
"v_pk_add_f32 v[24:25], v[24:25], 1.0 op_sel_hi:[1,0]\n"
"v_pk_add_f32 v[26:27], v[26:27], 1.0 op_sel_hi:[1,0]\n"
"v_pk_add_f32 v[28:29], v[28:29], 1.0 op_sel_hi:[1,0]\n"
"v_pk_add_f32 v[30:31], v[30:31], 1.0 op_sel_hi:[1,0]\n"
"v_pk_add_f32 v[32:33], v[32:33], 1.0 op_sel_hi:[1,0]\n"
"v_pk_add_f32 v[34:35], v[34:35], 1.0 op_sel_hi:[1,0]\n"
"v_pk_add_f32 v[36:37], v[36:37], 1.0 op_sel_hi:[1,0]\n"
"v_pk_add_f32 v[38:39], v[38:39], 1.0 op_sel_hi:[1,0]\n"
"v_pk_add_f32 v[40:41], v[40:41], 1.0 op_sel_hi:[1,0]\n"
"v_pk_add_f32 v[42:43], v[42:43], 1.0 op_sel_hi:[1,0]\n"
"v_pk_add_f32 v[44:45], v[44:45], 1.0 op_sel_hi:[1,0]\n"
"v_pk_add_f32 v[46:47], v[46:47], 1.0 op_sel_hi:[1,0]\n"
"v_cvt_pk_bf16_f32 v176, v16, v20\n"
"v_cvt_pk_bf16_f32 v177, v24, v28\n"
"ds_write_b64 v8, v[176:177]\n"
"v_cvt_pk_bf16_f32 v178, v17, v21\n"
"v_cvt_pk_bf16_f32 v179, v25, v29\n"
"ds_write_b64 v9, v[178:179] offset:2048\n"
"v_cvt_pk_bf16_f32 v180, v18, v22\n"
"v_cvt_pk_bf16_f32 v181, v26, v30\n"
"ds_write_b64 v10, v[180:181] offset:4096\n"
"v_cvt_pk_bf16_f32 v182, v19, v23\n"
"v_cvt_pk_bf16_f32 v183, v27, v31\n"
"ds_write_b64 v11, v[182:183] offset:6144\n"
"v_cvt_pk_bf16_f32 v184, v32, v36\n"
"v_cvt_pk_bf16_f32 v185, v40, v44\n"
"ds_write_b64 v8, v[184:185] offset:8192\n"
"v_cvt_pk_bf16_f32 v186, v33, v37\n"
"v_cvt_pk_bf16_f32 v187, v41, v45\n"
"ds_write_b64 v9, v[186:187] offset:10240\n"
"v_cvt_pk_bf16_f32 v188, v34, v38\n"
"v_cvt_pk_bf16_f32 v189, v42, v46\n"
"ds_write_b64 v10, v[188:189] offset:12288\n"
"v_cvt_pk_bf16_f32 v190, v35, v39\n"
"v_cvt_pk_bf16_f32 v191, v43, v47\n"
"ds_write_b64 v11, v[190:191] offset:14336\n"
"v_pk_add_f32 v[16:17], v[144:145], v[208:209] op_sel:[0,1] op_sel_hi:[1,1]\n"
"v_pk_add_f32 v[18:19], v[146:147], v[208:209] op_sel:[0,1] op_sel_hi:[1,1]\n"
"v_pk_add_f32 v[20:21], v[152:153], v[212:213] op_sel:[0,1] op_sel_hi:[1,1]\n"
"v_pk_add_f32 v[22:23], v[154:155], v[212:213] op_sel:[0,1] op_sel_hi:[1,1]\n"
"v_pk_add_f32 v[24:25], v[160:161], v[216:217] op_sel:[0,1] op_sel_hi:[1,1]\n"
"v_pk_add_f32 v[26:27], v[162:163], v[216:217] op_sel:[0,1] op_sel_hi:[1,1]\n"
"v_pk_add_f32 v[28:29], v[168:169], v[220:221] op_sel:[0,1] op_sel_hi:[1,1]\n"
"v_pk_add_f32 v[30:31], v[170:171], v[220:221] op_sel:[0,1] op_sel_hi:[1,1]\n"
"v_pk_add_f32 v[32:33], v[148:149], v[224:225] op_sel:[0,1] op_sel_hi:[1,1]\n"
"v_pk_add_f32 v[34:35], v[150:151], v[224:225] op_sel:[0,1] op_sel_hi:[1,1]\n"
"v_pk_add_f32 v[36:37], v[156:157], v[228:229] op_sel:[0,1] op_sel_hi:[1,1]\n"
"v_pk_add_f32 v[38:39], v[158:159], v[228:229] op_sel:[0,1] op_sel_hi:[1,1]\n"
"v_pk_add_f32 v[40:41], v[164:165], v[232:233] op_sel:[0,1] op_sel_hi:[1,1]\n"
"v_pk_add_f32 v[42:43], v[166:167], v[232:233] op_sel:[0,1] op_sel_hi:[1,1]\n"
"v_pk_add_f32 v[44:45], v[172:173], v[236:237] op_sel:[0,1] op_sel_hi:[1,1]\n"
"v_pk_add_f32 v[46:47], v[174:175], v[236:237] op_sel:[0,1] op_sel_hi:[1,1]\n"
"v_exp_f32 v16, v16\n"
"v_exp_f32 v17, v17\n"
"v_exp_f32 v18, v18\n"
"v_exp_f32 v19, v19\n"
"v_exp_f32 v20, v20\n"
"v_exp_f32 v21, v21\n"
"v_exp_f32 v22, v22\n"
"v_exp_f32 v23, v23\n"
"v_exp_f32 v24, v24\n"
"v_exp_f32 v25, v25\n"
"v_exp_f32 v26, v26\n"
"v_exp_f32 v27, v27\n"
"v_exp_f32 v28, v28\n"
"v_exp_f32 v29, v29\n"
"v_exp_f32 v30, v30\n"
"v_exp_f32 v31, v31\n"
"v_exp_f32 v32, v32\n"
"v_exp_f32 v33, v33\n"
"v_exp_f32 v34, v34\n"
"v_exp_f32 v35, v35\n"
"v_exp_f32 v36, v36\n"
"v_exp_f32 v37, v37\n"
"v_exp_f32 v38, v38\n"
"v_exp_f32 v39, v39\n"
"v_exp_f32 v40, v40\n"
"v_exp_f32 v41, v41\n"
"v_exp_f32 v42, v42\n"
"v_exp_f32 v43, v43\n"
"v_exp_f32 v44, v44\n"
"v_exp_f32 v45, v45\n"
"v_exp_f32 v46, v46\n"
"v_exp_f32 v47, v47\n"
"v_sub_f32 v16, -0.5, v16\n"
"v_sub_f32 v17, -0.5, v17\n"
"v_sub_f32 v18, -0.5, v18\n"
"v_sub_f32 v19, -0.5, v19\n"
"v_sub_f32 v20, -0.5, v20\n"
"v_sub_f32 v21, -0.5, v21\n"
"v_sub_f32 v22, -0.5, v22\n"
"v_sub_f32 v23, -0.5, v23\n"
"v_sub_f32 v24, -0.5, v24\n"
"v_sub_f32 v25, -0.5, v25\n"
"v_sub_f32 v26, -0.5, v26\n"
"v_sub_f32 v27, -0.5, v27\n"
"v_sub_f32 v28, -0.5, v28\n"
"v_sub_f32 v29, -0.5, v29\n"
"v_sub_f32 v30, -0.5, v30\n"
"v_sub_f32 v31, -0.5, v31\n"
"v_sub_f32 v32, -0.5, v32\n"
"v_sub_f32 v33, -0.5, v33\n"
"v_sub_f32 v34, -0.5, v34\n"
"v_sub_f32 v35, -0.5, v35\n"
"v_sub_f32 v36, -0.5, v36\n"
"v_sub_f32 v37, -0.5, v37\n"
"v_sub_f32 v38, -0.5, v38\n"
"v_sub_f32 v39, -0.5, v39\n"
"v_sub_f32 v40, -0.5, v40\n"
"v_sub_f32 v41, -0.5, v41\n"
"v_sub_f32 v42, -0.5, v42\n"
"v_sub_f32 v43, -0.5, v43\n"
"v_sub_f32 v44, -0.5, v44\n"
"v_sub_f32 v45, -0.5, v45\n"
"v_sub_f32 v46, -0.5, v46\n"
"v_sub_f32 v47, -0.5, v47\n"
"v_rcp_f32 v16, v16\n"
"v_rcp_f32 v20, v20\n"
"v_rcp_f32 v24, v24\n"
"v_rcp_f32 v28, v28\n"
"v_rcp_f32 v17, v17\n"
"v_rcp_f32 v21, v21\n"
"v_rcp_f32 v25, v25\n"
"v_rcp_f32 v29, v29\n"
"v_rcp_f32 v18, v18\n"
"v_rcp_f32 v22, v22\n"
"v_rcp_f32 v26, v26\n"
"v_rcp_f32 v30, v30\n"
"v_rcp_f32 v19, v19\n"
"v_rcp_f32 v23, v23\n"
"v_rcp_f32 v27, v27\n"
"v_rcp_f32 v31, v31\n"
"v_rcp_f32 v32, v32\n"
"v_rcp_f32 v36, v36\n"
"v_rcp_f32 v40, v40\n"
"v_rcp_f32 v44, v44\n"
"v_rcp_f32 v33, v33\n"
"v_rcp_f32 v37, v37\n"
"v_rcp_f32 v41, v41\n"
"v_rcp_f32 v45, v45\n"
"v_rcp_f32 v34, v34\n"
"v_rcp_f32 v38, v38\n"
"v_rcp_f32 v42, v42\n"
"v_rcp_f32 v46, v46\n"
"v_rcp_f32 v35, v35\n"
"v_rcp_f32 v39, v39\n"
"v_rcp_f32 v43, v43\n"
"v_rcp_f32 v47, v47\n"
"v_pk_add_f32 v[16:17], v[16:17], 1.0 op_sel_hi:[1,0]\n"
"v_pk_add_f32 v[18:19], v[18:19], 1.0 op_sel_hi:[1,0]\n"
"v_pk_add_f32 v[20:21], v[20:21], 1.0 op_sel_hi:[1,0]\n"
"v_pk_add_f32 v[22:23], v[22:23], 1.0 op_sel_hi:[1,0]\n"
"v_pk_add_f32 v[24:25], v[24:25], 1.0 op_sel_hi:[1,0]\n"
"v_pk_add_f32 v[26:27], v[26:27], 1.0 op_sel_hi:[1,0]\n"
"v_pk_add_f32 v[28:29], v[28:29], 1.0 op_sel_hi:[1,0]\n"
"v_pk_add_f32 v[30:31], v[30:31], 1.0 op_sel_hi:[1,0]\n"
"v_pk_add_f32 v[32:33], v[32:33], 1.0 op_sel_hi:[1,0]\n"
"v_pk_add_f32 v[34:35], v[34:35], 1.0 op_sel_hi:[1,0]\n"
"v_pk_add_f32 v[36:37], v[36:37], 1.0 op_sel_hi:[1,0]\n"
"v_pk_add_f32 v[38:39], v[38:39], 1.0 op_sel_hi:[1,0]\n"
"v_pk_add_f32 v[40:41], v[40:41], 1.0 op_sel_hi:[1,0]\n"
"v_pk_add_f32 v[42:43], v[42:43], 1.0 op_sel_hi:[1,0]\n"
"v_pk_add_f32 v[44:45], v[44:45], 1.0 op_sel_hi:[1,0]\n"
"v_pk_add_f32 v[46:47], v[46:47], 1.0 op_sel_hi:[1,0]\n"
"v_cvt_pk_bf16_f32 v176, v16, v20\n"
"v_cvt_pk_bf16_f32 v177, v24, v28\n"
"ds_write_b64 v8, v[176:177] offset:16384\n"
"v_cvt_pk_bf16_f32 v178, v17, v21\n"
"v_cvt_pk_bf16_f32 v179, v25, v29\n"
"ds_write_b64 v9, v[178:179] offset:18432\n"
"v_cvt_pk_bf16_f32 v180, v18, v22\n"
"v_cvt_pk_bf16_f32 v181, v26, v30\n"
"ds_write_b64 v10, v[180:181] offset:20480\n"
"v_cvt_pk_bf16_f32 v182, v19, v23\n"
"v_cvt_pk_bf16_f32 v183, v27, v31\n"
"ds_write_b64 v11, v[182:183] offset:22528\n"
"v_cvt_pk_bf16_f32 v184, v32, v36\n"
"v_cvt_pk_bf16_f32 v185, v40, v44\n"
"ds_write_b64 v8, v[184:185] offset:24576\n"
"v_cvt_pk_bf16_f32 v186, v33, v37\n"
"v_cvt_pk_bf16_f32 v187, v41, v45\n"
"ds_write_b64 v9, v[186:187] offset:26624\n"
"v_cvt_pk_bf16_f32 v188, v34, v38\n"
"v_cvt_pk_bf16_f32 v189, v42, v46\n"
"ds_write_b64 v10, v[188:189] offset:28672\n"
"v_cvt_pk_bf16_f32 v190, v35, v39\n"
"v_cvt_pk_bf16_f32 v191, v43, v47\n"
"ds_write_b64 v11, v[190:191] offset:30720\n"
"v_pk_add_f32 v[16:17], v[144:145], v[210:211] op_sel:[0,0] op_sel_hi:[1,0]\n"
"v_pk_add_f32 v[18:19], v[146:147], v[210:211] op_sel:[0,0] op_sel_hi:[1,0]\n"
"v_pk_add_f32 v[20:21], v[152:153], v[214:215] op_sel:[0,0] op_sel_hi:[1,0]\n"
"v_pk_add_f32 v[22:23], v[154:155], v[214:215] op_sel:[0,0] op_sel_hi:[1,0]\n"
"v_pk_add_f32 v[24:25], v[160:161], v[218:219] op_sel:[0,0] op_sel_hi:[1,0]\n"
"v_pk_add_f32 v[26:27], v[162:163], v[218:219] op_sel:[0,0] op_sel_hi:[1,0]\n"
"v_pk_add_f32 v[28:29], v[168:169], v[222:223] op_sel:[0,0] op_sel_hi:[1,0]\n"
"v_pk_add_f32 v[30:31], v[170:171], v[222:223] op_sel:[0,0] op_sel_hi:[1,0]\n"
"v_pk_add_f32 v[32:33], v[148:149], v[226:227] op_sel:[0,0] op_sel_hi:[1,0]\n"
"v_pk_add_f32 v[34:35], v[150:151], v[226:227] op_sel:[0,0] op_sel_hi:[1,0]\n"
"v_pk_add_f32 v[36:37], v[156:157], v[230:231] op_sel:[0,0] op_sel_hi:[1,0]\n"
"v_pk_add_f32 v[38:39], v[158:159], v[230:231] op_sel:[0,0] op_sel_hi:[1,0]\n"
"v_pk_add_f32 v[40:41], v[164:165], v[234:235] op_sel:[0,0] op_sel_hi:[1,0]\n"
"v_pk_add_f32 v[42:43], v[166:167], v[234:235] op_sel:[0,0] op_sel_hi:[1,0]\n"
"v_pk_add_f32 v[44:45], v[172:173], v[238:239] op_sel:[0,0] op_sel_hi:[1,0]\n"
"v_pk_add_f32 v[46:47], v[174:175], v[238:239] op_sel:[0,0] op_sel_hi:[1,0]\n"
"v_exp_f32 v16, v16\n"
"v_exp_f32 v17, v17\n"
"v_exp_f32 v18, v18\n"
"v_exp_f32 v19, v19\n"
"v_exp_f32 v20, v20\n"
"v_exp_f32 v21, v21\n"
"v_exp_f32 v22, v22\n"
"v_exp_f32 v23, v23\n"
"v_exp_f32 v24, v24\n"
"v_exp_f32 v25, v25\n"
"v_exp_f32 v26, v26\n"
"v_exp_f32 v27, v27\n"
"v_exp_f32 v28, v28\n"
"v_exp_f32 v29, v29\n"
"v_exp_f32 v30, v30\n"
"v_exp_f32 v31, v31\n"
"v_exp_f32 v32, v32\n"
"v_exp_f32 v33, v33\n"
"v_exp_f32 v34, v34\n"
"v_exp_f32 v35, v35\n"
"v_exp_f32 v36, v36\n"
"v_exp_f32 v37, v37\n"
"v_exp_f32 v38, v38\n"
"v_exp_f32 v39, v39\n"
"v_exp_f32 v40, v40\n"
"v_exp_f32 v41, v41\n"
"v_exp_f32 v42, v42\n"
"v_exp_f32 v43, v43\n"
"v_exp_f32 v44, v44\n"
"v_exp_f32 v45, v45\n"
"v_exp_f32 v46, v46\n"
"v_exp_f32 v47, v47\n"
"v_sub_f32 v16, -0.5, v16\n"
"v_sub_f32 v17, -0.5, v17\n"
"v_sub_f32 v18, -0.5, v18\n"
"v_sub_f32 v19, -0.5, v19\n"
"v_sub_f32 v20, -0.5, v20\n"
"v_sub_f32 v21, -0.5, v21\n"
"v_sub_f32 v22, -0.5, v22\n"
"v_sub_f32 v23, -0.5, v23\n"
"v_sub_f32 v24, -0.5, v24\n"
"v_sub_f32 v25, -0.5, v25\n"
"v_sub_f32 v26, -0.5, v26\n"
"v_sub_f32 v27, -0.5, v27\n"
"v_sub_f32 v28, -0.5, v28\n"
"v_sub_f32 v29, -0.5, v29\n"
"v_sub_f32 v30, -0.5, v30\n"
"v_sub_f32 v31, -0.5, v31\n"
"v_sub_f32 v32, -0.5, v32\n"
"v_sub_f32 v33, -0.5, v33\n"
"v_sub_f32 v34, -0.5, v34\n"
"v_sub_f32 v35, -0.5, v35\n"
"v_sub_f32 v36, -0.5, v36\n"
"v_sub_f32 v37, -0.5, v37\n"
"v_sub_f32 v38, -0.5, v38\n"
"v_sub_f32 v39, -0.5, v39\n"
"v_sub_f32 v40, -0.5, v40\n"
"v_sub_f32 v41, -0.5, v41\n"
"v_sub_f32 v42, -0.5, v42\n"
"v_sub_f32 v43, -0.5, v43\n"
"v_sub_f32 v44, -0.5, v44\n"
"v_sub_f32 v45, -0.5, v45\n"
"v_sub_f32 v46, -0.5, v46\n"
"v_sub_f32 v47, -0.5, v47\n"
"v_rcp_f32 v16, v16\n"
"v_rcp_f32 v20, v20\n"
"v_rcp_f32 v24, v24\n"
"v_rcp_f32 v28, v28\n"
"v_rcp_f32 v17, v17\n"
"v_rcp_f32 v21, v21\n"
"v_rcp_f32 v25, v25\n"
"v_rcp_f32 v29, v29\n"
"v_rcp_f32 v18, v18\n"
"v_rcp_f32 v22, v22\n"
"v_rcp_f32 v26, v26\n"
"v_rcp_f32 v30, v30\n"
"v_rcp_f32 v19, v19\n"
"v_rcp_f32 v23, v23\n"
"v_rcp_f32 v27, v27\n"
"v_rcp_f32 v31, v31\n"
"v_rcp_f32 v32, v32\n"
"v_rcp_f32 v36, v36\n"
"v_rcp_f32 v40, v40\n"
"v_rcp_f32 v44, v44\n"
"v_rcp_f32 v33, v33\n"
"v_rcp_f32 v37, v37\n"
"v_rcp_f32 v41, v41\n"
"v_rcp_f32 v45, v45\n"
"v_rcp_f32 v34, v34\n"
"v_rcp_f32 v38, v38\n"
"v_rcp_f32 v42, v42\n"
"v_rcp_f32 v46, v46\n"
"v_rcp_f32 v35, v35\n"
"v_rcp_f32 v39, v39\n"
"v_rcp_f32 v43, v43\n"
"v_rcp_f32 v47, v47\n"
"v_pk_add_f32 v[16:17], v[16:17], 1.0 op_sel_hi:[1,0]\n"
"v_pk_add_f32 v[18:19], v[18:19], 1.0 op_sel_hi:[1,0]\n"
"v_pk_add_f32 v[20:21], v[20:21], 1.0 op_sel_hi:[1,0]\n"
"v_pk_add_f32 v[22:23], v[22:23], 1.0 op_sel_hi:[1,0]\n"
"v_pk_add_f32 v[24:25], v[24:25], 1.0 op_sel_hi:[1,0]\n"
"v_pk_add_f32 v[26:27], v[26:27], 1.0 op_sel_hi:[1,0]\n"
"v_pk_add_f32 v[28:29], v[28:29], 1.0 op_sel_hi:[1,0]\n"
"v_pk_add_f32 v[30:31], v[30:31], 1.0 op_sel_hi:[1,0]\n"
"v_pk_add_f32 v[32:33], v[32:33], 1.0 op_sel_hi:[1,0]\n"
"v_pk_add_f32 v[34:35], v[34:35], 1.0 op_sel_hi:[1,0]\n"
"v_pk_add_f32 v[36:37], v[36:37], 1.0 op_sel_hi:[1,0]\n"
"v_pk_add_f32 v[38:39], v[38:39], 1.0 op_sel_hi:[1,0]\n"
"v_pk_add_f32 v[40:41], v[40:41], 1.0 op_sel_hi:[1,0]\n"
"v_pk_add_f32 v[42:43], v[42:43], 1.0 op_sel_hi:[1,0]\n"
"v_pk_add_f32 v[44:45], v[44:45], 1.0 op_sel_hi:[1,0]\n"
"v_pk_add_f32 v[46:47], v[46:47], 1.0 op_sel_hi:[1,0]\n"
"v_cvt_pk_bf16_f32 v176, v16, v20\n"
"v_cvt_pk_bf16_f32 v177, v24, v28\n"
"v_cvt_pk_bf16_f32 v178, v17, v21\n"
"v_cvt_pk_bf16_f32 v179, v25, v29\n"
"v_cvt_pk_bf16_f32 v180, v18, v22\n"
"v_cvt_pk_bf16_f32 v181, v26, v30\n"
"v_cvt_pk_bf16_f32 v182, v19, v23\n"
"v_cvt_pk_bf16_f32 v183, v27, v31\n"
"v_cvt_pk_bf16_f32 v184, v32, v36\n"
"v_cvt_pk_bf16_f32 v185, v40, v44\n"
"v_cvt_pk_bf16_f32 v186, v33, v37\n"
"v_cvt_pk_bf16_f32 v187, v41, v45\n"
"v_cvt_pk_bf16_f32 v188, v34, v38\n"
"v_cvt_pk_bf16_f32 v189, v42, v46\n"
"v_cvt_pk_bf16_f32 v190, v35, v39\n"
"v_cvt_pk_bf16_f32 v191, v43, v47\n"
"s_waitcnt lgkmcnt(0)\n"
"s_barrier\n"
"ds_read_b128 v[48:51], v12\n"
"ds_read_b128 v[52:55], v13\n"
"ds_read_b128 a[0:3], v240\n"
"ds_read_b128 a[4:7], v240 offset:1024\n"
"ds_read_b128 a[8:11], v240 offset:2048\n"
"ds_read_b128 a[12:15], v240 offset:3072\n"
"ds_read_b128 a[16:19], v240 offset:4096\n"
"ds_read_b128 a[20:23], v240 offset:5120\n"
"ds_read_b128 a[24:27], v240 offset:6144\n"
"ds_read_b128 a[28:31], v240 offset:7168\n"
"ds_read_b128 v[56:59], v14\n"
"ds_read_b128 v[60:63], v15\n"
"ds_read_b128 a[32:35], v240 offset:16384\n"
"s_waitcnt lgkmcnt(7)\n"
"s_nop 0\n"
"v_mfma_f32_16x16x32_bf16 v[80:83], v[48:51], a[0:3], 0\n"
"ds_read_b128 a[36:39], v240 offset:17408\n"
"v_mfma_f32_16x16x32_bf16 v[84:87], v[48:51], a[4:7], 0\n"
"ds_read_b128 a[40:43], v240 offset:18432\n"
"v_mfma_f32_16x16x32_bf16 v[88:91], v[48:51], a[8:11], 0\n"
"ds_read_b128 a[44:47], v240 offset:19456\n"
"v_mfma_f32_16x16x32_bf16 v[92:95], v[48:51], a[12:15], 0\n"
"ds_read_b128 a[48:51], v240 offset:20480\n"
"ds_read_b128 v[64:67], v12 offset:256\n"
"s_waitcnt lgkmcnt(8)\n"
"s_nop 0\n"
"v_mfma_f32_16x16x32_bf16 v[96:99], v[48:51], a[16:19], 0\n"
"ds_read_b128 a[52:55], v240 offset:21504\n"
"v_mfma_f32_16x16x32_bf16 v[100:103], v[48:51], a[20:23], 0\n"
"ds_read_b128 a[56:59], v240 offset:22528\n"
"v_mfma_f32_16x16x32_bf16 v[104:107], v[48:51], a[24:27], 0\n"
"ds_read_b128 a[60:63], v240 offset:23552\n"
"v_mfma_f32_16x16x32_bf16 v[108:111], v[48:51], a[28:31], 0\n"
"ds_read_b128 a[64:67], v240 offset:32768\n"
"ds_read_b128 v[68:71], v13 offset:256\n"
"s_waitcnt lgkmcnt(7)\n"
"s_nop 0\n"
"v_mfma_f32_16x16x32_bf16 v[80:83], v[52:55], a[32:35], v[80:83]\n"
"ds_read_b128 a[68:71], v240 offset:33792\n"
"v_mfma_f32_16x16x32_bf16 v[84:87], v[52:55], a[36:39], v[84:87]\n"
"ds_read_b128 a[72:75], v240 offset:34816\n"
"v_mfma_f32_16x16x32_bf16 v[88:91], v[52:55], a[40:43], v[88:91]\n"
"ds_read_b128 a[76:79], v240 offset:35840\n"
"v_mfma_f32_16x16x32_bf16 v[92:95], v[52:55], a[44:47], v[92:95]\n"
"ds_read_b128 a[80:83], v240 offset:36864\n"
"ds_read_b128 v[72:75], v14 offset:256\n"
"s_waitcnt lgkmcnt(7)\n"
"s_nop 0\n"
"v_mfma_f32_16x16x32_bf16 v[96:99], v[52:55], a[48:51], v[96:99]\n"
"ds_read_b128 a[84:87], v240 offset:37888\n"
"v_mfma_f32_16x16x32_bf16 v[100:103], v[52:55], a[52:55], v[100:103]\n"
"ds_read_b128 a[88:91], v240 offset:38912\n"
"v_mfma_f32_16x16x32_bf16 v[104:107], v[52:55], a[56:59], v[104:107]\n"
"ds_read_b128 a[92:95], v240 offset:39936\n"
"v_mfma_f32_16x16x32_bf16 v[108:111], v[52:55], a[60:63], v[108:111]\n"
"ds_read_b128 a[96:99], v240 offset:49152\n"
"ds_read_b128 v[76:79], v15 offset:256\n"
"s_waitcnt lgkmcnt(7)\n"
"v_mfma_f32_16x16x32_bf16 v[80:83], v[56:59], a[64:67], v[80:83]\n"
"s_barrier\n"
"ds_read_b128 a[100:103], v240 offset:50176\n"
"v_mfma_f32_16x16x32_bf16 v[84:87], v[56:59], a[68:71], v[84:87]\n"
"ds_read_b128 a[104:107], v240 offset:51200\n"
"v_mfma_f32_16x16x32_bf16 v[88:91], v[56:59], a[72:75], v[88:91]\n"
"ds_read_b128 a[108:111], v240 offset:52224\n"
"v_mfma_f32_16x16x32_bf16 v[92:95], v[56:59], a[76:79], v[92:95]\n"
"ds_read_b128 a[112:115], v240 offset:53248\n"
"s_waitcnt lgkmcnt(6)\n"
"s_nop 0\n"
"v_mfma_f32_16x16x32_bf16 v[96:99], v[56:59], a[80:83], v[96:99]\n"
"ds_write_b64 v244, v[176:177]\n"
"ds_read_b128 a[116:119], v240 offset:54272\n"
"v_mfma_f32_16x16x32_bf16 v[100:103], v[56:59], a[84:87], v[100:103]\n"
"ds_read_b128 a[120:123], v240 offset:55296\n"
"v_mfma_f32_16x16x32_bf16 v[104:107], v[56:59], a[88:91], v[104:107]\n"
"ds_read_b128 a[124:127], v240 offset:56320\n"
"v_mfma_f32_16x16x32_bf16 v[108:111], v[56:59], a[92:95], v[108:111]\n"
"ds_read_b128 a[128:131], v241\n"
"s_waitcnt lgkmcnt(6)\n"
"s_nop 0\n"
"v_mfma_f32_16x16x32_bf16 v[80:83], v[60:63], a[96:99], v[80:83]\n"
"ds_write_b64 v245, v[178:179] offset:2048\n"
"ds_read_b128 a[132:135], v241 offset:1024\n"
"v_mfma_f32_16x16x32_bf16 v[84:87], v[60:63], a[100:103], v[84:87]\n"
"ds_read_b128 a[136:139], v241 offset:2048\n"
"v_mfma_f32_16x16x32_bf16 v[88:91], v[60:63], a[104:107], v[88:91]\n"
"ds_read_b128 a[140:143], v241 offset:3072\n"
"v_mfma_f32_16x16x32_bf16 v[92:95], v[60:63], a[108:111], v[92:95]\n"
"ds_read_b128 a[144:147], v241 offset:4096\n"
"s_waitcnt lgkmcnt(6)\n"
"s_nop 0\n"
"v_mfma_f32_16x16x32_bf16 v[96:99], v[60:63], a[112:115], v[96:99]\n"
"ds_write_b64 v246, v[180:181] offset:4096\n"
"ds_read_b128 a[148:151], v241 offset:5120\n"
"v_mfma_f32_16x16x32_bf16 v[100:103], v[60:63], a[116:119], v[100:103]\n"
"ds_read_b128 a[152:155], v241 offset:6144\n"
"v_mfma_f32_16x16x32_bf16 v[104:107], v[60:63], a[120:123], v[104:107]\n"
"ds_read_b128 a[156:159], v241 offset:7168\n"
"v_mfma_f32_16x16x32_bf16 v[108:111], v[60:63], a[124:127], v[108:111]\n"
"ds_read_b128 a[160:163], v241 offset:16384\n"
"s_waitcnt lgkmcnt(6)\n"
"s_nop 0\n"
"v_mfma_f32_16x16x32_bf16 v[80:83], v[64:67], a[128:131], v[80:83]\n"
"ds_write_b64 v247, v[182:183] offset:6144\n"
"ds_read_b128 a[164:167], v241 offset:17408\n"
"v_mfma_f32_16x16x32_bf16 v[84:87], v[64:67], a[132:135], v[84:87]\n"
"ds_read_b128 a[168:171], v241 offset:18432\n"
"v_mfma_f32_16x16x32_bf16 v[88:91], v[64:67], a[136:139], v[88:91]\n"
"ds_read_b128 a[172:175], v241 offset:19456\n"
"v_mfma_f32_16x16x32_bf16 v[92:95], v[64:67], a[140:143], v[92:95]\n"
"ds_read_b128 a[176:179], v241 offset:20480\n"
"s_waitcnt lgkmcnt(6)\n"
"s_nop 0\n"
"v_mfma_f32_16x16x32_bf16 v[96:99], v[64:67], a[144:147], v[96:99]\n"
"ds_write_b64 v244, v[184:185] offset:8192\n"
"ds_read_b128 a[180:183], v241 offset:21504\n"
"v_mfma_f32_16x16x32_bf16 v[100:103], v[64:67], a[148:151], v[100:103]\n"
"ds_read_b128 a[184:187], v241 offset:22528\n"
"v_mfma_f32_16x16x32_bf16 v[104:107], v[64:67], a[152:155], v[104:107]\n"
"ds_read_b128 a[188:191], v241 offset:23552\n"
"v_mfma_f32_16x16x32_bf16 v[108:111], v[64:67], a[156:159], v[108:111]\n"
"ds_read_b128 a[192:195], v241 offset:32768\n"
"s_waitcnt lgkmcnt(6)\n"
"s_nop 0\n"
"v_mfma_f32_16x16x32_bf16 v[80:83], v[68:71], a[160:163], v[80:83]\n"
"ds_write_b64 v245, v[186:187] offset:10240\n"
"ds_read_b128 a[196:199], v241 offset:33792\n"
"v_mfma_f32_16x16x32_bf16 v[84:87], v[68:71], a[164:167], v[84:87]\n"
"ds_read_b128 a[200:203], v241 offset:34816\n"
"v_mfma_f32_16x16x32_bf16 v[88:91], v[68:71], a[168:171], v[88:91]\n"
"ds_read_b128 a[204:207], v241 offset:35840\n"
"v_mfma_f32_16x16x32_bf16 v[92:95], v[68:71], a[172:175], v[92:95]\n"
"ds_read_b128 a[208:211], v241 offset:36864\n"
"s_waitcnt lgkmcnt(6)\n"
"s_nop 0\n"
"v_mfma_f32_16x16x32_bf16 v[96:99], v[68:71], a[176:179], v[96:99]\n"
"ds_write_b64 v246, v[188:189] offset:12288\n"
"ds_read_b128 a[212:215], v241 offset:37888\n"
"v_mfma_f32_16x16x32_bf16 v[100:103], v[68:71], a[180:183], v[100:103]\n"
"ds_read_b128 a[216:219], v241 offset:38912\n"
"v_mfma_f32_16x16x32_bf16 v[104:107], v[68:71], a[184:187], v[104:107]\n"
"ds_read_b128 a[220:223], v241 offset:39936\n"
"v_mfma_f32_16x16x32_bf16 v[108:111], v[68:71], a[188:191], v[108:111]\n"
"ds_read_b128 a[224:227], v241 offset:49152\n"
"s_waitcnt lgkmcnt(6)\n"
"s_nop 0\n"
"v_mfma_f32_16x16x32_bf16 v[80:83], v[72:75], a[192:195], v[80:83]\n"
"ds_write_b64 v247, v[190:191] offset:14336\n"
"ds_read_b128 a[228:231], v241 offset:50176\n"
"v_mfma_f32_16x16x32_bf16 v[84:87], v[72:75], a[196:199], v[84:87]\n"
"ds_read_b128 a[232:235], v241 offset:51200\n"
"v_mfma_f32_16x16x32_bf16 v[88:91], v[72:75], a[200:203], v[88:91]\n"
"ds_read_b128 a[236:239], v241 offset:52224\n"
"v_mfma_f32_16x16x32_bf16 v[92:95], v[72:75], a[204:207], v[92:95]\n"
"ds_read_b128 a[240:243], v241 offset:53248\n"
"s_waitcnt lgkmcnt(6)\n"
"s_nop 0\n"
"v_mfma_f32_16x16x32_bf16 v[96:99], v[72:75], a[208:211], v[96:99]\n"
"ds_read_b128 a[244:247], v241 offset:54272\n"
"v_mfma_f32_16x16x32_bf16 v[100:103], v[72:75], a[212:215], v[100:103]\n"
"ds_read_b128 a[248:251], v241 offset:55296\n"
"v_mfma_f32_16x16x32_bf16 v[104:107], v[72:75], a[216:219], v[104:107]\n"
"ds_read_b128 a[252:255], v241 offset:56320\n"
"v_mfma_f32_16x16x32_bf16 v[108:111], v[72:75], a[220:223], v[108:111]\n"
"s_waitcnt lgkmcnt(4)\n"
"s_nop 0\n"
"v_mfma_f32_16x16x32_bf16 v[80:83], v[76:79], a[224:227], v[80:83]\n"
"ds_read_b128 v[48:51], v12 offset:16640\n"
"v_mfma_f32_16x16x32_bf16 v[84:87], v[76:79], a[228:231], v[84:87]\n"
"ds_read_b128 v[52:55], v13 offset:16640\n"
"v_mfma_f32_16x16x32_bf16 v[88:91], v[76:79], a[232:235], v[88:91]\n"
"ds_read_b128 v[56:59], v14 offset:16640\n"
"v_mfma_f32_16x16x32_bf16 v[92:95], v[76:79], a[236:239], v[92:95]\n"
"ds_read_b128 v[60:63], v15 offset:16640\n"
"s_waitcnt lgkmcnt(4)\n"
"s_nop 0\n"
"v_mfma_f32_16x16x32_bf16 v[96:99], v[76:79], a[240:243], v[96:99]\n"
"ds_read_b128 v[0:3], v12 offset:16384\n"
"v_mfma_f32_16x16x32_bf16 v[100:103], v[76:79], a[244:247], v[100:103]\n"
"ds_read_b128 v[4:7], v13 offset:16384\n"
"v_mfma_f32_16x16x32_bf16 v[104:107], v[76:79], a[248:251], v[104:107]\n"
"ds_read_b128 v[8:11], v14 offset:16384\n"
"v_mfma_f32_16x16x32_bf16 v[108:111], v[76:79], a[252:255], v[108:111]\n"
"ds_read_b128 v[12:15], v15 offset:16384\n"
"v_pk_add_f32 v[16:17], v[144:145], v[210:211] op_sel:[0,1] op_sel_hi:[1,1]\n"
"v_pk_add_f32 v[18:19], v[146:147], v[210:211] op_sel:[0,1] op_sel_hi:[1,1]\n"
"v_pk_add_f32 v[20:21], v[152:153], v[214:215] op_sel:[0,1] op_sel_hi:[1,1]\n"
"v_pk_add_f32 v[22:23], v[154:155], v[214:215] op_sel:[0,1] op_sel_hi:[1,1]\n"
"v_pk_add_f32 v[24:25], v[160:161], v[218:219] op_sel:[0,1] op_sel_hi:[1,1]\n"
"v_pk_add_f32 v[26:27], v[162:163], v[218:219] op_sel:[0,1] op_sel_hi:[1,1]\n"
"v_pk_add_f32 v[28:29], v[168:169], v[222:223] op_sel:[0,1] op_sel_hi:[1,1]\n"
"v_pk_add_f32 v[30:31], v[170:171], v[222:223] op_sel:[0,1] op_sel_hi:[1,1]\n"
"v_pk_add_f32 v[32:33], v[148:149], v[226:227] op_sel:[0,1] op_sel_hi:[1,1]\n"
"v_pk_add_f32 v[34:35], v[150:151], v[226:227] op_sel:[0,1] op_sel_hi:[1,1]\n"
"v_pk_add_f32 v[36:37], v[156:157], v[230:231] op_sel:[0,1] op_sel_hi:[1,1]\n"
"v_pk_add_f32 v[38:39], v[158:159], v[230:231] op_sel:[0,1] op_sel_hi:[1,1]\n"
"v_pk_add_f32 v[40:41], v[164:165], v[234:235] op_sel:[0,1] op_sel_hi:[1,1]\n"
"v_pk_add_f32 v[42:43], v[166:167], v[234:235] op_sel:[0,1] op_sel_hi:[1,1]\n"
"v_pk_add_f32 v[44:45], v[172:173], v[238:239] op_sel:[0,1] op_sel_hi:[1,1]\n"
"v_pk_add_f32 v[46:47], v[174:175], v[238:239] op_sel:[0,1] op_sel_hi:[1,1]\n"
"v_exp_f32 v16, v16\n"
"v_exp_f32 v17, v17\n"
"v_exp_f32 v18, v18\n"
"v_exp_f32 v19, v19\n"
"v_exp_f32 v20, v20\n"
"v_exp_f32 v21, v21\n"
"v_exp_f32 v22, v22\n"
"v_exp_f32 v23, v23\n"
"v_exp_f32 v24, v24\n"
"v_exp_f32 v25, v25\n"
"v_exp_f32 v26, v26\n"
"v_exp_f32 v27, v27\n"
"v_exp_f32 v28, v28\n"
"v_exp_f32 v29, v29\n"
"v_exp_f32 v30, v30\n"
"v_exp_f32 v31, v31\n"
"v_exp_f32 v32, v32\n"
"v_exp_f32 v33, v33\n"
"v_exp_f32 v34, v34\n"
"v_exp_f32 v35, v35\n"
"v_exp_f32 v36, v36\n"
"v_exp_f32 v37, v37\n"
"v_exp_f32 v38, v38\n"
"v_exp_f32 v39, v39\n"
"v_exp_f32 v40, v40\n"
"v_exp_f32 v41, v41\n"
"v_exp_f32 v42, v42\n"
"v_exp_f32 v43, v43\n"
"v_exp_f32 v44, v44\n"
"v_exp_f32 v45, v45\n"
"v_exp_f32 v46, v46\n"
"v_exp_f32 v47, v47\n"
"v_sub_f32 v16, -0.5, v16\n"
"v_sub_f32 v17, -0.5, v17\n"
"v_sub_f32 v18, -0.5, v18\n"
"v_sub_f32 v19, -0.5, v19\n"
"v_sub_f32 v20, -0.5, v20\n"
"v_sub_f32 v21, -0.5, v21\n"
"v_sub_f32 v22, -0.5, v22\n"
"v_sub_f32 v23, -0.5, v23\n"
"v_sub_f32 v24, -0.5, v24\n"
"v_sub_f32 v25, -0.5, v25\n"
"v_sub_f32 v26, -0.5, v26\n"
"v_sub_f32 v27, -0.5, v27\n"
"v_sub_f32 v28, -0.5, v28\n"
"v_sub_f32 v29, -0.5, v29\n"
"v_sub_f32 v30, -0.5, v30\n"
"v_sub_f32 v31, -0.5, v31\n"
"v_sub_f32 v32, -0.5, v32\n"
"v_sub_f32 v33, -0.5, v33\n"
"v_sub_f32 v34, -0.5, v34\n"
"v_sub_f32 v35, -0.5, v35\n"
"v_sub_f32 v36, -0.5, v36\n"
"v_sub_f32 v37, -0.5, v37\n"
"v_sub_f32 v38, -0.5, v38\n"
"v_sub_f32 v39, -0.5, v39\n"
"v_sub_f32 v40, -0.5, v40\n"
"v_sub_f32 v41, -0.5, v41\n"
"v_sub_f32 v42, -0.5, v42\n"
"v_sub_f32 v43, -0.5, v43\n"
"v_sub_f32 v44, -0.5, v44\n"
"v_sub_f32 v45, -0.5, v45\n"
"v_sub_f32 v46, -0.5, v46\n"
"v_sub_f32_e64 v47, -0.5, v47\n"
"s_waitcnt lgkmcnt(0)\n"
"v_mfma_f32_16x16x32_bf16 v[112:115], v[0:3], a[0:3], 0\n"
"v_rcp_f32_e64 v16, v16\n"
"v_mfma_f32_16x16x32_bf16 v[116:119], v[0:3], a[4:7], 0\n"
"v_rcp_f32_e64 v20, v20\n"
"v_mfma_f32_16x16x32_bf16 v[120:123], v[0:3], a[8:11], 0\n"
"v_cvt_pk_bf16_f32 v192, v80, v84\n"
"v_cvt_pk_bf16_f32 v193, v88, v92\n"
"v_cvt_pk_bf16_f32 v194, v96, v100\n"
"v_cvt_pk_bf16_f32 v195, v104, v108\n"
"v_mfma_f32_16x16x32_bf16 v[124:127], v[0:3], a[12:15], 0\n"
"v_rcp_f32_e64 v24, v24\n"
"v_mfma_f32_16x16x32_bf16 v[128:131], v[0:3], a[16:19], 0\n"
"v_rcp_f32_e64 v28, v28\n"
"v_mfma_f32_16x16x32_bf16 v[132:135], v[0:3], a[20:23], 0\n"
"s_xor_b32 s92, s9, 0\n"
"s_nop 0\n"
"buffer_store_dwordx4 v[192:195], v242, s[48:51], s92 offen sc1\n"
"v_cvt_pk_bf16_f32 v196, v81, v85\n"
"v_cvt_pk_bf16_f32 v197, v89, v93\n"
"v_mfma_f32_16x16x32_bf16 v[136:139], v[0:3], a[24:27], 0\n"
"v_rcp_f32_e64 v17, v17\n"
"v_mfma_f32_16x16x32_bf16 v[140:143], v[0:3], a[28:31], 0\n"
"v_rcp_f32_e64 v21, v21\n"
"v_mfma_f32_16x16x32_bf16 v[112:115], v[4:7], a[32:35], v[112:115]\n"
"v_cvt_pk_bf16_f32 v198, v97, v101\n"
"v_cvt_pk_bf16_f32 v199, v105, v109\n"
"s_xor_b32 s93, s9, 0x1000\n"
"buffer_store_dwordx4 v[196:199], v242, s[48:51], s93 offen sc1\n"
"v_mfma_f32_16x16x32_bf16 v[116:119], v[4:7], a[36:39], v[116:119]\n"
"v_rcp_f32_e64 v25, v25\n"
"v_mfma_f32_16x16x32_bf16 v[120:123], v[4:7], a[40:43], v[120:123]\n"
"v_rcp_f32_e64 v29, v29\n"
"v_mfma_f32_16x16x32_bf16 v[124:127], v[4:7], a[44:47], v[124:127]\n"
"v_cvt_pk_bf16_f32 v200, v82, v86\n"
"v_add_f32 v16, 1.0, v16\n"
"v_add_f32 v20, 1.0, v20\n"
"v_add_f32_e64 v24, 1.0, v24\n"
"v_mfma_f32_16x16x32_bf16 v[128:131], v[4:7], a[48:51], v[128:131]\n"
"v_rcp_f32_e64 v18, v18\n"
"v_mfma_f32_16x16x32_bf16 v[132:135], v[4:7], a[52:55], v[132:135]\n"
"v_rcp_f32_e64 v22, v22\n"
"v_mfma_f32_16x16x32_bf16 v[136:139], v[4:7], a[56:59], v[136:139]\n"
"v_add_f32_e64 v28, 1.0, v28\n"
"v_cvt_pk_bf16_f32 v176, v16, v20\n"
"v_cvt_pk_bf16_f32 v177, v24, v28\n"
"ds_write_b64 v244, v[176:177] offset:16384\n"
"v_mfma_f32_16x16x32_bf16 v[140:143], v[4:7], a[60:63], v[140:143]\n"
"v_rcp_f32_e64 v26, v26\n"
"v_mfma_f32_16x16x32_bf16 v[112:115], v[8:11], a[64:67], v[112:115]\n"
"v_rcp_f32_e64 v30, v30\n"
"v_mfma_f32_16x16x32_bf16 v[116:119], v[8:11], a[68:71], v[116:119]\n"
"v_add_f32 v17, 1.0, v17\n"
"v_add_f32 v21, 1.0, v21\n"
"v_add_f32 v25, 1.0, v25\n"
"v_add_f32 v29, 1.0, v29\n"
"v_mfma_f32_16x16x32_bf16 v[120:123], v[8:11], a[72:75], v[120:123]\n"
"v_rcp_f32_e64 v19, v19\n"
"v_mfma_f32_16x16x32_bf16 v[124:127], v[8:11], a[76:79], v[124:127]\n"
"v_rcp_f32_e64 v23, v23\n"
"v_mfma_f32_16x16x32_bf16 v[128:131], v[8:11], a[80:83], v[128:131]\n"
"v_cvt_pk_bf16_f32 v178, v17, v21\n"
"v_cvt_pk_bf16_f32 v179, v25, v29\n"
"ds_write_b64 v245, v[178:179] offset:18432\n"
"v_add_f32_e64 v18, 1.0, v18\n"
"v_mfma_f32_16x16x32_bf16 v[132:135], v[8:11], a[84:87], v[132:135]\n"
"v_rcp_f32_e64 v27, v27\n"
"v_mfma_f32_16x16x32_bf16 v[136:139], v[8:11], a[88:91], v[136:139]\n"
"v_rcp_f32_e64 v31, v31\n"
"v_mfma_f32_16x16x32_bf16 v[140:143], v[8:11], a[92:95], v[140:143]\n"
"v_add_f32 v22, 1.0, v22\n"
"v_add_f32 v26, 1.0, v26\n"
"v_add_f32_e64 v30, 1.0, v30\n"
"v_cvt_pk_bf16_f32 v180, v18, v22\n"
"v_mfma_f32_16x16x32_bf16 v[112:115], v[12:15], a[96:99], v[112:115]\n"
"v_rcp_f32_e64 v32, v32\n"
"v_mfma_f32_16x16x32_bf16 v[116:119], v[12:15], a[100:103], v[116:119]\n"
"v_rcp_f32_e64 v36, v36\n"
"v_mfma_f32_16x16x32_bf16 v[120:123], v[12:15], a[104:107], v[120:123]\n"
"v_cvt_pk_bf16_f32 v181, v26, v30\n"
"ds_write_b64 v246, v[180:181] offset:20480\n"
"v_add_f32 v19, 1.0, v19\n"
"v_add_f32 v23, 1.0, v23\n"
"v_mfma_f32_16x16x32_bf16 v[124:127], v[12:15], a[108:111], v[124:127]\n"
"v_rcp_f32_e64 v40, v40\n"
"v_mfma_f32_16x16x32_bf16 v[128:131], v[12:15], a[112:115], v[128:131]\n"
"v_rcp_f32_e64 v44, v44\n"
"v_mfma_f32_16x16x32_bf16 v[132:135], v[12:15], a[116:119], v[132:135]\n"
"v_add_f32 v27, 1.0, v27\n"
"v_add_f32 v31, 1.0, v31\n"
"v_cvt_pk_bf16_f32 v182, v19, v23\n"
"v_mfma_f32_16x16x32_bf16 v[136:139], v[12:15], a[120:123], v[136:139]\n"
"v_rcp_f32_e64 v33, v33\n"
"v_mfma_f32_16x16x32_bf16 v[140:143], v[12:15], a[124:127], v[140:143]\n"
"v_rcp_f32_e64 v37, v37\n"
"v_mfma_f32_16x16x32_bf16 v[112:115], v[48:51], a[128:131], v[112:115]\n"
"v_cvt_pk_bf16_f32 v183, v27, v31\n"
"ds_write_b64 v247, v[182:183] offset:22528\n"
"v_add_f32_e64 v32, 1.0, v32\n"
"v_mfma_f32_16x16x32_bf16 v[116:119], v[48:51], a[132:135], v[116:119]\n"
"v_rcp_f32_e64 v41, v41\n"
"v_mfma_f32_16x16x32_bf16 v[120:123], v[48:51], a[136:139], v[120:123]\n"
"v_rcp_f32_e64 v45, v45\n"
"v_mfma_f32_16x16x32_bf16 v[124:127], v[48:51], a[140:143], v[124:127]\n"
"v_add_f32 v36, 1.0, v36\n"
"v_add_f32 v40, 1.0, v40\n"
"v_add_f32_e64 v44, 1.0, v44\n"
"v_mfma_f32_16x16x32_bf16 v[128:131], v[48:51], a[144:147], v[128:131]\n"
"v_rcp_f32_e64 v34, v34\n"
"v_mfma_f32_16x16x32_bf16 v[132:135], v[48:51], a[148:151], v[132:135]\n"
"v_rcp_f32_e64 v38, v38\n"
"v_mfma_f32_16x16x32_bf16 v[136:139], v[48:51], a[152:155], v[136:139]\n"
"v_cvt_pk_bf16_f32 v184, v32, v36\n"
"v_cvt_pk_bf16_f32 v185, v40, v44\n"
"ds_write_b64 v244, v[184:185] offset:24576\n"
"v_mfma_f32_16x16x32_bf16 v[140:143], v[48:51], a[156:159], v[140:143]\n"
"v_rcp_f32_e64 v42, v42\n"
"v_mfma_f32_16x16x32_bf16 v[112:115], v[52:55], a[160:163], v[112:115]\n"
"v_rcp_f32_e64 v46, v46\n"
"v_mfma_f32_16x16x32_bf16 v[116:119], v[52:55], a[164:167], v[116:119]\n"
"v_add_f32 v33, 1.0, v33\n"
"v_add_f32 v37, 1.0, v37\n"
"v_add_f32_e64 v41, 1.0, v41\n"
"v_mfma_f32_16x16x32_bf16 v[120:123], v[52:55], a[168:171], v[120:123]\n"
"v_rcp_f32_e64 v35, v35\n"
"v_mfma_f32_16x16x32_bf16 v[124:127], v[52:55], a[172:175], v[124:127]\n"
"v_rcp_f32_e64 v39, v39\n"
"v_mfma_f32_16x16x32_bf16 v[128:131], v[52:55], a[176:179], v[128:131]\n"
"v_add_f32_e64 v45, 1.0, v45\n"
"v_cvt_pk_bf16_f32 v186, v33, v37\n"
"v_cvt_pk_bf16_f32 v187, v41, v45\n"
"v_mfma_f32_16x16x32_bf16 v[132:135], v[52:55], a[180:183], v[132:135]\n"
"v_rcp_f32_e64 v43, v43\n"
"v_mfma_f32_16x16x32_bf16 v[136:139], v[52:55], a[184:187], v[136:139]\n"
"v_rcp_f32_e64 v47, v47\n"
"v_mfma_f32_16x16x32_bf16 v[140:143], v[52:55], a[188:191], v[140:143]\n"
"ds_write_b64 v245, v[186:187] offset:26624\n"
"v_add_f32 v34, 1.0, v34\n"
"v_add_f32 v38, 1.0, v38\n"
"v_mfma_f32_16x16x32_bf16 v[112:115], v[56:59], a[192:195], v[112:115]\n"
"v_add_f32 v42, 1.0, v42\n"
"v_add_f32 v46, 1.0, v46\n"
"v_cvt_pk_bf16_f32 v188, v34, v38\n"
"v_mfma_f32_16x16x32_bf16 v[116:119], v[56:59], a[196:199], v[116:119]\n"
"v_cvt_pk_bf16_f32 v189, v42, v46\n"
"ds_write_b64 v246, v[188:189] offset:28672\n"
"v_add_f32_e64 v35, 1.0, v35\n"
"v_mfma_f32_16x16x32_bf16 v[120:123], v[56:59], a[200:203], v[120:123]\n"
"v_add_f32 v39, 1.0, v39\n"
"v_add_f32 v43, 1.0, v43\n"
"v_add_f32_e64 v47, 1.0, v47\n"
"v_mfma_f32_16x16x32_bf16 v[124:127], v[56:59], a[204:207], v[124:127]\n"
"v_cvt_pk_bf16_f32 v190, v35, v39\n"
"v_cvt_pk_bf16_f32 v191, v43, v47\n"
"ds_write_b64 v247, v[190:191] offset:30720\n"
"v_mfma_f32_16x16x32_bf16 v[128:131], v[56:59], a[208:211], v[128:131]\n"
"v_mfma_f32_16x16x32_bf16 v[132:135], v[56:59], a[212:215], v[132:135]\n"
"s_waitcnt lgkmcnt(0)\n"
"s_barrier\n"
"v_mfma_f32_16x16x32_bf16 v[136:139], v[56:59], a[216:219], v[136:139]\n"
"ds_read_b128 v[64:67], v248\n"
"ds_read_b128 v[68:71], v249\n"
"v_mfma_f32_16x16x32_bf16 v[140:143], v[56:59], a[220:223], v[140:143]\n"
"ds_read_b128 v[72:75], v250\n"
"ds_read_b128 v[76:79], v251\n"
"v_mfma_f32_16x16x32_bf16 v[112:115], v[60:63], a[224:227], v[112:115]\n"
"ds_read_b128 v[0:3], v248 offset:256\n"
"ds_read_b128 v[4:7], v249 offset:256\n"
"v_mfma_f32_16x16x32_bf16 v[116:119], v[60:63], a[228:231], v[116:119]\n"
"ds_read_b128 v[8:11], v250 offset:256\n"
"ds_read_b128 v[12:15], v251 offset:256\n"
"v_mfma_f32_16x16x32_bf16 v[120:123], v[60:63], a[232:235], v[120:123]\n"
"v_cvt_pk_bf16_f32 v201, v90, v94\n"
"v_cvt_pk_bf16_f32 v202, v98, v102\n"
"v_mfma_f32_16x16x32_bf16 v[124:127], v[60:63], a[236:239], v[124:127]\n"
"v_cvt_pk_bf16_f32 v203, v106, v110\n"
"s_xor_b32 s92, s9, 0x9000\n"
"v_mfma_f32_16x16x32_bf16 v[128:131], v[60:63], a[240:243], v[128:131]\n"
"buffer_store_dwordx4 v[200:203], v242, s[48:51], s92 offen sc1\n"
"v_cvt_pk_bf16_f32 v204, v83, v87\n"
"v_mfma_f32_16x16x32_bf16 v[132:135], v[60:63], a[244:247], v[132:135]\n"
"v_cvt_pk_bf16_f32 v205, v91, v95\n"
"v_cvt_pk_bf16_f32 v206, v99, v103\n"
"v_mfma_f32_16x16x32_bf16 v[136:139], v[60:63], a[248:251], v[136:139]\n"
"v_cvt_pk_bf16_f32 v207, v107, v111\n"
"s_xor_b32 s93, s9, 0x8000\n"
"v_mfma_f32_16x16x32_bf16 v[140:143], v[60:63], a[252:255], v[140:143]\n"
"buffer_store_dwordx4 v[204:207], v242, s[48:51], s93 offen sc1\n"
"v_pk_add_f32 v[16:17], v[148:149], v[208:209] op_sel:[0,0] op_sel_hi:[1,0]\n"
"v_pk_add_f32 v[18:19], v[150:151], v[208:209] op_sel:[0,0] op_sel_hi:[1,0]\n"
"v_pk_add_f32 v[20:21], v[156:157], v[212:213] op_sel:[0,0] op_sel_hi:[1,0]\n"
"v_pk_add_f32 v[22:23], v[158:159], v[212:213] op_sel:[0,0] op_sel_hi:[1,0]\n"
"v_pk_add_f32 v[24:25], v[164:165], v[216:217] op_sel:[0,0] op_sel_hi:[1,0]\n"
"v_pk_add_f32 v[26:27], v[166:167], v[216:217] op_sel:[0,0] op_sel_hi:[1,0]\n"
"v_pk_add_f32 v[28:29], v[172:173], v[220:221] op_sel:[0,0] op_sel_hi:[1,0]\n"
"v_pk_add_f32 v[30:31], v[174:175], v[220:221] op_sel:[0,0] op_sel_hi:[1,0]\n"
"v_pk_add_f32 v[32:33], v[144:145], v[224:225] op_sel:[0,0] op_sel_hi:[1,0]\n"
"v_pk_add_f32 v[34:35], v[146:147], v[224:225] op_sel:[0,0] op_sel_hi:[1,0]\n"
"v_pk_add_f32 v[36:37], v[152:153], v[228:229] op_sel:[0,0] op_sel_hi:[1,0]\n"
"v_pk_add_f32 v[38:39], v[154:155], v[228:229] op_sel:[0,0] op_sel_hi:[1,0]\n"
"v_pk_add_f32 v[40:41], v[160:161], v[232:233] op_sel:[0,0] op_sel_hi:[1,0]\n"
"v_pk_add_f32 v[42:43], v[162:163], v[232:233] op_sel:[0,0] op_sel_hi:[1,0]\n"
"v_pk_add_f32 v[44:45], v[168:169], v[236:237] op_sel:[0,0] op_sel_hi:[1,0]\n"
"v_pk_add_f32 v[46:47], v[170:171], v[236:237] op_sel:[0,0] op_sel_hi:[1,0]\n"
"v_exp_f32 v16, v16\n"
"v_exp_f32 v17, v17\n"
"v_exp_f32 v18, v18\n"
"v_exp_f32 v19, v19\n"
"v_exp_f32 v20, v20\n"
"v_exp_f32 v21, v21\n"
"v_exp_f32 v22, v22\n"
"v_exp_f32 v23, v23\n"
"v_exp_f32 v24, v24\n"
"v_exp_f32 v25, v25\n"
"v_exp_f32 v26, v26\n"
"v_exp_f32 v27, v27\n"
"v_exp_f32 v28, v28\n"
"v_exp_f32 v29, v29\n"
"v_exp_f32 v30, v30\n"
"v_exp_f32 v31, v31\n"
"v_exp_f32 v32, v32\n"
"v_exp_f32 v33, v33\n"
"v_exp_f32 v34, v34\n"
"v_exp_f32 v35, v35\n"
"v_exp_f32 v36, v36\n"
"v_exp_f32 v37, v37\n"
"v_exp_f32 v38, v38\n"
"v_exp_f32 v39, v39\n"
"v_exp_f32 v40, v40\n"
"v_exp_f32 v41, v41\n"
"v_exp_f32 v42, v42\n"
"v_exp_f32 v43, v43\n"
"v_exp_f32 v44, v44\n"
"v_exp_f32 v45, v45\n"
"v_exp_f32 v46, v46\n"
"v_exp_f32 v47, v47\n"
"v_sub_f32 v16, -0.5, v16\n"
"v_sub_f32 v17, -0.5, v17\n"
"v_sub_f32 v18, -0.5, v18\n"
"v_sub_f32 v19, -0.5, v19\n"
"v_sub_f32 v20, -0.5, v20\n"
"v_sub_f32 v21, -0.5, v21\n"
"v_sub_f32 v22, -0.5, v22\n"
"v_sub_f32 v23, -0.5, v23\n"
"v_sub_f32 v24, -0.5, v24\n"
"v_sub_f32 v25, -0.5, v25\n"
"v_sub_f32 v26, -0.5, v26\n"
"v_sub_f32 v27, -0.5, v27\n"
"v_sub_f32 v28, -0.5, v28\n"
"v_sub_f32 v29, -0.5, v29\n"
"v_sub_f32 v30, -0.5, v30\n"
"v_sub_f32 v31, -0.5, v31\n"
"v_sub_f32 v32, -0.5, v32\n"
"v_sub_f32 v33, -0.5, v33\n"
"v_sub_f32 v34, -0.5, v34\n"
"v_sub_f32 v35, -0.5, v35\n"
"v_sub_f32 v36, -0.5, v36\n"
"v_sub_f32 v37, -0.5, v37\n"
"v_sub_f32 v38, -0.5, v38\n"
"v_sub_f32 v39, -0.5, v39\n"
"v_sub_f32 v40, -0.5, v40\n"
"v_sub_f32 v41, -0.5, v41\n"
"v_sub_f32 v42, -0.5, v42\n"
"v_sub_f32 v43, -0.5, v43\n"
"v_sub_f32 v44, -0.5, v44\n"
"v_sub_f32 v45, -0.5, v45\n"
"v_sub_f32 v46, -0.5, v46\n"
"v_sub_f32_e64 v47, -0.5, v47\n"
"s_waitcnt lgkmcnt(0)\n"
"v_mfma_f32_16x16x32_bf16 v[80:83], v[64:67], a[0:3], 0\n"
"v_rcp_f32_e64 v16, v16\n"
"v_mfma_f32_16x16x32_bf16 v[84:87], v[64:67], a[4:7], 0\n"
"v_cvt_pk_bf16_f32 v192, v112, v116\n"
"v_cvt_pk_bf16_f32 v193, v120, v124\n"
"v_cvt_pk_bf16_f32 v194, v128, v132\n"
"v_mfma_f32_16x16x32_bf16 v[88:91], v[64:67], a[8:11], 0\n"
"v_rcp_f32_e64 v20, v20\n"
"v_mfma_f32_16x16x32_bf16 v[92:95], v[64:67], a[12:15], 0\n"
"v_cvt_pk_bf16_f32 v195, v136, v140\n"
"s_xor_b32 s92, s9, 0x2000\n"
"buffer_store_dwordx4 v[192:195], v242, s[48:51], s92 offen sc1\n"
"v_mfma_f32_16x16x32_bf16 v[96:99], v[64:67], a[16:19], 0\n"
"v_rcp_f32_e64 v24, v24\n"
"v_mfma_f32_16x16x32_bf16 v[100:103], v[64:67], a[20:23], 0\n"
"v_rcp_f32_e64 v28, v28\n"
"v_mfma_f32_16x16x32_bf16 v[104:107], v[64:67], a[24:27], 0\n"
"v_cvt_pk_bf16_f32 v196, v113, v117\n"
"v_cvt_pk_bf16_f32 v197, v121, v125\n"
"v_cvt_pk_bf16_f32 v198, v129, v133\n"
"v_mfma_f32_16x16x32_bf16 v[108:111], v[64:67], a[28:31], 0\n"
"v_rcp_f32_e64 v17, v17\n"
"v_mfma_f32_16x16x32_bf16 v[80:83], v[68:71], a[32:35], v[80:83]\n"
"v_cvt_pk_bf16_f32 v199, v137, v141\n"
"s_xor_b32 s93, s9, 0x3000\n"
"buffer_store_dwordx4 v[196:199], v242, s[48:51], s93 offen sc1\n"
"v_mfma_f32_16x16x32_bf16 v[84:87], v[68:71], a[36:39], v[84:87]\n"
"v_rcp_f32_e64 v21, v21\n"
"v_mfma_f32_16x16x32_bf16 v[88:91], v[68:71], a[40:43], v[88:91]\n"
"v_rcp_f32_e64 v25, v25\n"
"v_mfma_f32_16x16x32_bf16 v[92:95], v[68:71], a[44:47], v[92:95]\n"
"v_cvt_pk_bf16_f32 v200, v114, v118\n"
"v_cvt_pk_bf16_f32 v201, v122, v126\n"
"v_cvt_pk_bf16_f32 v202, v130, v134\n"
"v_mfma_f32_16x16x32_bf16 v[96:99], v[68:71], a[48:51], v[96:99]\n"
"v_rcp_f32_e64 v29, v29\n"
"v_mfma_f32_16x16x32_bf16 v[100:103], v[68:71], a[52:55], v[100:103]\n"
"v_cvt_pk_bf16_f32 v203, v138, v142\n"
"s_xor_b32 s92, s9, 0xb000\n"
"buffer_store_dwordx4 v[200:203], v242, s[48:51], s92 offen sc1\n"
"v_mfma_f32_16x16x32_bf16 v[104:107], v[68:71], a[56:59], v[104:107]\n"
"v_rcp_f32_e64 v18, v18\n"
"v_mfma_f32_16x16x32_bf16 v[108:111], v[68:71], a[60:63], v[108:111]\n"
"v_rcp_f32_e64 v22, v22\n"
"v_mfma_f32_16x16x32_bf16 v[80:83], v[72:75], a[64:67], v[80:83]\n"
"v_cvt_pk_bf16_f32 v204, v115, v119\n"
"v_cvt_pk_bf16_f32 v205, v123, v127\n"
"v_cvt_pk_bf16_f32 v206, v131, v135\n"
"v_mfma_f32_16x16x32_bf16 v[84:87], v[72:75], a[68:71], v[84:87]\n"
"v_rcp_f32_e64 v26, v26\n"
"v_mfma_f32_16x16x32_bf16 v[88:91], v[72:75], a[72:75], v[88:91]\n"
"v_cvt_pk_bf16_f32 v207, v139, v143\n"
"s_xor_b32 s93, s9, 0xa000\n"
"buffer_store_dwordx4 v[204:207], v242, s[48:51], s93 offen sc1\n"
"v_mfma_f32_16x16x32_bf16 v[92:95], v[72:75], a[76:79], v[92:95]\n"
"v_rcp_f32_e64 v30, v30\n"
"v_mfma_f32_16x16x32_bf16 v[96:99], v[72:75], a[80:83], v[96:99]\n"
"v_rcp_f32_e64 v19, v19\n"
"v_mfma_f32_16x16x32_bf16 v[100:103], v[72:75], a[84:87], v[100:103]\n"
"v_add_f32 v16, 1.0, v16\n"
"v_add_f32 v20, 1.0, v20\n"
"v_add_f32_e64 v24, 1.0, v24\n"
"v_mfma_f32_16x16x32_bf16 v[104:107], v[72:75], a[88:91], v[104:107]\n"
"v_rcp_f32_e64 v23, v23\n"
"v_mfma_f32_16x16x32_bf16 v[108:111], v[72:75], a[92:95], v[108:111]\n"
"v_add_f32_e64 v28, 1.0, v28\n"
"v_cvt_pk_bf16_f32 v176, v16, v20\n"
"v_cvt_pk_bf16_f32 v177, v24, v28\n"
"v_mfma_f32_16x16x32_bf16 v[80:83], v[76:79], a[96:99], v[80:83]\n"
"v_rcp_f32_e64 v27, v27\n"
"v_mfma_f32_16x16x32_bf16 v[84:87], v[76:79], a[100:103], v[84:87]\n"
"v_rcp_f32_e64 v31, v31\n"
"v_mfma_f32_16x16x32_bf16 v[88:91], v[76:79], a[104:107], v[88:91]\n"
"ds_write_b64 v244, v[176:177] offset:32768\n"
"v_add_f32 v17, 1.0, v17\n"
"v_add_f32 v21, 1.0, v21\n"
"v_mfma_f32_16x16x32_bf16 v[92:95], v[76:79], a[108:111], v[92:95]\n"
"v_rcp_f32_e64 v32, v32\n"
"v_mfma_f32_16x16x32_bf16 v[96:99], v[76:79], a[112:115], v[96:99]\n"
"v_add_f32 v25, 1.0, v25\n"
"v_add_f32 v29, 1.0, v29\n"
"v_cvt_pk_bf16_f32 v178, v17, v21\n"
"v_mfma_f32_16x16x32_bf16 v[100:103], v[76:79], a[116:119], v[100:103]\n"
"v_rcp_f32_e64 v36, v36\n"
"v_mfma_f32_16x16x32_bf16 v[104:107], v[76:79], a[120:123], v[104:107]\n"
"v_rcp_f32_e64 v40, v40\n"
"v_mfma_f32_16x16x32_bf16 v[108:111], v[76:79], a[124:127], v[108:111]\n"
"v_cvt_pk_bf16_f32 v179, v25, v29\n"
"ds_write_b64 v245, v[178:179] offset:34816\n"
"v_add_f32_e64 v18, 1.0, v18\n"
"v_mfma_f32_16x16x32_bf16 v[80:83], v[0:3], a[128:131], v[80:83]\n"
"v_rcp_f32_e64 v44, v44\n"
"v_mfma_f32_16x16x32_bf16 v[84:87], v[0:3], a[132:135], v[84:87]\n"
"v_add_f32 v22, 1.0, v22\n"
"v_add_f32 v26, 1.0, v26\n"
"v_add_f32_e64 v30, 1.0, v30\n"
"v_mfma_f32_16x16x32_bf16 v[88:91], v[0:3], a[136:139], v[88:91]\n"
"v_rcp_f32_e64 v33, v33\n"
"v_mfma_f32_16x16x32_bf16 v[92:95], v[0:3], a[140:143], v[92:95]\n"
"v_rcp_f32_e64 v37, v37\n"
"v_mfma_f32_16x16x32_bf16 v[96:99], v[0:3], a[144:147], v[96:99]\n"
"v_cvt_pk_bf16_f32 v180, v18, v22\n"
"v_cvt_pk_bf16_f32 v181, v26, v30\n"
"ds_write_b64 v246, v[180:181] offset:36864\n"
"v_mfma_f32_16x16x32_bf16 v[100:103], v[0:3], a[148:151], v[100:103]\n"
"v_rcp_f32_e64 v41, v41\n"
"v_mfma_f32_16x16x32_bf16 v[104:107], v[0:3], a[152:155], v[104:107]\n"
"v_add_f32 v19, 1.0, v19\n"
"v_add_f32 v23, 1.0, v23\n"
"v_add_f32_e64 v27, 1.0, v27\n"
"v_mfma_f32_16x16x32_bf16 v[108:111], v[0:3], a[156:159], v[108:111]\n"
"v_rcp_f32_e64 v45, v45\n"
"v_mfma_f32_16x16x32_bf16 v[80:83], v[4:7], a[160:163], v[80:83]\n"
"v_rcp_f32_e64 v34, v34\n"
"v_mfma_f32_16x16x32_bf16 v[84:87], v[4:7], a[164:167], v[84:87]\n"
"v_add_f32_e64 v31, 1.0, v31\n"
"v_cvt_pk_bf16_f32 v182, v19, v23\n"
"v_cvt_pk_bf16_f32 v183, v27, v31\n"
"v_mfma_f32_16x16x32_bf16 v[88:91], v[4:7], a[168:171], v[88:91]\n"
"v_rcp_f32_e64 v38, v38\n"
"v_mfma_f32_16x16x32_bf16 v[92:95], v[4:7], a[172:175], v[92:95]\n"
"ds_write_b64 v247, v[182:183] offset:38912\n"
"v_add_f32 v32, 1.0, v32\n"
"v_add_f32 v36, 1.0, v36\n"
"v_mfma_f32_16x16x32_bf16 v[96:99], v[4:7], a[176:179], v[96:99]\n"
"v_rcp_f32_e64 v42, v42\n"
"v_mfma_f32_16x16x32_bf16 v[100:103], v[4:7], a[180:183], v[100:103]\n"
"v_rcp_f32_e64 v46, v46\n"
"v_mfma_f32_16x16x32_bf16 v[104:107], v[4:7], a[184:187], v[104:107]\n"
"v_add_f32 v40, 1.0, v40\n"
"v_add_f32 v44, 1.0, v44\n"
"v_cvt_pk_bf16_f32 v184, v32, v36\n"
"v_mfma_f32_16x16x32_bf16 v[108:111], v[4:7], a[188:191], v[108:111]\n"
"v_rcp_f32_e64 v35, v35\n"
"v_mfma_f32_16x16x32_bf16 v[80:83], v[8:11], a[192:195], v[80:83]\n"
"v_cvt_pk_bf16_f32 v185, v40, v44\n"
"ds_write_b64 v244, v[184:185] offset:40960\n"
"ds_read_b128 v[48:51], v248 offset:16384\n"
"v_mfma_f32_16x16x32_bf16 v[84:87], v[8:11], a[196:199], v[84:87]\n"
"v_rcp_f32_e64 v39, v39\n"
"v_mfma_f32_16x16x32_bf16 v[88:91], v[8:11], a[200:203], v[88:91]\n"
"v_rcp_f32_e64 v43, v43\n"
"v_mfma_f32_16x16x32_bf16 v[92:95], v[8:11], a[204:207], v[92:95]\n"
"ds_read_b128 v[52:55], v249 offset:16384\n"
"v_add_f32 v33, 1.0, v33\n"
"v_add_f32 v37, 1.0, v37\n"
"v_mfma_f32_16x16x32_bf16 v[96:99], v[8:11], a[208:211], v[96:99]\n"
"v_rcp_f32_e64 v47, v47\n"
"v_mfma_f32_16x16x32_bf16 v[100:103], v[8:11], a[212:215], v[100:103]\n"
"v_add_f32 v41, 1.0, v41\n"
"v_add_f32 v45, 1.0, v45\n"
"v_cvt_pk_bf16_f32 v186, v33, v37\n"
"v_mfma_f32_16x16x32_bf16 v[104:107], v[8:11], a[216:219], v[104:107]\n"
"v_cvt_pk_bf16_f32 v187, v41, v45\n"
"ds_write_b64 v245, v[186:187] offset:43008\n"
"ds_read_b128 v[56:59], v250 offset:16384\n"
"v_mfma_f32_16x16x32_bf16 v[108:111], v[8:11], a[220:223], v[108:111]\n"
"ds_read_b128 v[60:63], v251 offset:16384\n"
"ds_read_b128 v[64:67], v248 offset:16640\n"
"v_add_f32_e64 v34, 1.0, v34\n"
"v_mfma_f32_16x16x32_bf16 v[80:83], v[12:15], a[224:227], v[80:83]\n"
"v_add_f32 v38, 1.0, v38\n"
"v_add_f32 v42, 1.0, v42\n"
"v_mfma_f32_16x16x32_bf16 v[84:87], v[12:15], a[228:231], v[84:87]\n"
"v_add_f32_e64 v46, 1.0, v46\n"
"v_cvt_pk_bf16_f32 v188, v34, v38\n"
"v_mfma_f32_16x16x32_bf16 v[88:91], v[12:15], a[232:235], v[88:91]\n"
"v_cvt_pk_bf16_f32 v189, v42, v46\n"
"ds_write_b64 v246, v[188:189] offset:45056\n"
"v_mfma_f32_16x16x32_bf16 v[92:95], v[12:15], a[236:239], v[92:95]\n"
"ds_read_b128 v[68:71], v249 offset:16640\n"
"ds_read_b128 v[72:75], v250 offset:16640\n"
"v_mfma_f32_16x16x32_bf16 v[96:99], v[12:15], a[240:243], v[96:99]\n"
"ds_read_b128 v[76:79], v251 offset:16640\n"
"v_add_f32_e64 v35, 1.0, v35\n"
"v_mfma_f32_16x16x32_bf16 v[100:103], v[12:15], a[244:247], v[100:103]\n"
"v_add_f32 v39, 1.0, v39\n"
"v_add_f32 v43, 1.0, v43\n"
"v_mfma_f32_16x16x32_bf16 v[104:107], v[12:15], a[248:251], v[104:107]\n"
"v_add_f32_e64 v47, 1.0, v47\n"
"v_cvt_pk_bf16_f32 v190, v35, v39\n"
"v_mfma_f32_16x16x32_bf16 v[108:111], v[12:15], a[252:255], v[108:111]\n"
"v_cvt_pk_bf16_f32 v191, v43, v47\n"
"ds_write_b64 v247, v[190:191] offset:47104\n"
"v_pk_add_f32 v[16:17], v[148:149], v[208:209] op_sel:[0,1] op_sel_hi:[1,1]\n"
"v_pk_add_f32 v[18:19], v[150:151], v[208:209] op_sel:[0,1] op_sel_hi:[1,1]\n"
"v_pk_add_f32 v[20:21], v[156:157], v[212:213] op_sel:[0,1] op_sel_hi:[1,1]\n"
"v_pk_add_f32 v[22:23], v[158:159], v[212:213] op_sel:[0,1] op_sel_hi:[1,1]\n"
"v_pk_add_f32 v[24:25], v[164:165], v[216:217] op_sel:[0,1] op_sel_hi:[1,1]\n"
"v_pk_add_f32 v[26:27], v[166:167], v[216:217] op_sel:[0,1] op_sel_hi:[1,1]\n"
"v_pk_add_f32 v[28:29], v[172:173], v[220:221] op_sel:[0,1] op_sel_hi:[1,1]\n"
"v_pk_add_f32 v[30:31], v[174:175], v[220:221] op_sel:[0,1] op_sel_hi:[1,1]\n"
"v_pk_add_f32 v[32:33], v[144:145], v[224:225] op_sel:[0,1] op_sel_hi:[1,1]\n"
"v_pk_add_f32 v[34:35], v[146:147], v[224:225] op_sel:[0,1] op_sel_hi:[1,1]\n"
"v_pk_add_f32 v[36:37], v[152:153], v[228:229] op_sel:[0,1] op_sel_hi:[1,1]\n"
"v_pk_add_f32 v[38:39], v[154:155], v[228:229] op_sel:[0,1] op_sel_hi:[1,1]\n"
"v_pk_add_f32 v[40:41], v[160:161], v[232:233] op_sel:[0,1] op_sel_hi:[1,1]\n"
"v_pk_add_f32 v[42:43], v[162:163], v[232:233] op_sel:[0,1] op_sel_hi:[1,1]\n"
"v_pk_add_f32 v[44:45], v[168:169], v[236:237] op_sel:[0,1] op_sel_hi:[1,1]\n"
"v_pk_add_f32 v[46:47], v[170:171], v[236:237] op_sel:[0,1] op_sel_hi:[1,1]\n"
"v_exp_f32 v16, v16\n"
"v_exp_f32 v17, v17\n"
"v_exp_f32 v18, v18\n"
"v_exp_f32 v19, v19\n"
"v_exp_f32 v20, v20\n"
"v_exp_f32 v21, v21\n"
"v_exp_f32 v22, v22\n"
"v_exp_f32 v23, v23\n"
"v_exp_f32 v24, v24\n"
"v_exp_f32 v25, v25\n"
"v_exp_f32 v26, v26\n"
"v_exp_f32 v27, v27\n"
"v_exp_f32 v28, v28\n"
"v_exp_f32 v29, v29\n"
"v_exp_f32 v30, v30\n"
"v_exp_f32 v31, v31\n"
"v_exp_f32 v32, v32\n"
"v_exp_f32 v33, v33\n"
"v_exp_f32 v34, v34\n"
"v_exp_f32 v35, v35\n"
"v_exp_f32 v36, v36\n"
"v_exp_f32 v37, v37\n"
"v_exp_f32 v38, v38\n"
"v_exp_f32 v39, v39\n"
"v_exp_f32 v40, v40\n"
"v_exp_f32 v41, v41\n"
"v_exp_f32 v42, v42\n"
"v_exp_f32 v43, v43\n"
"v_exp_f32 v44, v44\n"
"v_exp_f32 v45, v45\n"
"v_exp_f32 v46, v46\n"
"v_exp_f32 v47, v47\n"
"v_sub_f32 v16, -0.5, v16\n"
"v_sub_f32 v17, -0.5, v17\n"
"v_sub_f32 v18, -0.5, v18\n"
"v_sub_f32 v19, -0.5, v19\n"
"v_sub_f32 v20, -0.5, v20\n"
"v_sub_f32 v21, -0.5, v21\n"
"v_sub_f32 v22, -0.5, v22\n"
"v_sub_f32 v23, -0.5, v23\n"
"v_sub_f32 v24, -0.5, v24\n"
"v_sub_f32 v25, -0.5, v25\n"
"v_sub_f32 v26, -0.5, v26\n"
"v_sub_f32 v27, -0.5, v27\n"
"v_sub_f32 v28, -0.5, v28\n"
"v_sub_f32 v29, -0.5, v29\n"
"v_sub_f32 v30, -0.5, v30\n"
"v_sub_f32 v31, -0.5, v31\n"
"v_sub_f32 v32, -0.5, v32\n"
"v_sub_f32 v33, -0.5, v33\n"
"v_sub_f32 v34, -0.5, v34\n"
"v_sub_f32 v35, -0.5, v35\n"
"v_sub_f32 v36, -0.5, v36\n"
"v_sub_f32 v37, -0.5, v37\n"
"v_sub_f32 v38, -0.5, v38\n"
"v_sub_f32 v39, -0.5, v39\n"
"v_sub_f32 v40, -0.5, v40\n"
"v_sub_f32 v41, -0.5, v41\n"
"v_sub_f32 v42, -0.5, v42\n"
"v_sub_f32 v43, -0.5, v43\n"
"v_sub_f32 v44, -0.5, v44\n"
"v_sub_f32 v45, -0.5, v45\n"
"v_sub_f32 v46, -0.5, v46\n"
"v_sub_f32_e64 v47, -0.5, v47\n"
"s_waitcnt lgkmcnt(1)\n"
"v_mfma_f32_16x16x32_bf16 v[112:115], v[48:51], a[0:3], 0\n"
"v_rcp_f32_e64 v16, v16\n"
"v_mfma_f32_16x16x32_bf16 v[116:119], v[48:51], a[4:7], 0\n"
"v_rcp_f32_e64 v20, v20\n"
"v_mfma_f32_16x16x32_bf16 v[120:123], v[48:51], a[8:11], 0\n"
"v_cvt_pk_bf16_f32 v192, v80, v84\n"
"v_cvt_pk_bf16_f32 v193, v88, v92\n"
"v_cvt_pk_bf16_f32 v194, v96, v100\n"
"v_cvt_pk_bf16_f32 v195, v104, v108\n"
"v_mfma_f32_16x16x32_bf16 v[124:127], v[48:51], a[12:15], 0\n"
"v_rcp_f32_e64 v24, v24\n"
"v_mfma_f32_16x16x32_bf16 v[128:131], v[48:51], a[16:19], 0\n"
"v_rcp_f32_e64 v28, v28\n"
"v_mfma_f32_16x16x32_bf16 v[132:135], v[48:51], a[20:23], 0\n"
"s_xor_b32 s92, s9, 0x4000\n"
"buffer_store_dwordx4 v[192:195], v242, s[48:51], s92 offen sc1\n"
"v_cvt_pk_bf16_f32 v196, v81, v85\n"
"v_cvt_pk_bf16_f32 v197, v89, v93\n"
"v_mfma_f32_16x16x32_bf16 v[136:139], v[48:51], a[24:27], 0\n"
"v_rcp_f32_e64 v17, v17\n"
"v_mfma_f32_16x16x32_bf16 v[140:143], v[48:51], a[28:31], 0\n"
"v_rcp_f32_e64 v21, v21\n"
"v_mfma_f32_16x16x32_bf16 v[112:115], v[52:55], a[32:35], v[112:115]\n"
"v_cvt_pk_bf16_f32 v198, v97, v101\n"
"v_cvt_pk_bf16_f32 v199, v105, v109\n"
"s_xor_b32 s93, s9, 0x5000\n"
"buffer_store_dwordx4 v[196:199], v242, s[48:51], s93 offen sc1\n"
"v_mfma_f32_16x16x32_bf16 v[116:119], v[52:55], a[36:39], v[116:119]\n"
"v_rcp_f32_e64 v25, v25\n"
"v_mfma_f32_16x16x32_bf16 v[120:123], v[52:55], a[40:43], v[120:123]\n"
"v_rcp_f32_e64 v29, v29\n"
"v_mfma_f32_16x16x32_bf16 v[124:127], v[52:55], a[44:47], v[124:127]\n"
"v_cvt_pk_bf16_f32 v200, v82, v86\n"
"v_add_f32 v16, 1.0, v16\n"
"v_add_f32 v20, 1.0, v20\n"
"v_add_f32_e64 v24, 1.0, v24\n"
"v_mfma_f32_16x16x32_bf16 v[128:131], v[52:55], a[48:51], v[128:131]\n"
"v_rcp_f32_e64 v18, v18\n"
"v_mfma_f32_16x16x32_bf16 v[132:135], v[52:55], a[52:55], v[132:135]\n"
"v_rcp_f32_e64 v22, v22\n"
"v_mfma_f32_16x16x32_bf16 v[136:139], v[52:55], a[56:59], v[136:139]\n"
"v_add_f32_e64 v28, 1.0, v28\n"
"v_cvt_pk_bf16_f32 v176, v16, v20\n"
"v_cvt_pk_bf16_f32 v177, v24, v28\n"
"ds_write_b64 v244, v[176:177] offset:49152\n"
"v_mfma_f32_16x16x32_bf16 v[140:143], v[52:55], a[60:63], v[140:143]\n"
"v_rcp_f32_e64 v26, v26\n"
"v_mfma_f32_16x16x32_bf16 v[112:115], v[56:59], a[64:67], v[112:115]\n"
"v_rcp_f32_e64 v30, v30\n"
"v_mfma_f32_16x16x32_bf16 v[116:119], v[56:59], a[68:71], v[116:119]\n"
"v_add_f32 v17, 1.0, v17\n"
"v_add_f32 v21, 1.0, v21\n"
"v_add_f32 v25, 1.0, v25\n"
"v_add_f32 v29, 1.0, v29\n"
"v_mfma_f32_16x16x32_bf16 v[120:123], v[56:59], a[72:75], v[120:123]\n"
"v_rcp_f32_e64 v19, v19\n"
"v_mfma_f32_16x16x32_bf16 v[124:127], v[56:59], a[76:79], v[124:127]\n"
"v_rcp_f32_e64 v23, v23\n"
"v_mfma_f32_16x16x32_bf16 v[128:131], v[56:59], a[80:83], v[128:131]\n"
"v_cvt_pk_bf16_f32 v178, v17, v21\n"
"v_cvt_pk_bf16_f32 v179, v25, v29\n"
"ds_write_b64 v245, v[178:179] offset:51200\n"
"v_add_f32_e64 v18, 1.0, v18\n"
"v_mfma_f32_16x16x32_bf16 v[132:135], v[56:59], a[84:87], v[132:135]\n"
"v_rcp_f32_e64 v27, v27\n"
"v_mfma_f32_16x16x32_bf16 v[136:139], v[56:59], a[88:91], v[136:139]\n"
"v_rcp_f32_e64 v31, v31\n"
"v_mfma_f32_16x16x32_bf16 v[140:143], v[56:59], a[92:95], v[140:143]\n"
"v_add_f32 v22, 1.0, v22\n"
"v_add_f32 v26, 1.0, v26\n"
"v_add_f32_e64 v30, 1.0, v30\n"
"v_cvt_pk_bf16_f32 v180, v18, v22\n"
"v_mfma_f32_16x16x32_bf16 v[112:115], v[60:63], a[96:99], v[112:115]\n"
"v_rcp_f32_e64 v32, v32\n"
"v_mfma_f32_16x16x32_bf16 v[116:119], v[60:63], a[100:103], v[116:119]\n"
"v_rcp_f32_e64 v36, v36\n"
"v_mfma_f32_16x16x32_bf16 v[120:123], v[60:63], a[104:107], v[120:123]\n"
"v_cvt_pk_bf16_f32 v181, v26, v30\n"
"ds_write_b64 v246, v[180:181] offset:53248\n"
"v_add_f32 v19, 1.0, v19\n"
"v_add_f32 v23, 1.0, v23\n"
"v_mfma_f32_16x16x32_bf16 v[124:127], v[60:63], a[108:111], v[124:127]\n"
"v_rcp_f32_e64 v40, v40\n"
"v_mfma_f32_16x16x32_bf16 v[128:131], v[60:63], a[112:115], v[128:131]\n"
"v_rcp_f32_e64 v44, v44\n"
"v_mfma_f32_16x16x32_bf16 v[132:135], v[60:63], a[116:119], v[132:135]\n"
"v_add_f32 v27, 1.0, v27\n"
"v_add_f32 v31, 1.0, v31\n"
"v_cvt_pk_bf16_f32 v182, v19, v23\n"
"v_mfma_f32_16x16x32_bf16 v[136:139], v[60:63], a[120:123], v[136:139]\n"
"v_rcp_f32_e64 v33, v33\n"
"v_mfma_f32_16x16x32_bf16 v[140:143], v[60:63], a[124:127], v[140:143]\n"
"v_rcp_f32_e64 v37, v37\n"
"v_mfma_f32_16x16x32_bf16 v[112:115], v[64:67], a[128:131], v[112:115]\n"
"v_cvt_pk_bf16_f32 v183, v27, v31\n"
"ds_write_b64 v247, v[182:183] offset:55296\n"
"v_add_f32_e64 v32, 1.0, v32\n"
"v_mfma_f32_16x16x32_bf16 v[116:119], v[64:67], a[132:135], v[116:119]\n"
"v_rcp_f32_e64 v41, v41\n"
"v_mfma_f32_16x16x32_bf16 v[120:123], v[64:67], a[136:139], v[120:123]\n"
"v_rcp_f32_e64 v45, v45\n"
"v_mfma_f32_16x16x32_bf16 v[124:127], v[64:67], a[140:143], v[124:127]\n"
"v_add_f32 v36, 1.0, v36\n"
"v_add_f32 v40, 1.0, v40\n"
"v_add_f32_e64 v44, 1.0, v44\n"
"v_mfma_f32_16x16x32_bf16 v[128:131], v[64:67], a[144:147], v[128:131]\n"
"v_rcp_f32_e64 v34, v34\n"
"v_mfma_f32_16x16x32_bf16 v[132:135], v[64:67], a[148:151], v[132:135]\n"
"v_rcp_f32_e64 v38, v38\n"
"v_mfma_f32_16x16x32_bf16 v[136:139], v[64:67], a[152:155], v[136:139]\n"
"v_cvt_pk_bf16_f32 v184, v32, v36\n"
"v_cvt_pk_bf16_f32 v185, v40, v44\n"
"ds_write_b64 v244, v[184:185] offset:57344\n"
"v_mfma_f32_16x16x32_bf16 v[140:143], v[64:67], a[156:159], v[140:143]\n"
"v_rcp_f32_e64 v42, v42\n"
"v_mfma_f32_16x16x32_bf16 v[112:115], v[68:71], a[160:163], v[112:115]\n"
"v_rcp_f32_e64 v46, v46\n"
"v_mfma_f32_16x16x32_bf16 v[116:119], v[68:71], a[164:167], v[116:119]\n"
"v_add_f32 v33, 1.0, v33\n"
"v_add_f32 v37, 1.0, v37\n"
"v_add_f32_e64 v41, 1.0, v41\n"
"v_mfma_f32_16x16x32_bf16 v[120:123], v[68:71], a[168:171], v[120:123]\n"
"v_rcp_f32_e64 v35, v35\n"
"v_mfma_f32_16x16x32_bf16 v[124:127], v[68:71], a[172:175], v[124:127]\n"
"v_rcp_f32_e64 v39, v39\n"
"v_mfma_f32_16x16x32_bf16 v[128:131], v[68:71], a[176:179], v[128:131]\n"
"v_add_f32_e64 v45, 1.0, v45\n"
"v_cvt_pk_bf16_f32 v186, v33, v37\n"
"v_cvt_pk_bf16_f32 v187, v41, v45\n"
"v_mfma_f32_16x16x32_bf16 v[132:135], v[68:71], a[180:183], v[132:135]\n"
"v_rcp_f32_e64 v43, v43\n"
"v_mfma_f32_16x16x32_bf16 v[136:139], v[68:71], a[184:187], v[136:139]\n"
"v_rcp_f32_e64 v47, v47\n"
"v_mfma_f32_16x16x32_bf16 v[140:143], v[68:71], a[188:191], v[140:143]\n"
"ds_write_b64 v245, v[186:187] offset:59392\n"
"v_add_f32 v34, 1.0, v34\n"
"v_add_f32 v38, 1.0, v38\n"
"v_mfma_f32_16x16x32_bf16 v[112:115], v[72:75], a[192:195], v[112:115]\n"
"v_add_f32 v42, 1.0, v42\n"
"v_add_f32 v46, 1.0, v46\n"
"v_cvt_pk_bf16_f32 v188, v34, v38\n"
"v_mfma_f32_16x16x32_bf16 v[116:119], v[72:75], a[196:199], v[116:119]\n"
"v_cvt_pk_bf16_f32 v189, v42, v46\n"
"ds_write_b64 v246, v[188:189] offset:61440\n"
"v_add_f32_e64 v35, 1.0, v35\n"
"v_mfma_f32_16x16x32_bf16 v[120:123], v[72:75], a[200:203], v[120:123]\n"
"v_add_f32 v39, 1.0, v39\n"
"v_add_f32 v43, 1.0, v43\n"
"v_add_f32_e64 v47, 1.0, v47\n"
"v_mfma_f32_16x16x32_bf16 v[124:127], v[72:75], a[204:207], v[124:127]\n"
"v_cvt_pk_bf16_f32 v190, v35, v39\n"
"v_cvt_pk_bf16_f32 v191, v43, v47\n"
"ds_write_b64 v247, v[190:191] offset:63488\n"
"v_mfma_f32_16x16x32_bf16 v[128:131], v[72:75], a[208:211], v[128:131]\n"
"v_mfma_f32_16x16x32_bf16 v[132:135], v[72:75], a[212:215], v[132:135]\n"
"s_waitcnt lgkmcnt(0)\n"
"s_barrier\n"
"v_mfma_f32_16x16x32_bf16 v[136:139], v[72:75], a[216:219], v[136:139]\n"
"ds_read_b128 v[0:3], v248 offset:32768\n"
"ds_read_b128 v[4:7], v249 offset:32768\n"
"v_mfma_f32_16x16x32_bf16 v[140:143], v[72:75], a[220:223], v[140:143]\n"
"ds_read_b128 v[8:11], v250 offset:32768\n"
"ds_read_b128 v[12:15], v251 offset:32768\n"
"v_mfma_f32_16x16x32_bf16 v[112:115], v[76:79], a[224:227], v[112:115]\n"
"ds_read_b128 v[48:51], v248 offset:33024\n"
"ds_read_b128 v[52:55], v249 offset:33024\n"
"v_mfma_f32_16x16x32_bf16 v[116:119], v[76:79], a[228:231], v[116:119]\n"
"ds_read_b128 v[56:59], v250 offset:33024\n"
"ds_read_b128 v[60:63], v251 offset:33024\n"
"v_mfma_f32_16x16x32_bf16 v[120:123], v[76:79], a[232:235], v[120:123]\n"
"v_cvt_pk_bf16_f32 v201, v90, v94\n"
"v_cvt_pk_bf16_f32 v202, v98, v102\n"
"v_mfma_f32_16x16x32_bf16 v[124:127], v[76:79], a[236:239], v[124:127]\n"
"v_cvt_pk_bf16_f32 v203, v106, v110\n"
"s_xor_b32 s92, s9, 0xd000\n"
"v_mfma_f32_16x16x32_bf16 v[128:131], v[76:79], a[240:243], v[128:131]\n"
"buffer_store_dwordx4 v[200:203], v242, s[48:51], s92 offen sc1\n"
"v_cvt_pk_bf16_f32 v204, v83, v87\n"
"v_mfma_f32_16x16x32_bf16 v[132:135], v[76:79], a[244:247], v[132:135]\n"
"v_cvt_pk_bf16_f32 v205, v91, v95\n"
"v_cvt_pk_bf16_f32 v206, v99, v103\n"
"v_mfma_f32_16x16x32_bf16 v[136:139], v[76:79], a[248:251], v[136:139]\n"
"v_cvt_pk_bf16_f32 v207, v107, v111\n"
"s_xor_b32 s93, s9, 0xc000\n"
"v_mfma_f32_16x16x32_bf16 v[140:143], v[76:79], a[252:255], v[140:143]\n"
"buffer_store_dwordx4 v[204:207], v242, s[48:51], s93 offen sc1\n"
"v_pk_add_f32 v[16:17], v[148:149], v[210:211] op_sel:[0,0] op_sel_hi:[1,0]\n"
"v_pk_add_f32 v[18:19], v[150:151], v[210:211] op_sel:[0,0] op_sel_hi:[1,0]\n"
"v_pk_add_f32 v[20:21], v[156:157], v[214:215] op_sel:[0,0] op_sel_hi:[1,0]\n"
"v_pk_add_f32 v[22:23], v[158:159], v[214:215] op_sel:[0,0] op_sel_hi:[1,0]\n"
"v_pk_add_f32 v[24:25], v[164:165], v[218:219] op_sel:[0,0] op_sel_hi:[1,0]\n"
"v_pk_add_f32 v[26:27], v[166:167], v[218:219] op_sel:[0,0] op_sel_hi:[1,0]\n"
"v_pk_add_f32 v[28:29], v[172:173], v[222:223] op_sel:[0,0] op_sel_hi:[1,0]\n"
"v_pk_add_f32 v[30:31], v[174:175], v[222:223] op_sel:[0,0] op_sel_hi:[1,0]\n"
"v_pk_add_f32 v[32:33], v[144:145], v[226:227] op_sel:[0,0] op_sel_hi:[1,0]\n"
"v_pk_add_f32 v[34:35], v[146:147], v[226:227] op_sel:[0,0] op_sel_hi:[1,0]\n"
"v_pk_add_f32 v[36:37], v[152:153], v[230:231] op_sel:[0,0] op_sel_hi:[1,0]\n"
"v_pk_add_f32 v[38:39], v[154:155], v[230:231] op_sel:[0,0] op_sel_hi:[1,0]\n"
"v_pk_add_f32 v[40:41], v[160:161], v[234:235] op_sel:[0,0] op_sel_hi:[1,0]\n"
"v_pk_add_f32 v[42:43], v[162:163], v[234:235] op_sel:[0,0] op_sel_hi:[1,0]\n"
"v_pk_add_f32 v[44:45], v[168:169], v[238:239] op_sel:[0,0] op_sel_hi:[1,0]\n"
"v_pk_add_f32 v[46:47], v[170:171], v[238:239] op_sel:[0,0] op_sel_hi:[1,0]\n"
"v_exp_f32 v16, v16\n"
"v_exp_f32 v17, v17\n"
"v_exp_f32 v18, v18\n"
"v_exp_f32 v19, v19\n"
"v_exp_f32 v20, v20\n"
"v_exp_f32 v21, v21\n"
"v_exp_f32 v22, v22\n"
"v_exp_f32 v23, v23\n"
"v_exp_f32 v24, v24\n"
"v_exp_f32 v25, v25\n"
"v_exp_f32 v26, v26\n"
"v_exp_f32 v27, v27\n"
"v_exp_f32 v28, v28\n"
"v_exp_f32 v29, v29\n"
"v_exp_f32 v30, v30\n"
"v_exp_f32 v31, v31\n"
"v_exp_f32 v32, v32\n"
"v_exp_f32 v33, v33\n"
"v_exp_f32 v34, v34\n"
"v_exp_f32 v35, v35\n"
"v_exp_f32 v36, v36\n"
"v_exp_f32 v37, v37\n"
"v_exp_f32 v38, v38\n"
"v_exp_f32 v39, v39\n"
"v_exp_f32 v40, v40\n"
"v_exp_f32 v41, v41\n"
"v_exp_f32 v42, v42\n"
"v_exp_f32 v43, v43\n"
"v_exp_f32 v44, v44\n"
"v_exp_f32 v45, v45\n"
"v_exp_f32 v46, v46\n"
"v_exp_f32 v47, v47\n"
"v_sub_f32 v16, -0.5, v16\n"
"v_sub_f32 v17, -0.5, v17\n"
"v_sub_f32 v18, -0.5, v18\n"
"v_sub_f32 v19, -0.5, v19\n"
"v_sub_f32 v20, -0.5, v20\n"
"v_sub_f32 v21, -0.5, v21\n"
"v_sub_f32 v22, -0.5, v22\n"
"v_sub_f32 v23, -0.5, v23\n"
"v_sub_f32 v24, -0.5, v24\n"
"v_sub_f32 v25, -0.5, v25\n"
"v_sub_f32 v26, -0.5, v26\n"
"v_sub_f32 v27, -0.5, v27\n"
"v_sub_f32 v28, -0.5, v28\n"
"v_sub_f32 v29, -0.5, v29\n"
"v_sub_f32 v30, -0.5, v30\n"
"v_sub_f32 v31, -0.5, v31\n"
"v_sub_f32 v32, -0.5, v32\n"
"v_sub_f32 v33, -0.5, v33\n"
"v_sub_f32 v34, -0.5, v34\n"
"v_sub_f32 v35, -0.5, v35\n"
"v_sub_f32 v36, -0.5, v36\n"
"v_sub_f32 v37, -0.5, v37\n"
"v_sub_f32 v38, -0.5, v38\n"
"v_sub_f32 v39, -0.5, v39\n"
"v_sub_f32 v40, -0.5, v40\n"
"v_sub_f32 v41, -0.5, v41\n"
"v_sub_f32 v42, -0.5, v42\n"
"v_sub_f32 v43, -0.5, v43\n"
"v_sub_f32 v44, -0.5, v44\n"
"v_sub_f32 v45, -0.5, v45\n"
"v_sub_f32 v46, -0.5, v46\n"
"v_sub_f32_e64 v47, -0.5, v47\n"
"s_waitcnt lgkmcnt(0)\n"
"v_mfma_f32_16x16x32_bf16 v[80:83], v[0:3], a[0:3], 0\n"
"v_rcp_f32_e64 v16, v16\n"
"v_mfma_f32_16x16x32_bf16 v[84:87], v[0:3], a[4:7], 0\n"
"v_cvt_pk_bf16_f32 v192, v112, v116\n"
"v_cvt_pk_bf16_f32 v193, v120, v124\n"
"v_cvt_pk_bf16_f32 v194, v128, v132\n"
"v_mfma_f32_16x16x32_bf16 v[88:91], v[0:3], a[8:11], 0\n"
"v_rcp_f32_e64 v20, v20\n"
"v_mfma_f32_16x16x32_bf16 v[92:95], v[0:3], a[12:15], 0\n"
"v_cvt_pk_bf16_f32 v195, v136, v140\n"
"s_xor_b32 s92, s9, 0x6000\n"
"buffer_store_dwordx4 v[192:195], v242, s[48:51], s92 offen sc1\n"
"v_mfma_f32_16x16x32_bf16 v[96:99], v[0:3], a[16:19], 0\n"
"v_rcp_f32_e64 v24, v24\n"
"v_mfma_f32_16x16x32_bf16 v[100:103], v[0:3], a[20:23], 0\n"
"v_rcp_f32_e64 v28, v28\n"
"v_mfma_f32_16x16x32_bf16 v[104:107], v[0:3], a[24:27], 0\n"
"v_cvt_pk_bf16_f32 v196, v113, v117\n"
"v_cvt_pk_bf16_f32 v197, v121, v125\n"
"v_cvt_pk_bf16_f32 v198, v129, v133\n"
"v_mfma_f32_16x16x32_bf16 v[108:111], v[0:3], a[28:31], 0\n"
"v_rcp_f32_e64 v17, v17\n"
"v_mfma_f32_16x16x32_bf16 v[80:83], v[4:7], a[32:35], v[80:83]\n"
"v_cvt_pk_bf16_f32 v199, v137, v141\n"
"s_xor_b32 s93, s9, 0x7000\n"
"buffer_store_dwordx4 v[196:199], v242, s[48:51], s93 offen sc1\n"
"v_mfma_f32_16x16x32_bf16 v[84:87], v[4:7], a[36:39], v[84:87]\n"
"v_rcp_f32_e64 v21, v21\n"
"v_mfma_f32_16x16x32_bf16 v[88:91], v[4:7], a[40:43], v[88:91]\n"
"v_rcp_f32_e64 v25, v25\n"
"v_mfma_f32_16x16x32_bf16 v[92:95], v[4:7], a[44:47], v[92:95]\n"
"v_cvt_pk_bf16_f32 v200, v114, v118\n"
"v_cvt_pk_bf16_f32 v201, v122, v126\n"
"v_cvt_pk_bf16_f32 v202, v130, v134\n"
"v_mfma_f32_16x16x32_bf16 v[96:99], v[4:7], a[48:51], v[96:99]\n"
"v_rcp_f32_e64 v29, v29\n"
"v_mfma_f32_16x16x32_bf16 v[100:103], v[4:7], a[52:55], v[100:103]\n"
"v_cvt_pk_bf16_f32 v203, v138, v142\n"
"s_xor_b32 s92, s9, 0xf000\n"
"buffer_store_dwordx4 v[200:203], v242, s[48:51], s92 offen sc1\n"
"v_mfma_f32_16x16x32_bf16 v[104:107], v[4:7], a[56:59], v[104:107]\n"
"v_rcp_f32_e64 v18, v18\n"
"v_mfma_f32_16x16x32_bf16 v[108:111], v[4:7], a[60:63], v[108:111]\n"
"v_rcp_f32_e64 v22, v22\n"
"v_mfma_f32_16x16x32_bf16 v[80:83], v[8:11], a[64:67], v[80:83]\n"
"v_cvt_pk_bf16_f32 v204, v115, v119\n"
"v_cvt_pk_bf16_f32 v205, v123, v127\n"
"v_cvt_pk_bf16_f32 v206, v131, v135\n"
"v_mfma_f32_16x16x32_bf16 v[84:87], v[8:11], a[68:71], v[84:87]\n"
"v_rcp_f32_e64 v26, v26\n"
"v_mfma_f32_16x16x32_bf16 v[88:91], v[8:11], a[72:75], v[88:91]\n"
"v_cvt_pk_bf16_f32 v207, v139, v143\n"
"s_xor_b32 s93, s9, 0xe000\n"
"buffer_store_dwordx4 v[204:207], v242, s[48:51], s93 offen sc1\n"
"v_mfma_f32_16x16x32_bf16 v[92:95], v[8:11], a[76:79], v[92:95]\n"
"v_rcp_f32_e64 v30, v30\n"
"v_mfma_f32_16x16x32_bf16 v[96:99], v[8:11], a[80:83], v[96:99]\n"
"v_rcp_f32_e64 v19, v19\n"
"v_mfma_f32_16x16x32_bf16 v[100:103], v[8:11], a[84:87], v[100:103]\n"
"v_add_f32 v16, 1.0, v16\n"
"v_add_f32 v20, 1.0, v20\n"
"v_add_f32_e64 v24, 1.0, v24\n"
"v_mfma_f32_16x16x32_bf16 v[104:107], v[8:11], a[88:91], v[104:107]\n"
"v_rcp_f32_e64 v23, v23\n"
"v_mfma_f32_16x16x32_bf16 v[108:111], v[8:11], a[92:95], v[108:111]\n"
"v_add_f32_e64 v28, 1.0, v28\n"
"v_cvt_pk_bf16_f32 v176, v16, v20\n"
"v_cvt_pk_bf16_f32 v177, v24, v28\n"
"v_mfma_f32_16x16x32_bf16 v[80:83], v[12:15], a[96:99], v[80:83]\n"
"v_rcp_f32_e64 v27, v27\n"
"v_mfma_f32_16x16x32_bf16 v[84:87], v[12:15], a[100:103], v[84:87]\n"
"v_rcp_f32_e64 v31, v31\n"
"v_mfma_f32_16x16x32_bf16 v[88:91], v[12:15], a[104:107], v[88:91]\n"
"ds_write_b64 v244, v[176:177]\n"
"v_add_f32 v17, 1.0, v17\n"
"v_add_f32 v21, 1.0, v21\n"
"v_mfma_f32_16x16x32_bf16 v[92:95], v[12:15], a[108:111], v[92:95]\n"
"v_rcp_f32_e64 v32, v32\n"
"v_mfma_f32_16x16x32_bf16 v[96:99], v[12:15], a[112:115], v[96:99]\n"
"v_add_f32 v25, 1.0, v25\n"
"v_add_f32 v29, 1.0, v29\n"
"v_cvt_pk_bf16_f32 v178, v17, v21\n"
"v_mfma_f32_16x16x32_bf16 v[100:103], v[12:15], a[116:119], v[100:103]\n"
"v_rcp_f32_e64 v36, v36\n"
"v_mfma_f32_16x16x32_bf16 v[104:107], v[12:15], a[120:123], v[104:107]\n"
"v_rcp_f32_e64 v40, v40\n"
"v_mfma_f32_16x16x32_bf16 v[108:111], v[12:15], a[124:127], v[108:111]\n"
"v_cvt_pk_bf16_f32 v179, v25, v29\n"
"ds_write_b64 v245, v[178:179] offset:2048\n"
"v_add_f32_e64 v18, 1.0, v18\n"
"v_mfma_f32_16x16x32_bf16 v[80:83], v[48:51], a[128:131], v[80:83]\n"
"v_rcp_f32_e64 v44, v44\n"
"v_mfma_f32_16x16x32_bf16 v[84:87], v[48:51], a[132:135], v[84:87]\n"
"v_add_f32 v22, 1.0, v22\n"
"v_add_f32 v26, 1.0, v26\n"
"v_add_f32_e64 v30, 1.0, v30\n"
"v_mfma_f32_16x16x32_bf16 v[88:91], v[48:51], a[136:139], v[88:91]\n"
"v_rcp_f32_e64 v33, v33\n"
"v_mfma_f32_16x16x32_bf16 v[92:95], v[48:51], a[140:143], v[92:95]\n"
"v_rcp_f32_e64 v37, v37\n"
"v_mfma_f32_16x16x32_bf16 v[96:99], v[48:51], a[144:147], v[96:99]\n"
"v_cvt_pk_bf16_f32 v180, v18, v22\n"
"v_cvt_pk_bf16_f32 v181, v26, v30\n"
"ds_write_b64 v246, v[180:181] offset:4096\n"
"v_mfma_f32_16x16x32_bf16 v[100:103], v[48:51], a[148:151], v[100:103]\n"
"v_rcp_f32_e64 v41, v41\n"
"v_mfma_f32_16x16x32_bf16 v[104:107], v[48:51], a[152:155], v[104:107]\n"
"v_add_f32 v19, 1.0, v19\n"
"v_add_f32 v23, 1.0, v23\n"
"v_add_f32_e64 v27, 1.0, v27\n"
"v_mfma_f32_16x16x32_bf16 v[108:111], v[48:51], a[156:159], v[108:111]\n"
"v_rcp_f32_e64 v45, v45\n"
"v_mfma_f32_16x16x32_bf16 v[80:83], v[52:55], a[160:163], v[80:83]\n"
"v_rcp_f32_e64 v34, v34\n"
"v_mfma_f32_16x16x32_bf16 v[84:87], v[52:55], a[164:167], v[84:87]\n"
"v_add_f32_e64 v31, 1.0, v31\n"
"v_cvt_pk_bf16_f32 v182, v19, v23\n"
"v_cvt_pk_bf16_f32 v183, v27, v31\n"
"v_mfma_f32_16x16x32_bf16 v[88:91], v[52:55], a[168:171], v[88:91]\n"
"v_rcp_f32_e64 v38, v38\n"
"v_mfma_f32_16x16x32_bf16 v[92:95], v[52:55], a[172:175], v[92:95]\n"
"ds_write_b64 v247, v[182:183] offset:6144\n"
"v_add_f32 v32, 1.0, v32\n"
"v_add_f32 v36, 1.0, v36\n"
"v_mfma_f32_16x16x32_bf16 v[96:99], v[52:55], a[176:179], v[96:99]\n"
"v_rcp_f32_e64 v42, v42\n"
"v_mfma_f32_16x16x32_bf16 v[100:103], v[52:55], a[180:183], v[100:103]\n"
"v_rcp_f32_e64 v46, v46\n"
"v_mfma_f32_16x16x32_bf16 v[104:107], v[52:55], a[184:187], v[104:107]\n"
"v_add_f32 v40, 1.0, v40\n"
"v_add_f32 v44, 1.0, v44\n"
"v_cvt_pk_bf16_f32 v184, v32, v36\n"
"v_mfma_f32_16x16x32_bf16 v[108:111], v[52:55], a[188:191], v[108:111]\n"
"v_rcp_f32_e64 v35, v35\n"
"v_mfma_f32_16x16x32_bf16 v[80:83], v[56:59], a[192:195], v[80:83]\n"
"v_cvt_pk_bf16_f32 v185, v40, v44\n"
"ds_write_b64 v244, v[184:185] offset:8192\n"
"ds_read_b128 v[64:67], v248 offset:49152\n"
"v_mfma_f32_16x16x32_bf16 v[84:87], v[56:59], a[196:199], v[84:87]\n"
"v_rcp_f32_e64 v39, v39\n"
"v_mfma_f32_16x16x32_bf16 v[88:91], v[56:59], a[200:203], v[88:91]\n"
"v_rcp_f32_e64 v43, v43\n"
"v_mfma_f32_16x16x32_bf16 v[92:95], v[56:59], a[204:207], v[92:95]\n"
"ds_read_b128 v[68:71], v249 offset:49152\n"
"v_add_f32 v33, 1.0, v33\n"
"v_add_f32 v37, 1.0, v37\n"
"v_mfma_f32_16x16x32_bf16 v[96:99], v[56:59], a[208:211], v[96:99]\n"
"v_rcp_f32_e64 v47, v47\n"
"v_mfma_f32_16x16x32_bf16 v[100:103], v[56:59], a[212:215], v[100:103]\n"
"v_add_f32 v41, 1.0, v41\n"
"v_add_f32 v45, 1.0, v45\n"
"v_cvt_pk_bf16_f32 v186, v33, v37\n"
"v_mfma_f32_16x16x32_bf16 v[104:107], v[56:59], a[216:219], v[104:107]\n"
"v_cvt_pk_bf16_f32 v187, v41, v45\n"
"ds_write_b64 v245, v[186:187] offset:10240\n"
"ds_read_b128 v[72:75], v250 offset:49152\n"
"v_mfma_f32_16x16x32_bf16 v[108:111], v[56:59], a[220:223], v[108:111]\n"
"ds_read_b128 v[76:79], v251 offset:49152\n"
"ds_read_b128 v[0:3], v248 offset:49408\n"
"v_add_f32_e64 v34, 1.0, v34\n"
"v_mfma_f32_16x16x32_bf16 v[80:83], v[60:63], a[224:227], v[80:83]\n"
"v_add_f32 v38, 1.0, v38\n"
"v_add_f32 v42, 1.0, v42\n"
"v_mfma_f32_16x16x32_bf16 v[84:87], v[60:63], a[228:231], v[84:87]\n"
"v_add_f32_e64 v46, 1.0, v46\n"
"v_cvt_pk_bf16_f32 v188, v34, v38\n"
"v_mfma_f32_16x16x32_bf16 v[88:91], v[60:63], a[232:235], v[88:91]\n"
"v_cvt_pk_bf16_f32 v189, v42, v46\n"
"ds_write_b64 v246, v[188:189] offset:12288\n"
"v_mfma_f32_16x16x32_bf16 v[92:95], v[60:63], a[236:239], v[92:95]\n"
"ds_read_b128 v[4:7], v249 offset:49408\n"
"ds_read_b128 v[8:11], v250 offset:49408\n"
"v_mfma_f32_16x16x32_bf16 v[96:99], v[60:63], a[240:243], v[96:99]\n"
"ds_read_b128 v[12:15], v251 offset:49408\n"
"v_add_f32_e64 v35, 1.0, v35\n"
"v_mfma_f32_16x16x32_bf16 v[100:103], v[60:63], a[244:247], v[100:103]\n"
"v_add_f32 v39, 1.0, v39\n"
"v_add_f32 v43, 1.0, v43\n"
"v_mfma_f32_16x16x32_bf16 v[104:107], v[60:63], a[248:251], v[104:107]\n"
"v_add_f32_e64 v47, 1.0, v47\n"
"v_cvt_pk_bf16_f32 v190, v35, v39\n"
"v_mfma_f32_16x16x32_bf16 v[108:111], v[60:63], a[252:255], v[108:111]\n"
"v_cvt_pk_bf16_f32 v191, v43, v47\n"
"ds_write_b64 v247, v[190:191] offset:14336\n"
"v_pk_add_f32 v[16:17], v[148:149], v[210:211] op_sel:[0,1] op_sel_hi:[1,1]\n"
"v_pk_add_f32 v[18:19], v[150:151], v[210:211] op_sel:[0,1] op_sel_hi:[1,1]\n"
"v_pk_add_f32 v[20:21], v[156:157], v[214:215] op_sel:[0,1] op_sel_hi:[1,1]\n"
"v_pk_add_f32 v[22:23], v[158:159], v[214:215] op_sel:[0,1] op_sel_hi:[1,1]\n"
"v_pk_add_f32 v[24:25], v[164:165], v[218:219] op_sel:[0,1] op_sel_hi:[1,1]\n"
"v_pk_add_f32 v[26:27], v[166:167], v[218:219] op_sel:[0,1] op_sel_hi:[1,1]\n"
"v_pk_add_f32 v[28:29], v[172:173], v[222:223] op_sel:[0,1] op_sel_hi:[1,1]\n"
"v_pk_add_f32 v[30:31], v[174:175], v[222:223] op_sel:[0,1] op_sel_hi:[1,1]\n"
"v_pk_add_f32 v[32:33], v[144:145], v[226:227] op_sel:[0,1] op_sel_hi:[1,1]\n"
"v_pk_add_f32 v[34:35], v[146:147], v[226:227] op_sel:[0,1] op_sel_hi:[1,1]\n"
"v_pk_add_f32 v[36:37], v[152:153], v[230:231] op_sel:[0,1] op_sel_hi:[1,1]\n"
"v_pk_add_f32 v[38:39], v[154:155], v[230:231] op_sel:[0,1] op_sel_hi:[1,1]\n"
"v_pk_add_f32 v[40:41], v[160:161], v[234:235] op_sel:[0,1] op_sel_hi:[1,1]\n"
"v_pk_add_f32 v[42:43], v[162:163], v[234:235] op_sel:[0,1] op_sel_hi:[1,1]\n"
"v_pk_add_f32 v[44:45], v[168:169], v[238:239] op_sel:[0,1] op_sel_hi:[1,1]\n"
"v_pk_add_f32 v[46:47], v[170:171], v[238:239] op_sel:[0,1] op_sel_hi:[1,1]\n"
"v_exp_f32 v16, v16\n"
"v_exp_f32 v17, v17\n"
"v_exp_f32 v18, v18\n"
"v_exp_f32 v19, v19\n"
"v_exp_f32 v20, v20\n"
"v_exp_f32 v21, v21\n"
"v_exp_f32 v22, v22\n"
"v_exp_f32 v23, v23\n"
"v_exp_f32 v24, v24\n"
"v_exp_f32 v25, v25\n"
"v_exp_f32 v26, v26\n"
"v_exp_f32 v27, v27\n"
"v_exp_f32 v28, v28\n"
"v_exp_f32 v29, v29\n"
"v_exp_f32 v30, v30\n"
"v_exp_f32 v31, v31\n"
"v_exp_f32 v32, v32\n"
"v_exp_f32 v33, v33\n"
"v_exp_f32 v34, v34\n"
"v_exp_f32 v35, v35\n"
"v_exp_f32 v36, v36\n"
"v_exp_f32 v37, v37\n"
"v_exp_f32 v38, v38\n"
"v_exp_f32 v39, v39\n"
"v_exp_f32 v40, v40\n"
"v_exp_f32 v41, v41\n"
"v_exp_f32 v42, v42\n"
"v_exp_f32 v43, v43\n"
"v_exp_f32 v44, v44\n"
"v_exp_f32 v45, v45\n"
"v_exp_f32 v46, v46\n"
"v_exp_f32 v47, v47\n"
"v_sub_f32 v16, -0.5, v16\n"
"v_sub_f32 v17, -0.5, v17\n"
"v_sub_f32 v18, -0.5, v18\n"
"v_sub_f32 v19, -0.5, v19\n"
"v_sub_f32 v20, -0.5, v20\n"
"v_sub_f32 v21, -0.5, v21\n"
"v_sub_f32 v22, -0.5, v22\n"
"v_sub_f32 v23, -0.5, v23\n"
"v_sub_f32 v24, -0.5, v24\n"
"v_sub_f32 v25, -0.5, v25\n"
"v_sub_f32 v26, -0.5, v26\n"
"v_sub_f32 v27, -0.5, v27\n"
"v_sub_f32 v28, -0.5, v28\n"
"v_sub_f32 v29, -0.5, v29\n"
"v_sub_f32 v30, -0.5, v30\n"
"v_sub_f32 v31, -0.5, v31\n"
"v_sub_f32 v32, -0.5, v32\n"
"v_sub_f32 v33, -0.5, v33\n"
"v_sub_f32 v34, -0.5, v34\n"
"v_sub_f32 v35, -0.5, v35\n"
"v_sub_f32 v36, -0.5, v36\n"
"v_sub_f32 v37, -0.5, v37\n"
"v_sub_f32 v38, -0.5, v38\n"
"v_sub_f32 v39, -0.5, v39\n"
"v_sub_f32 v40, -0.5, v40\n"
"v_sub_f32 v41, -0.5, v41\n"
"v_sub_f32 v42, -0.5, v42\n"
"v_sub_f32 v43, -0.5, v43\n"
"v_sub_f32 v44, -0.5, v44\n"
"v_sub_f32 v45, -0.5, v45\n"
"v_sub_f32 v46, -0.5, v46\n"
"v_sub_f32_e64 v47, -0.5, v47\n"
"s_waitcnt lgkmcnt(1)\n"
"v_mfma_f32_16x16x32_bf16 v[112:115], v[64:67], a[0:3], 0\n"
"v_rcp_f32_e64 v16, v16\n"
"v_mfma_f32_16x16x32_bf16 v[116:119], v[64:67], a[4:7], 0\n"
"v_rcp_f32_e64 v20, v20\n"
"v_mfma_f32_16x16x32_bf16 v[120:123], v[64:67], a[8:11], 0\n"
"v_xor_b32 v242, 0x800, v242\n"
"v_cvt_pk_bf16_f32 v192, v80, v84\n"
"v_cvt_pk_bf16_f32 v193, v88, v92\n"
"v_cvt_pk_bf16_f32 v194, v96, v100\n"
"v_mfma_f32_16x16x32_bf16 v[124:127], v[64:67], a[12:15], 0\n"
"v_rcp_f32_e64 v24, v24\n"
"v_mfma_f32_16x16x32_bf16 v[128:131], v[64:67], a[16:19], 0\n"
"v_rcp_f32_e64 v28, v28\n"
"v_mfma_f32_16x16x32_bf16 v[132:135], v[64:67], a[20:23], 0\n"
"v_cvt_pk_bf16_f32 v195, v104, v108\n"
"s_xor_b32 s92, s9, 0\n"
"buffer_store_dwordx4 v[192:195], v242, s[48:51], s92 offen sc1\n"
"v_cvt_pk_bf16_f32 v196, v81, v85\n"
"v_mfma_f32_16x16x32_bf16 v[136:139], v[64:67], a[24:27], 0\n"
"v_rcp_f32 v17, v17\n"
"v_mfma_f32_16x16x32_bf16 v[140:143], v[64:67], a[28:31], 0\n"
"v_rcp_f32_e64 v21, v21\n"
"v_mfma_f32_16x16x32_bf16 v[112:115], v[68:71], a[32:35], v[112:115]\n"
"v_cvt_pk_bf16_f32 v197, v89, v93\n"
"v_cvt_pk_bf16_f32 v198, v97, v101\n"
"v_cvt_pk_bf16_f32 v199, v105, v109\n"
"s_xor_b32 s93, s9, 0x1000\n"
"v_mfma_f32_16x16x32_bf16 v[116:119], v[68:71], a[36:39], v[116:119]\n"
"v_rcp_f32_e64 v25, v25\n"
"v_mfma_f32_16x16x32_bf16 v[120:123], v[68:71], a[40:43], v[120:123]\n"
"v_rcp_f32_e64 v29, v29\n"
"v_mfma_f32_16x16x32_bf16 v[124:127], v[68:71], a[44:47], v[124:127]\n"
"buffer_store_dwordx4 v[196:199], v242, s[48:51], s93 offen sc1\n"
"v_add_f32 v16, 1.0, v16\n"
"v_add_f32 v20, 1.0, v20\n"
"v_add_f32_e64 v24, 1.0, v24\n"
"v_mfma_f32_16x16x32_bf16 v[128:131], v[68:71], a[48:51], v[128:131]\n"
"v_rcp_f32_e64 v18, v18\n"
"v_mfma_f32_16x16x32_bf16 v[132:135], v[68:71], a[52:55], v[132:135]\n"
"v_rcp_f32_e64 v22, v22\n"
"v_mfma_f32_16x16x32_bf16 v[136:139], v[68:71], a[56:59], v[136:139]\n"
"v_add_f32_e64 v28, 1.0, v28\n"
"v_cvt_pk_bf16_f32 v176, v16, v20\n"
"v_cvt_pk_bf16_f32 v177, v24, v28\n"
"ds_write_b64 v244, v[176:177] offset:16384\n"
"v_mfma_f32_16x16x32_bf16 v[140:143], v[68:71], a[60:63], v[140:143]\n"
"v_rcp_f32_e64 v26, v26\n"
"v_mfma_f32_16x16x32_bf16 v[112:115], v[72:75], a[64:67], v[112:115]\n"
"v_rcp_f32_e64 v30, v30\n"
"v_mfma_f32_16x16x32_bf16 v[116:119], v[72:75], a[68:71], v[116:119]\n"
"v_add_f32 v17, 1.0, v17\n"
"v_add_f32 v21, 1.0, v21\n"
"v_add_f32 v25, 1.0, v25\n"
"v_add_f32 v29, 1.0, v29\n"
"v_mfma_f32_16x16x32_bf16 v[120:123], v[72:75], a[72:75], v[120:123]\n"
"v_rcp_f32_e64 v19, v19\n"
"v_mfma_f32_16x16x32_bf16 v[124:127], v[72:75], a[76:79], v[124:127]\n"
"v_rcp_f32_e64 v23, v23\n"
"v_mfma_f32_16x16x32_bf16 v[128:131], v[72:75], a[80:83], v[128:131]\n"
"v_cvt_pk_bf16_f32 v178, v17, v21\n"
"v_cvt_pk_bf16_f32 v179, v25, v29\n"
"ds_write_b64 v245, v[178:179] offset:18432\n"
"v_add_f32_e64 v18, 1.0, v18\n"
"v_mfma_f32_16x16x32_bf16 v[132:135], v[72:75], a[84:87], v[132:135]\n"
"v_rcp_f32_e64 v27, v27\n"
"v_mfma_f32_16x16x32_bf16 v[136:139], v[72:75], a[88:91], v[136:139]\n"
"v_rcp_f32_e64 v31, v31\n"
"v_mfma_f32_16x16x32_bf16 v[140:143], v[72:75], a[92:95], v[140:143]\n"
"v_add_f32 v22, 1.0, v22\n"
"v_add_f32 v26, 1.0, v26\n"
"v_add_f32_e64 v30, 1.0, v30\n"
"v_cvt_pk_bf16_f32 v180, v18, v22\n"
"v_mfma_f32_16x16x32_bf16 v[112:115], v[76:79], a[96:99], v[112:115]\n"
"v_rcp_f32_e64 v32, v32\n"
"v_mfma_f32_16x16x32_bf16 v[116:119], v[76:79], a[100:103], v[116:119]\n"
"v_rcp_f32_e64 v36, v36\n"
"v_mfma_f32_16x16x32_bf16 v[120:123], v[76:79], a[104:107], v[120:123]\n"
"v_cvt_pk_bf16_f32 v181, v26, v30\n"
"ds_write_b64 v246, v[180:181] offset:20480\n"
"v_add_f32 v19, 1.0, v19\n"
"v_add_f32 v23, 1.0, v23\n"
"v_mfma_f32_16x16x32_bf16 v[124:127], v[76:79], a[108:111], v[124:127]\n"
"v_rcp_f32_e64 v40, v40\n"
"v_mfma_f32_16x16x32_bf16 v[128:131], v[76:79], a[112:115], v[128:131]\n"
"v_rcp_f32_e64 v44, v44\n"
"v_mfma_f32_16x16x32_bf16 v[132:135], v[76:79], a[116:119], v[132:135]\n"
"v_add_f32 v27, 1.0, v27\n"
"v_add_f32 v31, 1.0, v31\n"
"v_cvt_pk_bf16_f32 v182, v19, v23\n"
"v_mfma_f32_16x16x32_bf16 v[136:139], v[76:79], a[120:123], v[136:139]\n"
"v_rcp_f32_e64 v33, v33\n"
"v_mfma_f32_16x16x32_bf16 v[140:143], v[76:79], a[124:127], v[140:143]\n"
"v_rcp_f32_e64 v37, v37\n"
"v_mfma_f32_16x16x32_bf16 v[112:115], v[0:3], a[128:131], v[112:115]\n"
"v_cvt_pk_bf16_f32 v183, v27, v31\n"
"ds_write_b64 v247, v[182:183] offset:22528\n"
"v_add_f32_e64 v32, 1.0, v32\n"
"v_mfma_f32_16x16x32_bf16 v[116:119], v[0:3], a[132:135], v[116:119]\n"
"v_rcp_f32_e64 v41, v41\n"
"v_mfma_f32_16x16x32_bf16 v[120:123], v[0:3], a[136:139], v[120:123]\n"
"v_rcp_f32_e64 v45, v45\n"
"v_mfma_f32_16x16x32_bf16 v[124:127], v[0:3], a[140:143], v[124:127]\n"
"v_add_f32 v36, 1.0, v36\n"
"v_add_f32 v40, 1.0, v40\n"
"v_add_f32_e64 v44, 1.0, v44\n"
"v_mfma_f32_16x16x32_bf16 v[128:131], v[0:3], a[144:147], v[128:131]\n"
"v_rcp_f32_e64 v34, v34\n"
"v_mfma_f32_16x16x32_bf16 v[132:135], v[0:3], a[148:151], v[132:135]\n"
"v_rcp_f32_e64 v38, v38\n"
"v_mfma_f32_16x16x32_bf16 v[136:139], v[0:3], a[152:155], v[136:139]\n"
"v_cvt_pk_bf16_f32 v184, v32, v36\n"
"v_cvt_pk_bf16_f32 v185, v40, v44\n"
"ds_write_b64 v244, v[184:185] offset:24576\n"
"v_mfma_f32_16x16x32_bf16 v[140:143], v[0:3], a[156:159], v[140:143]\n"
"v_rcp_f32_e64 v42, v42\n"
"v_mfma_f32_16x16x32_bf16 v[112:115], v[4:7], a[160:163], v[112:115]\n"
"v_rcp_f32_e64 v46, v46\n"
"v_mfma_f32_16x16x32_bf16 v[116:119], v[4:7], a[164:167], v[116:119]\n"
"v_add_f32 v33, 1.0, v33\n"
"v_add_f32 v37, 1.0, v37\n"
"v_add_f32_e64 v41, 1.0, v41\n"
"v_mfma_f32_16x16x32_bf16 v[120:123], v[4:7], a[168:171], v[120:123]\n"
"v_rcp_f32_e64 v35, v35\n"
"v_mfma_f32_16x16x32_bf16 v[124:127], v[4:7], a[172:175], v[124:127]\n"
"v_rcp_f32_e64 v39, v39\n"
"v_mfma_f32_16x16x32_bf16 v[128:131], v[4:7], a[176:179], v[128:131]\n"
"v_add_f32_e64 v45, 1.0, v45\n"
"v_cvt_pk_bf16_f32 v186, v33, v37\n"
"v_cvt_pk_bf16_f32 v187, v41, v45\n"
"v_mfma_f32_16x16x32_bf16 v[132:135], v[4:7], a[180:183], v[132:135]\n"
"v_rcp_f32_e64 v43, v43\n"
"v_mfma_f32_16x16x32_bf16 v[136:139], v[4:7], a[184:187], v[136:139]\n"
"v_rcp_f32_e64 v47, v47\n"
"v_mfma_f32_16x16x32_bf16 v[140:143], v[4:7], a[188:191], v[140:143]\n"
"ds_write_b64 v245, v[186:187] offset:26624\n"
"v_add_f32 v34, 1.0, v34\n"
"v_add_f32 v38, 1.0, v38\n"
"v_mfma_f32_16x16x32_bf16 v[112:115], v[8:11], a[192:195], v[112:115]\n"
"v_add_f32 v42, 1.0, v42\n"
"v_add_f32 v46, 1.0, v46\n"
"v_cvt_pk_bf16_f32 v188, v34, v38\n"
"v_mfma_f32_16x16x32_bf16 v[116:119], v[8:11], a[196:199], v[116:119]\n"
"v_cvt_pk_bf16_f32 v189, v42, v46\n"
"ds_write_b64 v246, v[188:189] offset:28672\n"
"v_add_f32_e64 v35, 1.0, v35\n"
"v_mfma_f32_16x16x32_bf16 v[120:123], v[8:11], a[200:203], v[120:123]\n"
"v_add_f32 v39, 1.0, v39\n"
"v_add_f32 v43, 1.0, v43\n"
"v_add_f32_e64 v47, 1.0, v47\n"
"v_mfma_f32_16x16x32_bf16 v[124:127], v[8:11], a[204:207], v[124:127]\n"
"v_cvt_pk_bf16_f32 v190, v35, v39\n"
"v_cvt_pk_bf16_f32 v191, v43, v47\n"
"ds_write_b64 v247, v[190:191] offset:30720\n"
"v_mfma_f32_16x16x32_bf16 v[128:131], v[8:11], a[208:211], v[128:131]\n"
"v_mfma_f32_16x16x32_bf16 v[132:135], v[8:11], a[212:215], v[132:135]\n"
"s_waitcnt lgkmcnt(0)\n"
"s_barrier\n"
"v_mfma_f32_16x16x32_bf16 v[136:139], v[8:11], a[216:219], v[136:139]\n"
"ds_read_b128 v[48:51], v248\n"
"ds_read_b128 v[52:55], v249\n"
"v_mfma_f32_16x16x32_bf16 v[140:143], v[8:11], a[220:223], v[140:143]\n"
"ds_read_b128 v[56:59], v250\n"
"ds_read_b128 v[60:63], v251\n"
"v_mfma_f32_16x16x32_bf16 v[112:115], v[12:15], a[224:227], v[112:115]\n"
"ds_read_b128 v[64:67], v248 offset:256\n"
"ds_read_b128 v[68:71], v249 offset:256\n"
"v_mfma_f32_16x16x32_bf16 v[116:119], v[12:15], a[228:231], v[116:119]\n"
"ds_read_b128 v[72:75], v250 offset:256\n"
"ds_read_b128 v[76:79], v251 offset:256\n"
"v_mfma_f32_16x16x32_bf16 v[120:123], v[12:15], a[232:235], v[120:123]\n"
"v_cvt_pk_bf16_f32 v200, v82, v86\n"
"v_cvt_pk_bf16_f32 v201, v90, v94\n"
"v_mfma_f32_16x16x32_bf16 v[124:127], v[12:15], a[236:239], v[124:127]\n"
"v_cvt_pk_bf16_f32 v202, v98, v102\n"
"v_cvt_pk_bf16_f32 v203, v106, v110\n"
"v_mfma_f32_16x16x32_bf16 v[128:131], v[12:15], a[240:243], v[128:131]\n"
"s_xor_b32 s92, s9, 0x9000\n"
"buffer_store_dwordx4 v[200:203], v242, s[48:51], s92 offen sc1\n"
"v_mfma_f32_16x16x32_bf16 v[132:135], v[12:15], a[244:247], v[132:135]\n"
"v_cvt_pk_bf16_f32 v204, v83, v87\n"
"v_cvt_pk_bf16_f32 v205, v91, v95\n"
"v_mfma_f32_16x16x32_bf16 v[136:139], v[12:15], a[248:251], v[136:139]\n"
"v_cvt_pk_bf16_f32 v206, v99, v103\n"
"v_cvt_pk_bf16_f32 v207, v107, v111\n"
"v_mfma_f32_16x16x32_bf16 v[140:143], v[12:15], a[252:255], v[140:143]\n"
"s_xor_b32 s93, s9, 0x8000\n"
"buffer_store_dwordx4 v[204:207], v242, s[48:51], s93 offen sc1\n"
"s_waitcnt lgkmcnt(0)\n"
"s_nop 0\n"
"v_mfma_f32_16x16x32_bf16 v[80:83], v[48:51], a[0:3], 0\n"
"v_cvt_pk_bf16_f32 v192, v112, v116\n"
"v_cvt_pk_bf16_f32 v193, v120, v124\n"
"v_mfma_f32_16x16x32_bf16 v[84:87], v[48:51], a[4:7], 0\n"
"v_cvt_pk_bf16_f32 v194, v128, v132\n"
"v_cvt_pk_bf16_f32 v195, v136, v140\n"
"v_mfma_f32_16x16x32_bf16 v[88:91], v[48:51], a[8:11], 0\n"
"s_xor_b32 s92, s9, 0x2000\n"
"buffer_store_dwordx4 v[192:195], v242, s[48:51], s92 offen sc1\n"
"v_mfma_f32_16x16x32_bf16 v[92:95], v[48:51], a[12:15], 0\n"
"v_cvt_pk_bf16_f32 v196, v113, v117\n"
"v_cvt_pk_bf16_f32 v197, v121, v125\n"
"v_mfma_f32_16x16x32_bf16 v[96:99], v[48:51], a[16:19], 0\n"
"v_cvt_pk_bf16_f32 v198, v129, v133\n"
"v_cvt_pk_bf16_f32 v199, v137, v141\n"
"v_mfma_f32_16x16x32_bf16 v[100:103], v[48:51], a[20:23], 0\n"
"s_xor_b32 s93, s9, 0x3000\n"
"buffer_store_dwordx4 v[196:199], v242, s[48:51], s93 offen sc1\n"
"v_mfma_f32_16x16x32_bf16 v[104:107], v[48:51], a[24:27], 0\n"
"v_cvt_pk_bf16_f32 v200, v114, v118\n"
"v_cvt_pk_bf16_f32 v201, v122, v126\n"
"v_mfma_f32_16x16x32_bf16 v[108:111], v[48:51], a[28:31], 0\n"
"v_cvt_pk_bf16_f32 v202, v130, v134\n"
"v_cvt_pk_bf16_f32 v203, v138, v142\n"
"v_mfma_f32_16x16x32_bf16 v[80:83], v[52:55], a[32:35], v[80:83]\n"
"s_xor_b32 s92, s9, 0xb000\n"
"buffer_store_dwordx4 v[200:203], v242, s[48:51], s92 offen sc1\n"
"v_mfma_f32_16x16x32_bf16 v[84:87], v[52:55], a[36:39], v[84:87]\n"
"v_cvt_pk_bf16_f32 v204, v115, v119\n"
"v_cvt_pk_bf16_f32 v205, v123, v127\n"
"v_mfma_f32_16x16x32_bf16 v[88:91], v[52:55], a[40:43], v[88:91]\n"
"v_cvt_pk_bf16_f32 v206, v131, v135\n"
"v_cvt_pk_bf16_f32 v207, v139, v143\n"
"v_mfma_f32_16x16x32_bf16 v[92:95], v[52:55], a[44:47], v[92:95]\n"
"s_xor_b32 s93, s9, 0xa000\n"
"buffer_store_dwordx4 v[204:207], v242, s[48:51], s93 offen sc1\n"
"v_mfma_f32_16x16x32_bf16 v[96:99], v[52:55], a[48:51], v[96:99]\n"
"v_mfma_f32_16x16x32_bf16 v[100:103], v[52:55], a[52:55], v[100:103]\n"
"v_mfma_f32_16x16x32_bf16 v[104:107], v[52:55], a[56:59], v[104:107]\n"
"v_mfma_f32_16x16x32_bf16 v[108:111], v[52:55], a[60:63], v[108:111]\n"
"v_mfma_f32_16x16x32_bf16 v[80:83], v[56:59], a[64:67], v[80:83]\n"
"v_mfma_f32_16x16x32_bf16 v[84:87], v[56:59], a[68:71], v[84:87]\n"
"v_mfma_f32_16x16x32_bf16 v[88:91], v[56:59], a[72:75], v[88:91]\n"
"v_mfma_f32_16x16x32_bf16 v[92:95], v[56:59], a[76:79], v[92:95]\n"
"v_mfma_f32_16x16x32_bf16 v[96:99], v[56:59], a[80:83], v[96:99]\n"
"v_mfma_f32_16x16x32_bf16 v[100:103], v[56:59], a[84:87], v[100:103]\n"
"v_mfma_f32_16x16x32_bf16 v[104:107], v[56:59], a[88:91], v[104:107]\n"
"v_mfma_f32_16x16x32_bf16 v[108:111], v[56:59], a[92:95], v[108:111]\n"
"v_mfma_f32_16x16x32_bf16 v[80:83], v[60:63], a[96:99], v[80:83]\n"
"v_mfma_f32_16x16x32_bf16 v[84:87], v[60:63], a[100:103], v[84:87]\n"
"v_mfma_f32_16x16x32_bf16 v[88:91], v[60:63], a[104:107], v[88:91]\n"
"v_mfma_f32_16x16x32_bf16 v[92:95], v[60:63], a[108:111], v[92:95]\n"
"v_mfma_f32_16x16x32_bf16 v[96:99], v[60:63], a[112:115], v[96:99]\n"
"v_mfma_f32_16x16x32_bf16 v[100:103], v[60:63], a[116:119], v[100:103]\n"
"v_mfma_f32_16x16x32_bf16 v[104:107], v[60:63], a[120:123], v[104:107]\n"
"v_mfma_f32_16x16x32_bf16 v[108:111], v[60:63], a[124:127], v[108:111]\n"
"v_mfma_f32_16x16x32_bf16 v[80:83], v[64:67], a[128:131], v[80:83]\n"
"v_mfma_f32_16x16x32_bf16 v[84:87], v[64:67], a[132:135], v[84:87]\n"
"v_mfma_f32_16x16x32_bf16 v[88:91], v[64:67], a[136:139], v[88:91]\n"
"v_mfma_f32_16x16x32_bf16 v[92:95], v[64:67], a[140:143], v[92:95]\n"
"v_mfma_f32_16x16x32_bf16 v[96:99], v[64:67], a[144:147], v[96:99]\n"
"v_mfma_f32_16x16x32_bf16 v[100:103], v[64:67], a[148:151], v[100:103]\n"
"v_mfma_f32_16x16x32_bf16 v[104:107], v[64:67], a[152:155], v[104:107]\n"
"v_mfma_f32_16x16x32_bf16 v[108:111], v[64:67], a[156:159], v[108:111]\n"
"v_mfma_f32_16x16x32_bf16 v[80:83], v[68:71], a[160:163], v[80:83]\n"
"ds_read_b128 v[0:3], v248 offset:16384\n"
"v_mfma_f32_16x16x32_bf16 v[84:87], v[68:71], a[164:167], v[84:87]\n"
"v_mfma_f32_16x16x32_bf16 v[88:91], v[68:71], a[168:171], v[88:91]\n"
"ds_read_b128 v[4:7], v249 offset:16384\n"
"v_mfma_f32_16x16x32_bf16 v[92:95], v[68:71], a[172:175], v[92:95]\n"
"v_mfma_f32_16x16x32_bf16 v[96:99], v[68:71], a[176:179], v[96:99]\n"
"ds_read_b128 v[8:11], v250 offset:16384\n"
"v_mfma_f32_16x16x32_bf16 v[100:103], v[68:71], a[180:183], v[100:103]\n"
"v_mfma_f32_16x16x32_bf16 v[104:107], v[68:71], a[184:187], v[104:107]\n"
"ds_read_b128 v[12:15], v251 offset:16384\n"
"v_mfma_f32_16x16x32_bf16 v[108:111], v[68:71], a[188:191], v[108:111]\n"
"v_mfma_f32_16x16x32_bf16 v[80:83], v[72:75], a[192:195], v[80:83]\n"
"ds_read_b128 v[48:51], v248 offset:16640\n"
"v_mfma_f32_16x16x32_bf16 v[84:87], v[72:75], a[196:199], v[84:87]\n"
"v_mfma_f32_16x16x32_bf16 v[88:91], v[72:75], a[200:203], v[88:91]\n"
"ds_read_b128 v[52:55], v249 offset:16640\n"
"v_mfma_f32_16x16x32_bf16 v[92:95], v[72:75], a[204:207], v[92:95]\n"
"v_mfma_f32_16x16x32_bf16 v[96:99], v[72:75], a[208:211], v[96:99]\n"
"ds_read_b128 v[56:59], v250 offset:16640\n"
"v_mfma_f32_16x16x32_bf16 v[100:103], v[72:75], a[212:215], v[100:103]\n"
"v_mfma_f32_16x16x32_bf16 v[104:107], v[72:75], a[216:219], v[104:107]\n"
"ds_read_b128 v[60:63], v251 offset:16640\n"
"v_mfma_f32_16x16x32_bf16 v[108:111], v[72:75], a[220:223], v[108:111]\n"
"v_mfma_f32_16x16x32_bf16 v[80:83], v[76:79], a[224:227], v[80:83]\n"
"v_mfma_f32_16x16x32_bf16 v[84:87], v[76:79], a[228:231], v[84:87]\n"
"v_mfma_f32_16x16x32_bf16 v[88:91], v[76:79], a[232:235], v[88:91]\n"
"v_mfma_f32_16x16x32_bf16 v[92:95], v[76:79], a[236:239], v[92:95]\n"
"v_mfma_f32_16x16x32_bf16 v[96:99], v[76:79], a[240:243], v[96:99]\n"
"v_mfma_f32_16x16x32_bf16 v[100:103], v[76:79], a[244:247], v[100:103]\n"
"v_mfma_f32_16x16x32_bf16 v[104:107], v[76:79], a[248:251], v[104:107]\n"
"v_mfma_f32_16x16x32_bf16 v[108:111], v[76:79], a[252:255], v[108:111]\n"
"s_waitcnt lgkmcnt(0)\n"
"v_mfma_f32_16x16x32_bf16 v[112:115], v[0:3], a[0:3], 0\n"
"v_cvt_pk_bf16_f32 v192, v80, v84\n"
"s_nop 0\n"
"v_cvt_pk_bf16_f32 v193, v88, v92\n"
"v_mfma_f32_16x16x32_bf16 v[116:119], v[0:3], a[4:7], 0\n"
"v_cvt_pk_bf16_f32 v194, v96, v100\n"
"s_nop 0\n"
"s_nop 0\n"
"v_cvt_pk_bf16_f32 v195, v104, v108\n"
"v_mfma_f32_16x16x32_bf16 v[120:123], v[0:3], a[8:11], 0\n"
"s_xor_b32 s92, s9, 0x4000\n"
"buffer_store_dwordx4 v[192:195], v242, s[48:51], s92 offen sc1\n"
"v_mfma_f32_16x16x32_bf16 v[124:127], v[0:3], a[12:15], 0\n"
"v_cvt_pk_bf16_f32 v196, v81, v85\n"
"v_cvt_pk_bf16_f32 v197, v89, v93\n"
"v_mfma_f32_16x16x32_bf16 v[128:131], v[0:3], a[16:19], 0\n"
"v_cvt_pk_bf16_f32 v198, v97, v101\n"
"v_cvt_pk_bf16_f32 v199, v105, v109\n"
"v_mfma_f32_16x16x32_bf16 v[132:135], v[0:3], a[20:23], 0\n"
"s_xor_b32 s93, s9, 0x5000\n"
"buffer_store_dwordx4 v[196:199], v242, s[48:51], s93 offen sc1\n"
"v_mfma_f32_16x16x32_bf16 v[136:139], v[0:3], a[24:27], 0\n"
"v_cvt_pk_bf16_f32 v200, v82, v86\n"
"v_cvt_pk_bf16_f32 v201, v90, v94\n"
"v_mfma_f32_16x16x32_bf16 v[140:143], v[0:3], a[28:31], 0\n"
"v_cvt_pk_bf16_f32 v202, v98, v102\n"
"v_cvt_pk_bf16_f32 v203, v106, v110\n"
"v_mfma_f32_16x16x32_bf16 v[112:115], v[4:7], a[32:35], v[112:115]\n"
"s_xor_b32 s92, s9, 0xd000\n"
"buffer_store_dwordx4 v[200:203], v242, s[48:51], s92 offen sc1\n"
"v_mfma_f32_16x16x32_bf16 v[116:119], v[4:7], a[36:39], v[116:119]\n"
"v_cvt_pk_bf16_f32 v204, v83, v87\n"
"v_cvt_pk_bf16_f32 v205, v91, v95\n"
"v_mfma_f32_16x16x32_bf16 v[120:123], v[4:7], a[40:43], v[120:123]\n"
"v_cvt_pk_bf16_f32 v206, v99, v103\n"
"v_cvt_pk_bf16_f32 v207, v107, v111\n"
"v_mfma_f32_16x16x32_bf16 v[124:127], v[4:7], a[44:47], v[124:127]\n"
"s_xor_b32 s93, s9, 0xc000\n"
"buffer_store_dwordx4 v[204:207], v242, s[48:51], s93 offen sc1\n"
"v_mfma_f32_16x16x32_bf16 v[128:131], v[4:7], a[48:51], v[128:131]\n"
"v_mfma_f32_16x16x32_bf16 v[132:135], v[4:7], a[52:55], v[132:135]\n"
"v_mfma_f32_16x16x32_bf16 v[136:139], v[4:7], a[56:59], v[136:139]\n"
"v_mfma_f32_16x16x32_bf16 v[140:143], v[4:7], a[60:63], v[140:143]\n"
"v_mfma_f32_16x16x32_bf16 v[112:115], v[8:11], a[64:67], v[112:115]\n"
"v_mfma_f32_16x16x32_bf16 v[116:119], v[8:11], a[68:71], v[116:119]\n"
"v_mfma_f32_16x16x32_bf16 v[120:123], v[8:11], a[72:75], v[120:123]\n"
"v_mfma_f32_16x16x32_bf16 v[124:127], v[8:11], a[76:79], v[124:127]\n"
"v_mfma_f32_16x16x32_bf16 v[128:131], v[8:11], a[80:83], v[128:131]\n"
"v_mfma_f32_16x16x32_bf16 v[132:135], v[8:11], a[84:87], v[132:135]\n"
"v_mfma_f32_16x16x32_bf16 v[136:139], v[8:11], a[88:91], v[136:139]\n"
"v_mfma_f32_16x16x32_bf16 v[140:143], v[8:11], a[92:95], v[140:143]\n"
"v_mfma_f32_16x16x32_bf16 v[112:115], v[12:15], a[96:99], v[112:115]\n"
"v_mfma_f32_16x16x32_bf16 v[116:119], v[12:15], a[100:103], v[116:119]\n"
"v_mfma_f32_16x16x32_bf16 v[120:123], v[12:15], a[104:107], v[120:123]\n"
"v_mfma_f32_16x16x32_bf16 v[124:127], v[12:15], a[108:111], v[124:127]\n"
"v_mfma_f32_16x16x32_bf16 v[128:131], v[12:15], a[112:115], v[128:131]\n"
"v_mfma_f32_16x16x32_bf16 v[132:135], v[12:15], a[116:119], v[132:135]\n"
"v_mfma_f32_16x16x32_bf16 v[136:139], v[12:15], a[120:123], v[136:139]\n"
"v_mfma_f32_16x16x32_bf16 v[140:143], v[12:15], a[124:127], v[140:143]\n"
"v_mfma_f32_16x16x32_bf16 v[112:115], v[48:51], a[128:131], v[112:115]\n"
"v_mfma_f32_16x16x32_bf16 v[116:119], v[48:51], a[132:135], v[116:119]\n"
"v_mfma_f32_16x16x32_bf16 v[120:123], v[48:51], a[136:139], v[120:123]\n"
"v_mfma_f32_16x16x32_bf16 v[124:127], v[48:51], a[140:143], v[124:127]\n"
"v_mfma_f32_16x16x32_bf16 v[128:131], v[48:51], a[144:147], v[128:131]\n"
"v_mfma_f32_16x16x32_bf16 v[132:135], v[48:51], a[148:151], v[132:135]\n"
"v_mfma_f32_16x16x32_bf16 v[136:139], v[48:51], a[152:155], v[136:139]\n"
"v_mfma_f32_16x16x32_bf16 v[140:143], v[48:51], a[156:159], v[140:143]\n"
"v_mfma_f32_16x16x32_bf16 v[112:115], v[52:55], a[160:163], v[112:115]\n"
"v_mfma_f32_16x16x32_bf16 v[116:119], v[52:55], a[164:167], v[116:119]\n"
"v_mfma_f32_16x16x32_bf16 v[120:123], v[52:55], a[168:171], v[120:123]\n"
"v_mfma_f32_16x16x32_bf16 v[124:127], v[52:55], a[172:175], v[124:127]\n"
"v_mfma_f32_16x16x32_bf16 v[128:131], v[52:55], a[176:179], v[128:131]\n"
"v_mfma_f32_16x16x32_bf16 v[132:135], v[52:55], a[180:183], v[132:135]\n"
"v_mfma_f32_16x16x32_bf16 v[136:139], v[52:55], a[184:187], v[136:139]\n"
"v_mfma_f32_16x16x32_bf16 v[140:143], v[52:55], a[188:191], v[140:143]\n"
"v_mfma_f32_16x16x32_bf16 v[112:115], v[56:59], a[192:195], v[112:115]\n"
"v_mfma_f32_16x16x32_bf16 v[116:119], v[56:59], a[196:199], v[116:119]\n"
"v_mfma_f32_16x16x32_bf16 v[120:123], v[56:59], a[200:203], v[120:123]\n"
"v_mfma_f32_16x16x32_bf16 v[124:127], v[56:59], a[204:207], v[124:127]\n"
"v_mfma_f32_16x16x32_bf16 v[128:131], v[56:59], a[208:211], v[128:131]\n"
"v_mfma_f32_16x16x32_bf16 v[132:135], v[56:59], a[212:215], v[132:135]\n"
"v_mfma_f32_16x16x32_bf16 v[136:139], v[56:59], a[216:219], v[136:139]\n"
"v_mfma_f32_16x16x32_bf16 v[140:143], v[56:59], a[220:223], v[140:143]\n"
"v_mfma_f32_16x16x32_bf16 v[112:115], v[60:63], a[224:227], v[112:115]\n"
"v_mfma_f32_16x16x32_bf16 v[116:119], v[60:63], a[228:231], v[116:119]\n"
"v_mfma_f32_16x16x32_bf16 v[120:123], v[60:63], a[232:235], v[120:123]\n"
"v_mfma_f32_16x16x32_bf16 v[124:127], v[60:63], a[236:239], v[124:127]\n"
"v_mfma_f32_16x16x32_bf16 v[128:131], v[60:63], a[240:243], v[128:131]\n"
"v_mfma_f32_16x16x32_bf16 v[132:135], v[60:63], a[244:247], v[132:135]\n"
"v_mfma_f32_16x16x32_bf16 v[136:139], v[60:63], a[248:251], v[136:139]\n"
"v_mfma_f32_16x16x32_bf16 v[140:143], v[60:63], a[252:255], v[140:143]\n"
"s_nop 1\n"
"v_cvt_pk_bf16_f32 v192, v112, v116\n"
"s_nop 0\n"
"v_cvt_pk_bf16_f32 v193, v120, v124\n"
"s_nop 0\n"
"v_cvt_pk_bf16_f32 v194, v128, v132\n"
"s_nop 0\n"
"v_cvt_pk_bf16_f32 v195, v136, v140\n"
"s_xor_b32 s92, s9, 0x6000\n"
"buffer_store_dwordx4 v[192:195], v242, s[48:51], s92 offen sc1\n"
"v_cvt_pk_bf16_f32 v196, v113, v117\n"
"v_cvt_pk_bf16_f32 v197, v121, v125\n"
"v_cvt_pk_bf16_f32 v198, v129, v133\n"
"v_cvt_pk_bf16_f32 v199, v137, v141\n"
"s_xor_b32 s93, s9, 0x7000\n"
"buffer_store_dwordx4 v[196:199], v242, s[48:51], s93 offen sc1\n"
"v_cvt_pk_bf16_f32 v200, v114, v118\n"
"v_cvt_pk_bf16_f32 v201, v122, v126\n"
"v_cvt_pk_bf16_f32 v202, v130, v134\n"
"v_cvt_pk_bf16_f32 v203, v138, v142\n"
"s_xor_b32 s92, s9, 0xf000\n"
"buffer_store_dwordx4 v[200:203], v242, s[48:51], s92 offen sc1\n"
"v_cvt_pk_bf16_f32 v204, v115, v119\n"
"v_cvt_pk_bf16_f32 v205, v123, v127\n"
"v_cvt_pk_bf16_f32 v206, v131, v135\n"
"v_cvt_pk_bf16_f32 v207, v139, v143\n"
"s_xor_b32 s93, s9, 0xe000\n"
"buffer_store_dwordx4 v[204:207], v242, s[48:51], s93 offen sc1\n"
"s_endpgm\n"
".Lhelper0:\n"
"s_cmp_lg_u32 %1, 0\n"
"s_cbranch_scc1 .Lhelper\n"
"s_load_dwordx2 s[8:9], %0, 0x40\n"
"v_and_b32 v1, 63, %2\n"
"v_lshlrev_b32 v1, 3, v1\n"
"v_mov_b32 v2, 0\n"
"v_readfirstlane_b32 s5, %2\n"
"s_movk_i32 s6, 0x3000\n"
"s_mov_b32 s10, 0x4000\n"
"s_mov_b32 s11, 0x20000\n"
"s_mov_b32 s7, 0\n"
"s_waitcnt lgkmcnt(0)\n"
"s_and_b32 s9, s9, 0xffff\n"
"s_cmp_ge_u32 s5, 64\n"
"s_cbranch_scc1 .Lld_sync\n"
"buffer_load_dwordx2 v[4:5], v2, s[8:11], 0 offen sc1\n"
"buffer_load_dword v6, v1, s[8:11], 0 offen offset:132 sc1\n"
"buffer_load_dword v7, v1, s[8:11], 0 offen offset:644 sc1\n"
"buffer_load_dword v8, v1, s[8:11], 0 offen offset:1156 sc1\n"
"buffer_load_dword v9, v1, s[8:11], 0 offen offset:1668 sc1\n"
"buffer_load_dword v10, v1, s[8:11], s6 offen offset:4 sc1\n"
"buffer_load_dword v11, v1, s[8:11], s6 offen offset:516 sc1\n"
"s_waitcnt vmcnt(0)\n"
"v_readfirstlane_b32 s4, v4\n"
"s_nop 3\n"
".Lld_pick:\n"
"s_add_u32 s4, s4, 1\n"
"v_cmp_eq_u32 s[12:13], s4, v5\n"
"v_cmp_eq_u32 vcc, s4, v6\n"
"s_nop 0\n"
"s_or_b64 s[12:13], s[12:13], vcc\n"
"v_cmp_eq_u32 vcc, s4, v7\n"
"s_nop 0\n"
"s_or_b64 s[12:13], s[12:13], vcc\n"
"v_cmp_eq_u32 vcc, s4, v8\n"
"s_nop 0\n"
"s_or_b64 s[12:13], s[12:13], vcc\n"
"v_cmp_eq_u32 vcc, s4, v9\n"
"s_nop 0\n"
"s_or_b64 s[12:13], s[12:13], vcc\n"
"v_cmp_eq_u32 vcc, s4, v10\n"
"s_nop 0\n"
"s_or_b64 s[12:13], s[12:13], vcc\n"
"v_cmp_eq_u32 vcc, s4, v11\n"
"s_nop 0\n"
"s_or_b64 s[12:13], s[12:13], vcc\n"
"s_cbranch_scc1 .Lld_pick\n"
"v_mov_b32 v3, s4\n"
"buffer_store_dword v3, v2, s[8:11], 0 offen sc1\n"
"ds_write_b32 v2, v3\n"
".Lld_sync:\n"
"s_waitcnt lgkmcnt(0)\n"
"s_barrier\n"
"ds_read_b32 v3, v2\n"
"s_cmp_ge_u32 s5, 64\n"
"s_waitcnt lgkmcnt(0)\n"
"s_cbranch_scc1 .Lld_hello\n"
"s_nop 0\n"
"v_min_u32 v12, 0x88, v1\n"
".Lld_poll:\n"
"buffer_load_dword v10, v1, s[8:11], s6 offen offset:4 sc1\n"
"buffer_load_dword v11, v12, s[8:11], s6 offen offset:516 sc1\n"
"s_add_u32 s7, s7, 1\n"
"s_cmp_gt_u32 s7, 0x10000\n"
"s_cbranch_scc1 .Lld_ready\n"
"s_waitcnt vmcnt(0)\n"
"v_cmp_ne_u32 s[12:13], v10, v3\n"
"v_cmp_ne_u32 vcc, v11, v3\n"
"s_nop 0\n"
"s_or_b64 s[12:13], s[12:13], vcc\n"
"s_cbranch_scc1 .Lld_poll\n"
".Lld_ready:\n"
"buffer_store_dword v3, v2, s[8:11], 0 offen offset:4 sc1\n"
"s_endpgm\n"
".Lld_hello:\n"
"v_subrev_u32 v4, 64, %2\n"
"v_add_u32 v5, 0xc0, v4\n"
"v_min_u32 v5, 0xfb, v5\n"
"v_lshlrev_b32 v6, 3, v4\n"
"v_lshlrev_b32 v7, 3, v5\n"
"v_lshlrev_b32 v8, 2, v4\n"
"v_lshlrev_b32 v9, 2, v5\n"
"s_movk_i32 s6, 0x2000\n"
"v_mov_b32 v16, 0x7ffffff0\n"
".Lld_hl:\n"
"buffer_load_dwordx2 v[10:11], v6, s[8:11], 0 offen offset:128 sc1\n"
"buffer_load_dwordx2 v[12:13], v7, s[8:11], 0 offen offset:128 sc1\n"
"s_add_u32 s7, s7, 1\n"
"s_cmp_gt_u32 s7, 0x10000\n"
"s_cbranch_scc1 .Lld_end\n"
"s_waitcnt vmcnt(0)\n"
"v_cmp_eq_u32 vcc, v11, v3\n"
"s_nop 1\n"
"v_cndmask_b32 v14, v16, v8, vcc\n"
"v_cndmask_b32 v8, v8, v16, vcc\n"
"v_cmp_eq_u32 vcc, v13, v3\n"
"s_nop 1\n"
"v_cndmask_b32 v15, v16, v9, vcc\n"
"v_cndmask_b32 v9, v9, v16, vcc\n"
"buffer_store_dword v10, v14, s[8:11], s6 offen sc1\n"
"buffer_store_dword v12, v15, s[8:11], s6 offen sc1\n"
"v_min_u32 v17, v8, v9\n"
"v_cmp_ne_u32 vcc, v17, v16\n"
"s_nop 1\n"
"s_cmp_lg_u64 vcc, 0\n"
"s_cbranch_scc1 .Lld_hl\n"
".Lld_end:\n"
"s_endpgm\n"
".Lhelper:\n"
    : "+s"(kp), "+s"(bid), "+v"(tid)
    :: "v1", "v2", "v3", "v4", "v5", "v6", "v7", "v8", "v9", "v10", "v11", "v12", "v13", "v14", "v15", "v16", "v17", "v18", "v19", "v20", "v21", "v22", "v23", "v24", "v25", "v26", "v27", "v28", "v29", "v30", "v31", "v32", "v33", "v34", "v35", "v36", "v37", "v38", "v39", "v40", "v41", "v42", "v43", "v44", "v45", "v46", "v47", "v48", "v49", "v50", "v51", "v52", "v53", "v54", "v55", "v56", "v57", "v58", "v59", "v60", "v61", "v62", "v63", "v64", "v65", "v66", "v67", "v68", "v69", "v70", "v71", "v72", "v73", "v74", "v75", "v76", "v77", "v78", "v79", "v80", "v81", "v82", "v83", "v84", "v85", "v86", "v87", "v88", "v89", "v90", "v91", "v92", "v93", "v94", "v95", "v96", "v97", "v98", "v99", "v100", "v101", "v102", "v103", "v104", "v105", "v106", "v107", "v108", "v109", "v110", "v111", "v112", "v113", "v114", "v115", "v116", "v117", "v118", "v119", "v120", "v121", "v122", "v123", "v124", "v125", "v126", "v127", "v128", "v129", "v130", "v131", "v132", "v133", "v134", "v135", "v136", "v137", "v138", "v139", "v140", "v141", "v142", "v143", "v144", "v145", "v146", "v147", "v148", "v149", "v150", "v151", "v152", "v153", "v154", "v155", "v156", "v157", "v158", "v159", "v160", "v161", "v162", "v163", "v164", "v165", "v166", "v167", "v168", "v169", "v170", "v171", "v172", "v173", "v174", "v175", "v176", "v177", "v178", "v179", "v180", "v181", "v182", "v183", "v184", "v185", "v186", "v187", "v188", "v189", "v190", "v191", "v192", "v193", "v194", "v195", "v196", "v197", "v198", "v199", "v200", "v201", "v202", "v203", "v204", "v205", "v206", "v207", "v208", "v209", "v210", "v211", "v212", "v213", "v214", "v215", "v216", "v217", "v218", "v219", "v220", "v221", "v222", "v223", "v224", "v225", "v226", "v227", "v228", "v229", "v230", "v231", "v232", "v233", "v234", "v235", "v236", "v237", "v238", "v239", "v240", "v241", "v242", "v243", "v244", "v245", "v246", "v247", "v248", "v249", "v250", "v251", "v252", "v253", "v254", "v255", "a0", "a1", "a2", "a3", "a4", "a5", "a6", "a7", "a8", "a9", "a10", "a11", "a12", "a13", "a14", "a15", "a16", "a17", "a18", "a19", "a20", "a21", "a22", "a23", "a24", "a25", "a26", "a27", "a28", "a29", "a30", "a31", "a32", "a33", "a34", "a35", "a36", "a37", "a38", "a39", "a40", "a41", "a42", "a43", "a44", "a45", "a46", "a47", "a48", "a49", "a50", "a51", "a52", "a53", "a54", "a55", "a56", "a57", "a58", "a59", "a60", "a61", "a62", "a63", "a64", "a65", "a66", "a67", "a68", "a69", "a70", "a71", "a72", "a73", "a74", "a75", "a76", "a77", "a78", "a79", "a80", "a81", "a82", "a83", "a84", "a85", "a86", "a87", "a88", "a89", "a90", "a91", "a92", "a93", "a94", "a95", "a96", "a97", "a98", "a99", "a100", "a101", "a102", "a103", "a104", "a105", "a106", "a107", "a108", "a109", "a110", "a111", "a112", "a113", "a114", "a115", "a116", "a117", "a118", "a119", "a120", "a121", "a122", "a123", "a124", "a125", "a126", "a127", "a128", "a129", "a130", "a131", "a132", "a133", "a134", "a135", "a136", "a137", "a138", "a139", "a140", "a141", "a142", "a143", "a144", "a145", "a146", "a147", "a148", "a149", "a150", "a151", "a152", "a153", "a154", "a155", "a156", "a157", "a158", "a159", "a160", "a161", "a162", "a163", "a164", "a165", "a166", "a167", "a168", "a169", "a170", "a171", "a172", "a173", "a174", "a175", "a176", "a177", "a178", "a179", "a180", "a181", "a182", "a183", "a184", "a185", "a186", "a187", "a188", "a189", "a190", "a191", "a192", "a193", "a194", "a195", "a196", "a197", "a198", "a199", "a200", "a201", "a202", "a203", "a204", "a205", "a206", "a207", "a208", "a209", "a210", "a211", "a212", "a213", "a214", "a215", "a216", "a217", "a218", "a219", "a220", "a221", "a222", "a223", "a224", "a225", "a226", "a227", "a228", "a229", "a230", "a231", "a232", "a233", "a234", "a235", "a236", "a237", "a238", "a239", "a240", "a241", "a242", "a243", "a244", "a245", "a246", "a247", "a248", "a249", "a250", "a251", "a252", "a253", "a254", "a255", "s4", "s5", "s6", "s7", "s8", "s9", "s10", "s11", "s12", "s13", "s14", "s15", "s16", "s17", "s18", "s19", "s20", "s21", "s22", "s23", "s24", "s25", "s26", "s27", "s28", "s29", "s30", "s31", "s33", "s34", "s35", "s36", "s37", "s38", "s39", "s40", "s41", "s42", "s43", "s44", "s45", "s46", "s47", "s48", "s49", "s50", "s51", "s52", "s53", "s54", "s55", "s56", "s57", "s58", "s59", "s60", "s61", "s62", "s63", "s64", "s65", "s66", "s67", "s68", "s69", "s70", "s71", "s72", "s73", "s74", "s75", "s76", "s77", "s78", "s79", "s80", "s81", "s82", "s83", "s84", "s85", "s86", "s87", "s88", "s89", "s90", "s91", "s92", "s93", "s94", "s95", "s96", "s97", "s98", "s99", "vcc", "scc", "memory");
    __builtin_amdgcn_sched_barrier(0);
    producer(bid, tid, kp[3], kp[5], kp[8], nullptr);
}
extern "C" void kernel_launch(void* const* d_in, const int* in_sizes, int n_in,
                              void* d_out, int out_size, void* d_ws, size_t ws_size,
                              hipStream_t stream) {
    (void)in_sizes; (void)n_in; (void)out_size; (void)ws_size;
    (void)hipFuncSetAttribute((const void*)rnnt_fused, hipFuncAttributeMaxDynamicSharedMemorySize, LDS_BYTES);
    rnnt_fused<<<253, 256, LDS_BYTES, stream>>>(d_in[0], d_in[1], d_in[2], d_in[3], d_in[4], d_in[5], d_in[6], d_out, d_ws);
}
```
